```python
import torch

def _softmax(x, axis=-1):
    return torch.softmax(x, dim=axis)

def _gelu(x):
    return torch.nn.functional.gelu(x, approximate='tanh')

def _silu(x):
    return torch.nn.functional.silu(x)

def _sigmoid(x):
    return torch.sigmoid(x)

def _layernorm(x, axis=-1, eps=1e-05):
    m = x.mean(dim=axis, keepdim=True)
    v = x.var(dim=axis, keepdim=True, unbiased=False)
    return (x - m) / torch.sqrt(v + eps)

def _scatter_add(tgt, idx, src, axis=0):
    return tgt.clone().index_add_(axis, idx, src)

def _segment_sum(src, seg, n):
    out = torch.zeros((n,) + src.shape[1:], dtype=src.dtype)
    return out.index_add_(0, seg, src)

def _pad(x, pw):
    flat = tuple((v for pair in reversed(pw) for v in pair))
    return torch.nn.functional.pad(x, flat)
kernel_name = 'adaLN_cond__sliding_block_cached__dense_activation_silu__decode'

def setup_inputs():

    _w = lambda a: (lambda t: t.to(torch.bfloat16) if t.is_floating_point() else t)(torch.as_tensor(a))

    def _A_setup():
        B, Sq, D = (8, 1, 640)
        Hq, Hkv, Sk, Dh = (32, 32, 4096, 128)
        assert Hq % Hkv == 0, (Hq, Hkv)
        return {'x': _w((lambda *s: torch.randn(*s, dtype=torch.bfloat16) * 0.1)(B, Sq, D)), 'k_cache': _w((lambda *s: torch.randn(*s, dtype=torch.bfloat16) * 0.8)(B, Hkv, Sk, Dh)), 'v_cache': _w((lambda *s: torch.randn(*s, dtype=torch.bfloat16) * 3.2)(B, Hkv, Sk, Dh)), 'wq': _w((lambda *s: torch.randn(*s, dtype=torch.bfloat16) * 0.8)(D, Hq * Dh) * D ** (-0.5)), 'wo': _w((lambda *s: torch.randn(*s, dtype=torch.bfloat16) * 3.2)(Hq * Dh, D) * (Hq * Dh) ** (-0.5))}
    _A_in = _A_setup()

    def _B_setup():
        B, S, D, F = (8, 1, 640, 1728)
        return {'x': _w((lambda *s: torch.randn(*s, dtype=torch.bfloat16) * 0.1)(B, S, D)), 'w1': _w((lambda *s: torch.randn(*s, dtype=torch.bfloat16) * 3.2)(D, F) * D ** (-0.5)), 'w2': _w((lambda *s: torch.randn(*s, dtype=torch.bfloat16) * 3.2)(F, D) * F ** (-0.5))}
    _B_in = _B_setup()
    return {'A_x': _A_in['x'], 'A_k_cache': _A_in['k_cache'], 'A_v_cache': _A_in['v_cache'], 'A_wq': _A_in['wq'], 'A_wo': _A_in['wo'], 'B_x': _B_in['x'], 'B_w1': _B_in['w1'], 'B_w2': _B_in['w2'], 'c': _w((lambda *s: torch.randn(*s, dtype=torch.bfloat16) * 0.1)(8, 640) * 8.0), 'w_mod': _w((lambda *s: torch.randn(*s, dtype=torch.bfloat16) * 0.1)(640, 6 * 640) * 640 ** (-0.5) * 8.0)}

def reference(A_x, A_k_cache, A_v_cache, A_wq, A_wo, B_x, B_w1, B_w2, c, w_mod):

    def _A_ref(x, k_cache, v_cache, wq, wo):
        B, Sq, D = x.shape
        Hq, Dh = (32, 128)
        Hkv, Sk = (k_cache.shape[1], k_cache.shape[2])
        W = min(4096, max(256, Sk // 4))
        rep = Hq // Hkv
        q = (lambda x, *d: x.permute(*d))((x @ wq).reshape(B, Sq, Hq, Dh), 0, 2, 1, 3)
        k = (lambda x, n, axis: torch.repeat_interleave(x, n, dim=axis))(k_cache[:, :, Sk - W:, :], rep, axis=1)
        v = (lambda x, n, axis: torch.repeat_interleave(x, n, dim=axis))(v_cache[:, :, Sk - W:, :], rep, axis=1)
        scores = torch.einsum('bhqd,bhkd->bhqk', q, k) * Dh ** (-0.5)
        p = _softmax(scores, axis=-1)
        o = torch.einsum('bhqk,bhkd->bhqd', p, v)
        return (lambda x, *d: x.permute(*d))(o, 0, 2, 1, 3).reshape(B, Sq, Hq * Dh) @ wo

    def _B_ref(x, w1, w2):
        h = torch.einsum('bsd,df->bsf', x, w1)
        h = _silu(h)
        out = torch.einsum('bsf,fd->bsd', h, w2)
        return out
    x = A_x
    mod = (_silu(c) @ w_mod).reshape(c.shape[0], 6, 1, -1)
    sa, ca, ga, sb, cb, gb = (mod[:, 0].clone(), mod[:, 1].clone(), mod[:, 2].clone(), mod[:, 3].clone(), mod[:, 4].clone(), mod[:, 5].clone())
    h = _layernorm(x, axis=-1) * (1 + ca) + sa
    x = x + ga * _A_ref(h, A_k_cache, A_v_cache, A_wq, A_wo)
    h = _layernorm(x, axis=-1) * (1 + cb) + sb
    x = x + gb * _B_ref(h, B_w1, B_w2)
    return x
```

```cpp
#include <hip/hip_runtime.h>
#include <hip/hip_bf16.h>
#include <stdint.h>

typedef unsigned short u16;
constexpr int B = 8, D = 640, H = 32, DH = 128, SK = 4096, W = 1024, F = 1728;

__device__ __forceinline__ float bf2f(u16 v) { return __uint_as_float(((unsigned)v) << 16); }
__device__ __forceinline__ u16 f2bf(float f) {
  unsigned u = __float_as_uint(f);
  u += 0x7FFFu + ((u >> 16) & 1u);
  return (u16)(u >> 16);
}
__device__ __forceinline__ float rbf(float f) { return bf2f(f2bf(f)); }

__global__ void k_mod(const u16* c, const u16* wmod, float* mod) {
  __shared__ float sc[B][D];
  for (int i = threadIdx.x; i < B * D; i += blockDim.x) {
    float v = bf2f(c[i]);
    sc[i / D][i % D] = rbf(v / (1.f + __expf(-v)));
  }
  __syncthreads();
  int n = blockIdx.x * 64 + (threadIdx.x & 63);
  int kq = threadIdx.x >> 6;
  float acc[B];
  for (int b = 0; b < B; ++b) acc[b] = 0.f;
  for (int k = kq; k < D; k += 4) {
    float w = bf2f(wmod[(size_t)k * (6 * D) + n]);
    for (int b = 0; b < B; ++b) acc[b] += sc[b][k] * w;
  }
  __shared__ float red[4][B][64];
  for (int b = 0; b < B; ++b) red[kq][b][threadIdx.x & 63] = acc[b];
  __syncthreads();
  if (kq == 0) {
    for (int b = 0; b < B; ++b) {
      int l = threadIdx.x & 63;
      mod[b * 6 * D + n] = rbf(red[0][b][l] + red[1][b][l] + red[2][b][l] + red[3][b][l]);
    }
  }
}

__device__ void ln_mod(const float* xin  , const float* shift, const float* scale, float* h,
                       float* red) {
  int t = threadIdx.x;
  float s = 0.f;
  for (int i = t; i < D; i += blockDim.x) s += xin[i];
  for (int o = 32; o; o >>= 1) s += __shfl_xor(s, o);
  if ((t & 63) == 0) red[t >> 6] = s;
  __syncthreads();
  float tot = 0.f;
  for (int i = 0; i < (int)(blockDim.x >> 6); ++i) tot += red[i];
  float mean = tot / D;
  __syncthreads();
  float s2 = 0.f;
  for (int i = t; i < D; i += blockDim.x) { float d = xin[i] - mean; s2 += d * d; }
  for (int o = 32; o; o >>= 1) s2 += __shfl_xor(s2, o);
  if ((t & 63) == 0) red[t >> 6] = s2;
  __syncthreads();
  float tot2 = 0.f;
  for (int i = 0; i < (int)(blockDim.x >> 6); ++i) tot2 += red[i];
  float var = rbf(tot2 / D);
  float m = rbf(mean);
  float den = rbf(sqrtf(rbf(var + 1e-5f)));
  for (int i = t; i < D; i += blockDim.x) {
    float v = rbf(rbf(xin[i] - m) / den);
    h[i] = rbf(rbf(v * rbf(1.f + scale[i])) + shift[i]);
  }
  __syncthreads();
}

__global__ void k_q(const u16* x, const float* mod, const u16* wq, float* q) {
  __shared__ float xs[D], hs[D], red[8], part[2][DH];
  int h = blockIdx.x, b = blockIdx.y, t = threadIdx.x;
  for (int i = t; i < D; i += blockDim.x) xs[i] = bf2f(x[b * D + i]);
  __syncthreads();
  ln_mod(xs, mod + b * 6 * D + 0 * D, mod + b * 6 * D + 1 * D, hs, red);
  int n = t & 127, kh = t >> 7;
  float acc = 0.f;
  for (int k = kh; k < D; k += 2) acc += hs[k] * bf2f(wq[(size_t)k * (H * DH) + h * DH + n]);
  part[kh][n] = acc;
  __syncthreads();
  if (t < DH) q[(b * H + h) * DH + t] = rbf(part[0][t] + part[1][t]);
}

__global__ void k_attn(const float* q, const u16* kc, const u16* vc, float* o) {
  __shared__ float qs[DH], sc[W], red[8], part[2][DH];
  int h = blockIdx.x, b = blockIdx.y, t = threadIdx.x;
  if (t < DH) qs[t] = q[(b * H + h) * DH + t];
  __syncthreads();
  const u16* K = kc + ((size_t)(b * H + h) * SK + (SK - W)) * DH;
  const u16* V = vc + ((size_t)(b * H + h) * SK + (SK - W)) * DH;
  for (int k = t; k < W; k += 256) {
    float s = 0.f;
    for (int d = 0; d < DH; ++d) s += qs[d] * bf2f(K[(size_t)k * DH + d]);
    sc[k] = rbf(rbf(s) * 0.08838834764831845f);
  }
  __syncthreads();
  float m = -INFINITY;
  for (int k = t; k < W; k += 256) m = fmaxf(m, sc[k]);
  for (int o2 = 32; o2; o2 >>= 1) m = fmaxf(m, __shfl_xor(m, o2));
  if ((t & 63) == 0) red[t >> 6] = m;
  __syncthreads();
  m = fmaxf(fmaxf(red[0], red[1]), fmaxf(red[2], red[3]));
  __syncthreads();
  float s = 0.f;
  for (int k = t; k < W; k += 256) { float e = __expf(sc[k] - m); sc[k] = e; s += e; }
  for (int o2 = 32; o2; o2 >>= 1) s += __shfl_xor(s, o2);
  if ((t & 63) == 0) red[t >> 6] = s;
  __syncthreads();
  float l = red[0] + red[1] + red[2] + red[3];
  int d = t & 127, kh = t >> 7;
  float acc = 0.f;
  for (int k = kh; k < W; k += 2) acc += rbf(sc[k] / l) * bf2f(V[(size_t)k * DH + d]);
  part[kh][d] = acc;
  __syncthreads();
  if (t < DH) o[(b * H + h) * DH + t] = rbf(part[0][t] + part[1][t]);
}

__global__ void k_wo(const float* o, const u16* wo, const u16* x, const float* mod, float* x1) {
  __shared__ float os[H * DH], red[4][64];
  int b = blockIdx.y, t = threadIdx.x, n = blockIdx.x * 64 + (t & 63), kq = t >> 6;
  for (int i = t; i < H * DH; i += 256) os[i] = o[b * H * DH + i];
  __syncthreads();
  float acc = 0.f;
  for (int k = kq; k < H * DH; k += 4) acc += os[k] * bf2f(wo[(size_t)k * D + n]);
  red[kq][t & 63] = acc;
  __syncthreads();
  if (kq == 0) {
    float a = rbf(red[0][t] + red[1][t] + red[2][t] + red[3][t]);
    x1[b * D + n] = rbf(bf2f(x[b * D + n]) + rbf(mod[b * 6 * D + 2 * D + n] * a));
  }
}

__global__ void k_w1(const float* x1, const float* mod, const u16* w1, float* h1) {
  __shared__ float xs[D], hs[D], red8[8], red[4][64];
  int b = blockIdx.y, t = threadIdx.x, n = blockIdx.x * 64 + (t & 63), kq = t >> 6;
  for (int i = t; i < D; i += 256) xs[i] = x1[b * D + i];
  __syncthreads();
  ln_mod(xs, mod + b * 6 * D + 3 * D, mod + b * 6 * D + 4 * D, hs, red8);
  float acc = 0.f;
  for (int k = kq; k < D; k += 4) acc += hs[k] * bf2f(w1[(size_t)k * F + n]);
  red[kq][t & 63] = acc;
  __syncthreads();
  if (kq == 0) {
    float a = rbf(red[0][t] + red[1][t] + red[2][t] + red[3][t]);
    h1[b * F + n] = rbf(a / (1.f + __expf(-a)));
  }
}

__global__ void k_w2(const float* h1, const u16* w2, const float* x1, const float* mod, u16* out) {
  __shared__ float hs[F], red[4][64];
  int b = blockIdx.y, t = threadIdx.x, n = blockIdx.x * 64 + (t & 63), kq = t >> 6;
  for (int i = t; i < F; i += 256) hs[i] = h1[b * F + i];
  __syncthreads();
  float acc = 0.f;
  for (int k = kq; k < F; k += 4) acc += hs[k] * bf2f(w2[(size_t)k * D + n]);
  red[kq][t & 63] = acc;
  __syncthreads();
  if (kq == 0) {
    float a = rbf(red[0][t] + red[1][t] + red[2][t] + red[3][t]);
    out[b * D + n] = f2bf(x1[b * D + n] + rbf(mod[b * 6 * D + 5 * D + n] * a));
  }
}

extern "C" void kernel_launch(void* const* d_in, const int* in_sizes, int n_in, void* d_out, int out_size, void* d_ws,
                              size_t ws_size, hipStream_t stream) {
  const u16* x = (const u16*)d_in[0];
  const u16* kc = (const u16*)d_in[1];
  const u16* vc = (const u16*)d_in[2];
  const u16* wq = (const u16*)d_in[3];
  const u16* wo = (const u16*)d_in[4];
  const u16* w1 = (const u16*)d_in[6];
  const u16* w2 = (const u16*)d_in[7];
  const u16* c = (const u16*)d_in[8];
  const u16* wmod = (const u16*)d_in[9];
  float* ws = (float*)d_ws;
  float* mod = ws;
  float* q = mod + B * 6 * D;
  float* o = q + B * H * DH;
  float* x1 = o + B * H * DH;
  float* h1 = x1 + B * D;
  k_mod<<<6 * D / 64, 256, 0, stream>>>(c, wmod, mod);
  k_q<<<dim3(H, B), 256, 0, stream>>>(x, mod, wq, q);
  k_attn<<<dim3(H, B), 256, 0, stream>>>(q, kc, vc, o);
  k_wo<<<dim3(D / 64, B), 256, 0, stream>>>(o, wo, x, mod, x1);
  k_w1<<<dim3(F / 64, B), 256, 0, stream>>>(x1, mod, w1, h1);
  k_w2<<<dim3(D / 64, B), 256, 0, stream>>>(h1, w2, x1, mod, (u16*)d_out);
}
```

```cpp
#include <hip/hip_runtime.h>
#include <stdint.h>

#define DIAG 0

namespace {

typedef unsigned short u16;
typedef unsigned v4u __attribute__((ext_vector_type(4)));
typedef unsigned v2u __attribute__((ext_vector_type(2)));
typedef float f4 __attribute__((ext_vector_type(4)));
typedef short s4 __attribute__((ext_vector_type(4)));
typedef short s8 __attribute__((ext_vector_type(8)));
typedef __bf16 bf2 __attribute__((ext_vector_type(2)));
typedef __bf16 bf8 __attribute__((ext_vector_type(8)));
typedef __amdgpu_buffer_rsrc_t rsrc_t;
#define LAS __attribute__((address_space(3)))
#define GAS __attribute__((address_space(1)))
#define DI __device__ __forceinline__

constexpr int NB = 8, DM = 640, NH = 32, DH = 128, SK = 4096, WIN = 1024, FF = 1728;
constexpr int NSW = 7;
constexpr int NL = 37;
#define TD_A 8
#define TD_B 8
#define TD_C 16
#define TD_D 8
#define TD_D2 4
#define TS 6
#define ABL_CTRL 0
#define ABL_SLOT 0
#define ABL_ROLE 0
#define ABL_SENT 0
#define LEAD 3
#define MP0 12
#define MPACE 11
#define DELTA 0
#define HEAVY 1
#define ABL_G1 0
#define ABL_G2 0
#define ABL_G3 0
#define ABL_TAIL 0
#define GB 40
#define GC 40
#define GD1 40
#define GD2 190
#define WARM 1
#define INDIC 1
#define ABL_WO 0
#define ABL_W12 0
#define D1 600
#define D2 100
#define PSLEEP 32
#define DPACE 15
#define PACE_ 3
constexpr int PACE = PACE_;
#define NPRE_ 16
constexpr int NPRE = NPRE_;

constexpr int NMLP = 54;
constexpr unsigned WS_EPOCH = 0;
constexpr unsigned WS_INIT = 4096;
constexpr unsigned WS_MODG = 8192;
constexpr unsigned WS_QP = WS_MODG + NB * 1920 * 8;
constexpr unsigned WS_OX = WS_QP + NH * 8 * 8 * 128 * 4;
constexpr unsigned WS_T1P = WS_OX + NH * 8 * 256;
constexpr unsigned WS_X1B = WS_T1P + NB * NH * DM * 4;
constexpr unsigned WS_T5P = WS_X1B + NB * 320 * 4;
constexpr unsigned WS_END = WS_T5P + NMLP * NB * DM * 4;
constexpr unsigned WS_DIAG = 4u << 20;
constexpr unsigned SENT = 0xFFFFFFFFu;
static_assert(WS_END <= WS_DIAG, "workspace map");

constexpr unsigned L_RT = 0;
constexpr unsigned L_QT = 40960;
constexpr unsigned L_W2 = 61440;
constexpr unsigned L_T1 = 102400;
constexpr unsigned L_CS = 122880;
constexpr unsigned L_MISC = 143360;
constexpr unsigned L_FLAG = L_MISC;
constexpr unsigned L_Q = L_MISC + 64;
constexpr unsigned L_STAT = L_Q + 256;
constexpr unsigned L_VEC = L_STAT + 64;
constexpr unsigned L_X1O = L_VEC + 128;
constexpr unsigned L_SC = L_X1O + 128;
constexpr unsigned L_H2 = L_SC;
constexpr unsigned L_HS = L_SC + 8 * 1296;
constexpr unsigned L_MRG = L_HS + 8 * 176;
constexpr unsigned L_OS = L_MRG + 7 * 132 * 4;
constexpr unsigned L_T4P = L_MRG;
constexpr unsigned L_H1 = L_T4P + 4096;
constexpr unsigned L_END = 163840;
static_assert(L_OS + 8 * 272 <= L_END && L_H1 + 8 * 80 <= L_OS + 8 * 272, "LDS map");
static_assert((L_SC % 16) == 0 && (L_HS % 16) == 0 && (L_MRG % 16) == 0 && (L_OS % 16) == 0 && (L_H1 % 16) == 0, "LDS alignment");

constexpr unsigned SPIN_G = 1u << 16;
constexpr unsigned SPIN_L = 1u << 22;

struct Params {
  const u16 *x, *kc, *vc, *wq, *wo, *w1, *w2, *c, *wmod;
  u16* out;
  unsigned char* ws;
};

DI unsigned rfl(unsigned x) { return __builtin_amdgcn_readfirstlane(x); }
DI float asf(unsigned u) { return __uint_as_float(u); }
DI unsigned asu(float f) { return __float_as_uint(f); }
DI unsigned pk2(float lo, float hi) { bf2 v = {(__bf16)lo, (__bf16)hi}; return __builtin_bit_cast(unsigned, v); }
DI float rbf(float f) { return asf(pk2(f, 0.f) << 16); }
DI float blo(unsigned u) { return asf(u << 16); }
DI float bhi(unsigned u) { return asf(u & 0xffff0000u); }
DI float dot2(unsigned a, unsigned b, float c) {
  return __builtin_amdgcn_fdot2_f32_bf16(__builtin_bit_cast(bf2, a), __builtin_bit_cast(bf2, b), c, false);
}
template <int CTRL> DI float dpp(float x) {
  return __builtin_bit_cast(float, __builtin_amdgcn_mov_dpp(__builtin_bit_cast(int, x), CTRL, 0xf, 0xf, true));
}
DI float row16_sum(float x) { x += dpp<0x128>(x); x += dpp<0x124>(x); x += dpp<0x122>(x); x += dpp<0x121>(x); return x; }
DI float oct_sum(float x) { x += dpp<0xB1>(x); x += dpp<0x4E>(x); x += dpp<0x141>(x); return x; }
DI float x16_sum(float x) {
  auto s = __builtin_amdgcn_permlane16_swap(asu(x), asu(x), false, false);
  return asf(s[0]) + asf(s[1]);
}
DI float x32_sum(float x) {
  auto t = __builtin_amdgcn_permlane32_swap(asu(x), asu(x), false, false);
  return asf(t[0]) + asf(t[1]);
}
DI float xrow_sum(float x) { return x32_sum(x16_sum(x)); }
DI float xrow_max(float x) {
  auto s = __builtin_amdgcn_permlane16_swap(asu(x), asu(x), false, false);
  x = fmaxf(asf(s[0]), asf(s[1]));
  auto t = __builtin_amdgcn_permlane32_swap(asu(x), asu(x), false, false);
  return fmaxf(asf(t[0]), asf(t[1]));
}
DI float wave_sum(float x) { return xrow_sum(row16_sum(x)); }
DI f4 mfma32(v4u a, v4u b, f4 c) {
  return __builtin_amdgcn_mfma_f32_16x16x32_bf16(__builtin_bit_cast(bf8, a), __builtin_bit_cast(bf8, b), c, 0, 0, 0);
}
DI f4 mfma16(v2u a, v2u b, f4 c) {
  return __builtin_amdgcn_mfma_f32_16x16x16bf16_1k(__builtin_bit_cast(s4, a), __builtin_bit_cast(s4, b), c, 0, 0, 0);
}
DI v2u trread(const LAS unsigned char* p) {
  return __builtin_bit_cast(v2u, __builtin_amdgcn_ds_read_tr16_b64_v4i16((LAS s4*)p));
}
DI rsrc_t mkrs(const void* p, unsigned bytes) { return __builtin_amdgcn_make_buffer_rsrc((void*)p, 0, (int)bytes, 0x00020000); }
DI v4u ld_nt(rsrc_t r, unsigned vo, unsigned so) { return __builtin_amdgcn_raw_buffer_load_b128(r, vo, so, 2); }
DI v4u ld_sc(rsrc_t r, unsigned vo) { return __builtin_amdgcn_raw_buffer_load_b128(r, vo, 0, 16); }
DI v2u ld_sc2(rsrc_t r, unsigned vo) { return __builtin_amdgcn_raw_buffer_load_b64(r, vo, 0, 16); }
DI void st_sc(v4u v, rsrc_t r, unsigned vo) { __builtin_amdgcn_raw_buffer_store_b128(v, r, vo, 0, 16); }
DI void st_sc2(v2u v, rsrc_t r, unsigned vo) { __builtin_amdgcn_raw_buffer_store_b64(v, r, vo, 0, 16); }
DI unsigned canonu(unsigned u) { return u == 0xFFFFFFFFu ? 0x7FFF7FFFu : u; }
DI void dma16(rsrc_t r, unsigned lds, unsigned vo, unsigned so) {
  unsigned keep;
  asm volatile("s_nop 4\n\ts_mov_b32 %0, m0\n\ts_mov_b32 m0, %1\n\ts_nop 0\n\tbuffer_load_dwordx4 %2, %3, %4 offen nt lds\n\ts_mov_b32 m0, %0"
               : "=&s"(keep) : "s"(lds), "v"(vo), "s"(r), "s"(so) : "memory");
}
template <int N> DI void vmwait() { asm volatile("s_waitcnt vmcnt(%0)" :: "n"(N) : "memory"); }
#define VMWAIT(n) vmwait<(n)>()
DI void bar() { asm volatile("s_waitcnt lgkmcnt(0)\n\ts_barrier" ::: "memory"); }
#define CFENCE() asm volatile("" ::: "memory")
#define SCHED() __builtin_amdgcn_sched_barrier(0)
DI unsigned canon(float f) { unsigned u = asu(f); return u == SENT ? 0x7FFFFFFFu : u; }
DI unsigned tg(float f, unsigned t10) { return ((asu(f) + 0x200u) & ~0x3FFu) | t10; }
DI v4u tg4(f4 v, unsigned t10) { return v4u{tg(v[0], t10), tg(v[1], t10), tg(v[2], t10), tg(v[3], t10)}; }
DI bool tagged4(v4u v, unsigned t10) { return (((v.x ^ t10) | (v.y ^ t10) | (v.z ^ t10) | (v.w ^ t10)) & 0x3FFu) == 0u; }
DI float untg(unsigned u) { return asf(u & ~0x3FFu); }
DI bool valid4(v4u v) { return v.x != SENT && v.y != SENT && v.z != SENT && v.w != SENT; }
DI float rcp(float x) { return __builtin_amdgcn_rcpf(x); }
DI float silu(float v) { return v * rcp(1.f + __builtin_amdgcn_exp2f(v * -1.4426950408889634f)); }

#define STAMP(k) do { } while (0)
#define STAMP2(k) do { } while (0)

__global__ void __launch_bounds__(512, 2) fused_block(Params p) {
  __shared__ __attribute__((aligned(16))) unsigned char smem[L_END];
  LAS unsigned char* L = (LAS unsigned char*)smem;
  const unsigned lds0 = (unsigned)(uintptr_t)L;
  const int tid = threadIdx.x, lane = tid & 63;
  const int w = (int)rfl(tid >> 6);
  const int id = blockIdx.x, xcd = id & 7, sl = id >> 3;
  const int h = xcd * 4 + (sl >> 3), b = sl & 7;
  const int g = lane >> 4, i16 = lane & 15;
  const int tq = i16 >> 2, tp = lane & 3;
  const rsrc_t rsws = mkrs(p.ws, WS_END);
  volatile LAS unsigned* flags = (volatile LAS unsigned*)(L + L_FLAG);
  const int mpr = (sl >> 1) * 8 + xcd;
  const bool has_mod = !ABL_ROLE && sl < 16 && mpr < 60;
  const bool crit = has_mod && mpr < 20;
  const int mcol0 = (2 * mpr + (sl & 1)) * 32;
  const int fpr = ((sl - 16) >> 1) * 8 + xcd;
  const bool is_mlp = !ABL_ROLE && sl >= 16 && sl < 24 && fpr < 27;
  const int mm = 2 * fpr + (sl & 1);

  STAMP(0);
  const unsigned tstart = (unsigned)__builtin_amdgcn_s_memrealtime();
  auto sched = [&](int i, bool light) {
    if (DELTA && light) { while ((unsigned)__builtin_amdgcn_s_memrealtime() - tstart < (unsigned)(i * DELTA / 10)) __builtin_amdgcn_s_sleep(1); }
  };

  if (w < NSW) {
    const rsrc_t rsk = mkrs(p.kc + ((size_t)(b * NH + h) * SK + (SK - WIN)) * DH, WIN * DH * 2);
    const rsrc_t rsv = mkrs(p.vc + ((size_t)(b * NH + h) * SK + (SK - WIN)) * DH, WIN * DH * 2);
    const unsigned vo = (unsigned)(lane * 16 + w * 1024);
    v4u kr[NL], vr[NL], cr[2];
    if (has_mod && w < 5) {
      const GAS v4u* cp = (const GAS v4u*)p.c;
      cr[0] = cp[w * 128 + lane];
      cr[1] = cp[w * 128 + 64 + lane];
    }
    auto silu_c = [&]() {
      if (w < 5) {
#pragma unroll
        for (int u = 0; u < 2; ++u) {
          const int q = w * 128 + u * 64 + lane, j = q / 80, k8 = q - 80 * j;
          v4u o;
          o.x = pk2(silu(blo(cr[u].x)), silu(bhi(cr[u].x)));
          o.y = pk2(silu(blo(cr[u].y)), silu(bhi(cr[u].y)));
          o.z = pk2(silu(blo(cr[u].z)), silu(bhi(cr[u].z)));
          o.w = pk2(silu(blo(cr[u].w)), silu(bhi(cr[u].w)));
          *(LAS v4u*)(L + L_SC + j * 1296 + k8 * 16) = o;
        }
      }
    };
    const rsrc_t rsrt = mkrs(p.wmod, DM * 6 * DM * 2);
    const unsigned rt_vo = (unsigned)((lane >> 2) * 7680 + mcol0 * 2 + (lane & 3) * 16);
    auto dma_rt = [&](int m) { dma16(rsrt, lds0 + L_RT + m * 1024, rt_vo, (unsigned)m * 16u * 7680u); };
    if (!crit) {
#pragma unroll
      for (int i = 0; i < PACE; ++i) kr[i] = ld_nt(rsk, vo, i * 7168);
    }
    if (has_mod) { for (int m = w; m < 40; m += NSW) dma_rt(m); }
    SCHED();
    bar();
    if (has_mod) {
      silu_c();
      VMWAIT(0);
      asm volatile("s_waitcnt lgkmcnt(0)" ::: "memory");
      if (lane == 0) __hip_atomic_fetch_add((LAS unsigned*)(L + L_FLAG), 1u, __ATOMIC_RELAXED, __HIP_MEMORY_SCOPE_WORKGROUP);
    }
    {
      const rsrc_t rsq = mkrs(p.wq, DM * NH * DH * 2);
      for (int n = w; n < 20; n += NSW) {
        const int rl = 4 * n + g;
        dma16(rsq, lds0 + L_QT + n * 1024, (unsigned)((80 * b + rl) * 8192 + h * 256 + ((i16 ^ (2 * (rl & 7))) * 16)), 0);
      }
    }
    if (crit) {
#pragma unroll
      for (int i = 0; i < PACE; ++i) kr[i] = ld_nt(rsk, vo, i * 7168);
    }
    SCHED();
    const rsrc_t rsw1 = mkrs(p.w1, DM * FF * 2);
    const rsrc_t rsw2 = mkrs(p.w2 + (size_t)(is_mlp ? mm : 0) * 32 * DM, 32 * DM * 2);
    auto slot = [&](int s) {
      const int n = 7 * s + w;
      if (n < 40) {
        dma16(rsw1, lds0 + L_RT + n * 1024, (unsigned)((lane >> 2) * (FF * 2) + mm * 64 + (lane & 3) * 16), (unsigned)n * 16u * (FF * 2));
      } else if (n < 80) {
        const int ci = (n - 40) * 64 + lane, row = ci / 80, pos = ci - 80 * row;
        dma16(rsw2, lds0 + L_W2 + (n - 40) * 1024, (unsigned)(row * 1280 + (pos ^ (2 * (row & 3))) * 16), 0);
      }
    };
    const bool heavy = HEAVY == 1 ? (is_mlp || has_mod) : HEAVY == 2 ? is_mlp : false;
#pragma unroll
    for (int i = PACE; i < NL + NPRE; ++i) {
      sched(i, !is_mlp);
      if (heavy) VMWAIT(PACE); else VMWAIT(PACE - 1);
      if (i < NL) kr[i] = ld_nt(rsk, vo, i * 7168); else vr[i - NL] = ld_nt(rsv, vo, (i - NL) * 7168);
      SCHED();
      if (i == PACE + 4) {
        asm volatile("s_waitcnt lgkmcnt(0)" ::: "memory");
        if (lane == 0) __hip_atomic_fetch_add((LAS unsigned*)(L + L_FLAG) + 2, 1u, __ATOMIC_RELAXED, __HIP_MEMORY_SCOPE_WORKGROUP);
        STAMP(1);
      }
      if (i >= 7 && (i % 4) == 3 && (i - 7) / 4 < 12) { if (is_mlp) slot((i - 7) / 4); SCHED(); }
    }
    STAMP(2);
    {
      unsigned spins = 0;
      while (flags[1] == 0u) { __builtin_amdgcn_s_sleep(1); if (++spins > SPIN_L) break; }
    }
    CFENCE();
    STAMP(3);
    const v4u q4 = *(const LAS v4u*)(L + L_Q + i16 * 16);
    float s[NL];
#pragma unroll
    for (int i = 0; i < NL; ++i) {
      float d = dot2(kr[i].x, q4.x, 0.f);
      d = dot2(kr[i].y, q4.y, d); d = dot2(kr[i].z, q4.z, d); d = dot2(kr[i].w, q4.w, d);
      s[i] = row16_sum(d);
      if ((i & 3) == 3 && NPRE + (i >> 2) < NL) { SCHED(); sched(NL + NPRE + (i >> 2), !is_mlp); if (heavy) VMWAIT(PACE); else VMWAIT(PACE - 1); vr[NPRE + (i >> 2)] = ld_nt(rsv, vo, (NPRE + (i >> 2)) * 7168); SCHED(); }
    }
    constexpr int NV1 = NPRE + (NL >> 2);
    if (w >= 4) s[NL - 1] = -INFINITY;
    float m = s[0];
#pragma unroll
    for (int i = 1; i < NL; ++i) m = fmaxf(m, s[i]);
    m = xrow_max(m);
    constexpr float C2 = 0.08838834764831845f * 1.4426950408889634f;
    const float mc = m * C2;
    unsigned pp[NL];
    float l = 0.f;
#pragma unroll
    for (int i = 0; i < NL; ++i) {
      pp[i] = pk2(0.f, __builtin_amdgcn_exp2f(fmaf(s[i], C2, -mc)));
      l += asf(pp[i]);
    }
    l = xrow_sum(l);
    STAMP(4);
    float acc[8];
#pragma unroll
    for (int k = 0; k < 8; ++k) acc[k] = 0.f;
#pragma unroll
    for (int i = 0; i < NL; ++i) {
      const unsigned p1 = pp[i], p0 = p1 >> 16;
      acc[0] = dot2(vr[i].x, p0, acc[0]); acc[1] = dot2(vr[i].x, p1, acc[1]);
      acc[2] = dot2(vr[i].y, p0, acc[2]); acc[3] = dot2(vr[i].y, p1, acc[3]);
      acc[4] = dot2(vr[i].z, p0, acc[4]); acc[5] = dot2(vr[i].z, p1, acc[5]);
      acc[6] = dot2(vr[i].w, p0, acc[6]); acc[7] = dot2(vr[i].w, p1, acc[7]);
      SCHED();
      if (NV1 + i < NL) { sched(NL + NV1 + i, !is_mlp); if (heavy) VMWAIT(PACE); else VMWAIT(PACE - 1); vr[NV1 + i] = ld_nt(rsv, vo, (NV1 + i) * 7168); SCHED(); }
      if (NV1 + i == NL - 1 - LEAD) {
        if (lane == 0) __hip_atomic_fetch_add((LAS unsigned*)(L + L_FLAG) + 3, 1u, __ATOMIC_RELAXED, __HIP_MEMORY_SCOPE_WORKGROUP);
      }
    }
    STAMP(5);
#pragma unroll
    for (int k = 0; k < 8; ++k) acc[k] = xrow_sum(acc[k]);
    if (g == 0) {
      LAS float* mr = (LAS float*)(L + L_MRG) + w * 132;
      *(LAS f4*)(mr + 4 + 8 * i16) = f4{acc[0], acc[1], acc[2], acc[3]};
      *(LAS f4*)(mr + 8 + 8 * i16) = f4{acc[4], acc[5], acc[6], acc[7]};
      if (i16 == 0) { mr[0] = m; mr[1] = l; }
    }
  } else {
    __builtin_amdgcn_s_setprio(3);
    if (lane == 0) { flags[0] = 0u; flags[1] = 0u; flags[2] = 0u; flags[3] = 0u; }
    if (ABL_CTRL) {
      bar();
      while ((unsigned)__builtin_amdgcn_s_memrealtime() - tstart < 800) __builtin_amdgcn_s_sleep(8);
      if (lane == 0) flags[1] = 1u;
    } else {
    const unsigned E = __builtin_amdgcn_raw_buffer_load_b32(rsws, WS_EPOCH, 0, 16);
    unsigned warm = 0;
    {
      const int n = sl * 64 + lane, pi = n / 640, row = n - 640 * pi, pr = xcd + 8 * pi;
      if (WARM && pr < 20) warm = __builtin_amdgcn_raw_buffer_load_b32(mkrs(p.wmod, DM * 6 * DM * 2), (unsigned)(row * 7680 + pr * 128), 0, 2);
    }
    const GAS v4u* xp = (const GAS v4u*)p.x;
    v4u xr[10];
    const int sj = lane >> 3, sub = lane & 7;
#pragma unroll
    for (int u = 0; u < 10; ++u) xr[u] = xp[sj * 80 + sub + 8 * u];
    SCHED();
    bar();
    unsigned xh[5];
#pragma unroll
    for (int u = 0; u < 5; ++u) { const int idx = u * 64 + lane, j = idx / 40, m = idx - 40 * j; xh[u] = ((const GAS unsigned*)p.x)[j * 320 + 40 * b + m]; }
    if (!ABL_SENT) {
      const v4u sv = {SENT, SENT, SENT, SENT};
      if (lane < 16) st_sc(sv, rsws, WS_OX + (unsigned)(h * 8 + b) * 256 + lane * 16);
      if (lane < 5) st_sc2(v2u{SENT, SENT}, rsws, WS_X1B + (unsigned)(b * 320 + 10 * h) * 4 + lane * 8);
    }
    STAMP(1);
    {
      float sm = 0.f;
#pragma unroll
      for (int u = 0; u < 10; ++u)
        sm += ((blo(xr[u].x) + bhi(xr[u].x)) + (blo(xr[u].y) + bhi(xr[u].y))) + ((blo(xr[u].z) + bhi(xr[u].z)) + (blo(xr[u].w) + bhi(xr[u].w)));
      const float mean = oct_sum(sm) * (1.f / DM);
      float sq = 0.f;
#pragma unroll
      for (int u = 0; u < 10; ++u) {
#pragma unroll
        for (int k = 0; k < 4; ++k) { const float a = blo(xr[u][k]) - mean, c2 = bhi(xr[u][k]) - mean; sq = fmaf(a, a, sq); sq = fmaf(c2, c2, sq); }
      }
      const float var = oct_sum(sq) * (1.f / DM);
      if (sub == 0) {
        LAS float* st = (LAS float*)(L + L_STAT) + sj * 2;
        st[0] = rbf(mean);
        st[1] = rcp(rbf(__builtin_amdgcn_sqrtf(rbf(rbf(var) + 1e-5f))));
      }
    }
    VMWAIT(0);
    asm volatile("" :: "v"(warm));
    const unsigned T = rfl(E) + 1u;
    const unsigned t10 = T & 0x3FFu;
    if (lane == 0) flags[4] = T;
    if (lane == 0) st_sc2(v2u{T, ~T}, rsws, WS_INIT + (unsigned)(b * 32 + h) * 8);
    STAMP2(9);
    if (has_mod) {
      unsigned spins = 0;
      while (flags[0] < (unsigned)NSW) { __builtin_amdgcn_s_sleep(1); if (++spins > SPIN_L) break; }
      CFENCE();
      STAMP(2);
      f4 a0 = {0, 0, 0, 0}, a1 = {0, 0, 0, 0};
      const LAS unsigned char* ap = L + L_RT + (8 * g + tq) * 64 + tp * 8;
      const LAS unsigned char* bp = L + L_SC + (lane & 7) * 1296 + g * 16;
#pragma unroll 2
      for (int s = 0; s < 20; ++s) {
        const v4u bb = *(const LAS v4u*)(bp + s * 64);
        const v2u x0 = trread(ap + s * 2048), x1 = trread(ap + s * 2048 + 256);
        const v2u y0 = trread(ap + s * 2048 + 32), y1 = trread(ap + s * 2048 + 256 + 32);
        a0 = mfma32(v4u{x0.x, x0.y, x1.x, x1.y}, bb, a0);
        a1 = mfma32(v4u{y0.x, y0.y, y1.x, y1.y}, bb, a1);
      }
      if (i16 < 8) {
        const unsigned o = WS_MODG + (unsigned)(i16 * 1920 + (mcol0 + 4 * g) / 2) * 8;
        st_sc(v4u{pk2(a0[0], a0[1]), T, pk2(a0[2], a0[3]), T}, rsws, o);
        st_sc(v4u{pk2(a1[0], a1[1]), T, pk2(a1[2], a1[3]), T}, rsws, o + 64);
      }
    }
    STAMP(3);
    {
      v2u sa[5], ca[5], fl[4];
      if (D1) { while ((unsigned)__builtin_amdgcn_s_memrealtime() - tstart < D1) __builtin_amdgcn_s_sleep(2); }
      STAMP2(10);
      if (ABL_G1) { for (int u = 0; u < 5; ++u) { sa[u] = v2u{0u, T}; ca[u] = v2u{0u, T}; } }
      for (unsigned spins = 0; !ABL_G1; ++spins) {
        CFENCE();
        bool ok = true;
#pragma unroll
        for (int u = 0; u < 5; ++u) {
          const int idx = u * 64 + lane, j = idx / 40, m = idx - 40 * j;
          const unsigned o = WS_MODG + (unsigned)(j * 1920 + 40 * b + m) * 8;
          sa[u] = ld_sc2(rsws, o);
          ca[u] = ld_sc2(rsws, o + 320 * 8);
        }
#pragma unroll
        for (int u = 0; u < 4; ++u) fl[u] = ld_sc2(rsws, WS_INIT + (unsigned)(u * 64 + lane) * 8);
#pragma unroll
        for (int u = 0; u < 5; ++u) ok = ok && sa[u].y == T && ca[u].y == T;
#pragma unroll
        for (int u = 0; u < 4; ++u) ok = ok && fl[u].x == T && fl[u].y == ~T;
        if (__all(ok) || spins > SPIN_G) break;
        __builtin_amdgcn_s_sleep(PSLEEP);
      }
      STAMP2(11);
      asm volatile("s_waitcnt lgkmcnt(0)" ::: "memory");
#pragma unroll
      for (int u = 0; u < 5; ++u) {
        const int idx = u * 64 + lane, j = idx / 40, m = idx - 40 * j;
        const float mean = ((LAS float*)(L + L_STAT))[2 * j], den = ((LAS float*)(L + L_STAT))[2 * j + 1];
        const float n0 = rbf(rbf(blo(xh[u]) - mean) * den), n1 = rbf(rbf(bhi(xh[u]) - mean) * den);
        const float h0 = rbf(n0 * rbf(1.f + blo(ca[u].x))) + blo(sa[u].x), h1 = rbf(n1 * rbf(1.f + bhi(ca[u].x))) + bhi(sa[u].x);
        *(LAS unsigned*)(L + L_HS + j * 176 + m * 4) = pk2(h0, h1);
      }
    }
    STAMP(4);
    {
      unsigned spins = 0;
      while (flags[2] < (unsigned)NSW) { __builtin_amdgcn_s_sleep(1); if (++spins > SPIN_L) break; }
      CFENCE();
    }
    STAMP2(12);
    {
      f4 qa[8];
#pragma unroll
      for (int t = 0; t < 8; ++t) qa[t] = f4{0, 0, 0, 0};
#pragma unroll
      for (int s = 0; s < 5; ++s) {
        const v2u bb = *(const LAS v2u*)(L + L_HS + (lane & 7) * 176 + s * 32 + g * 8);
        const int row = 16 * s + 4 * g + tq;
        const LAS unsigned char* rp = L + L_QT + row * 256 + (tp & 1) * 8;
        const int sw = 2 * (row & 7);
#pragma unroll
        for (int t = 0; t < 8; ++t) qa[t] = mfma16(trread(rp + (((2 * t + (tp >> 1)) ^ sw) * 16)), bb, qa[t]);
      }
      if (i16 < 8 && !ABL_G2) {
        const unsigned o = WS_QP + (unsigned)(((h * 8 + b) * 8 + i16) * 128 + 4 * g) * 4;
#pragma unroll
        for (int t = 0; t < 8; ++t) st_sc(tg4(qa[t], t10), rsws, o + t * 64);
      }
    }
    STAMP(5);
    const unsigned tpub = (unsigned)__builtin_amdgcn_s_memrealtime();
    {
      v4u qv[4];
      if (D2) { while ((unsigned)__builtin_amdgcn_s_memrealtime() - tpub < D2) __builtin_amdgcn_s_sleep(2); }
      STAMP2(13);
      if (ABL_G2) { for (int u = 0; u < 4; ++u) qv[u] = v4u{t10, t10, t10, t10}; }
      for (unsigned spins = 0; !ABL_G2; ++spins) {
        CFENCE();
        bool ok = true;
#pragma unroll
        for (int u = 0; u < 4; ++u) qv[u] = ld_sc(rsws, WS_QP + (unsigned)(((h * 8 + 2 * u + (lane >> 5)) * 8 + b) * 128 + 4 * (lane & 31)) * 4);
#pragma unroll
        for (int u = 0; u < 4; ++u) ok = ok && tagged4(qv[u], t10);
        if (__all(ok) || spins > SPIN_G) break;
        __builtin_amdgcn_s_sleep(PSLEEP);
      }
      f4 qs;
#pragma unroll
      for (int k = 0; k < 4; ++k) qs[k] = x32_sum((untg(qv[0][k]) + untg(qv[1][k])) + (untg(qv[2][k]) + untg(qv[3][k])));
      if (lane < 32) *(LAS v2u*)(L + L_Q + lane * 8) = v2u{pk2(qs[0], qs[1]), pk2(qs[2], qs[3])};
      asm volatile("s_waitcnt lgkmcnt(0)" ::: "memory");
      if (lane == 0) flags[1] = 1u;
    }
    STAMP(6);
    if (!ABL_G3) {
      const unsigned go = WS_MODG + (unsigned)(b * 1920 + (lane < 10 ? 640 + 10 * h + lane : 1600 + 10 * h + (lane < 20 ? lane - 10 : 0))) * 8;
      const unsigned xv = ((const GAS unsigned*)p.x)[b * 320 + 10 * h + (lane < 10 ? lane : 0)];
      v2u gv;
      for (unsigned spins = 0;; ++spins) {
        CFENCE();
        gv = ld_sc2(rsws, go);
        if (__all(gv.y == T) || spins > SPIN_G) break;
        __builtin_amdgcn_s_sleep(32);
      }
      if (lane < 20) *(LAS unsigned*)(L + L_VEC + lane * 4) = gv.x;
      if (lane < 10) *(LAS unsigned*)(L + L_VEC + 80 + lane * 4) = xv;
      if (is_mlp) {
        for (int j = 0; j < 8; ++j) {
          v4u cv[5];
          for (unsigned spins = 0;; ++spins) {
            CFENCE();
            bool ok = true;
#pragma unroll
            for (int u = 0; u < 5; ++u) cv[u] = ld_sc(rsws, WS_MODG + (unsigned)(j * 1920 + 960) * 8 + (unsigned)(u * 64 + lane) * 16);
#pragma unroll
            for (int u = 0; u < 5; ++u) ok = ok && cv[u].y == T && cv[u].w == T;
            if (__all(ok) || spins > SPIN_G) break;
            __builtin_amdgcn_s_sleep(32);
          }
#pragma unroll
          for (int u = 0; u < 5; ++u) {
            const int pc = u * 64 + lane;
            const unsigned o = (pc < 160 ? 10240u + (unsigned)(j * 320 + 2 * pc) * 4 : (unsigned)(j * 320 + 2 * (pc - 160)) * 4);
            const v2u sc1 = {pk2(1.f + blo(cv[u].x), 1.f + bhi(cv[u].x)), pk2(1.f + blo(cv[u].z), 1.f + bhi(cv[u].z))};
            *(LAS v2u*)(L + L_CS + o) = pc < 160 ? v2u{cv[u].x, cv[u].z} : sc1;
          }
        }
      }
    }
    STAMP(7);
    {
      unsigned spins = 0;
      while (flags[3] < (unsigned)NSW) { __builtin_amdgcn_s_sleep(1); if (++spins > SPIN_L) break; }
      CFENCE();
    }
    {
      const rsrc_t rswo = mkrs(p.wo + (size_t)h * DH * DM, DH * DM * 2);
      for (int n = 0; n < 20; ++n) {
        const int ci = n * 64 + lane, row = ci / 10, ch = ci - 10 * row;
        dma16(rswo, lds0 + L_T1 + n * 1024, (unsigned)(row * 1280 + b * 160 + ch * 16), 0);
      }
    }
    }
  }

  bar();
  STAMP(8);
  if (ABL_CTRL || ABL_TAIL) {
    STAMP(15);
    return;
  }
  const unsigned t10 = flags[4] & 0x3FFu;
  auto until = [&](unsigned t0, unsigned ticks) { while ((unsigned)__builtin_amdgcn_s_memrealtime() - t0 < ticks) __builtin_amdgcn_s_sleep(1); };
  if (w == 0) {
    if (lane < 32) {
      const LAS float* mr = (const LAS float*)(L + L_MRG);
      float M = mr[0];
#pragma unroll
      for (int u = 1; u < NSW; ++u) M = fmaxf(M, mr[u * 132]);
      constexpr float C2 = 0.08838834764831845f * 1.4426950408889634f;
      float Lsum = 0.f;
      f4 o = {0, 0, 0, 0};
#pragma unroll
      for (int u = 0; u < NSW; ++u) {
        const float f = __builtin_amdgcn_exp2f((mr[u * 132] - M) * C2);
        Lsum = fmaf(f, mr[u * 132 + 1], Lsum);
        o += f * *(const LAS f4*)(mr + u * 132 + 4 + 4 * lane);
      }
      const float inv = rcp(Lsum);
      st_sc2(v2u{canonu(pk2(o[0] * inv, o[1] * inv)), canonu(pk2(o[2] * inv, o[3] * inv))}, rsws, WS_OX + (unsigned)(h * 8 + b) * 256 + lane * 8);
    }
  } else if (w < 3) {
    const int t = tid - 64;
    __builtin_amdgcn_s_sleep(TD_A);
    v4u v;
    for (unsigned spins = 0;; ++spins) {
      CFENCE();
      v = ld_sc(rsws, WS_OX + (unsigned)h * 2048 + t * 16);
      if (__all(valid4(v)) || spins > SPIN_G) break;
      __builtin_amdgcn_s_sleep(TS);
    }
    *(LAS v4u*)(L + L_OS + (t >> 4) * 272 + (t & 15) * 16) = v;
  } else if (w == 7) {
    VMWAIT(0);
  }
  bar();
  const unsigned tB2 = (unsigned)__builtin_amdgcn_s_memrealtime();
  STAMP(9);
  if (w < 4) {
    const LAS unsigned char* bp = L + L_OS + (lane & 7) * 272 + g * 16;
    v4u bb[4];
#pragma unroll
    for (int s = 0; s < 4; ++s) bb[s] = *(const LAS v4u*)(bp + s * 64);
    for (int t = w; t < 5; t += 4) {
      f4 y = {0, 0, 0, 0};
      const LAS unsigned char* ap = L + L_T1 + (8 * g + tq) * 160 + (16 * t + 4 * tp) * 2;
#pragma unroll
      for (int s = 0; s < 4; ++s) {
        const v2u x0 = trread(ap + s * 32 * 160), x1 = trread(ap + s * 32 * 160 + 4 * 160);
        y = mfma32(v4u{x0.x, x0.y, x1.x, x1.y}, bb[s], y);
      }
      const int cc = 4 * t + g;
      if (i16 < 8) st_sc(tg4(y, t10), rsws,
                         WS_T1P + (unsigned)((((i16 * 32 + 4 * b + cc / 5) * 32 + h) * 5 + cc % 5) * 16));
    }
  } else if (w < 7) {
    const int t = tid - 256;
    const bool act = t < 160;
    const unsigned o = WS_T1P + (unsigned)((b * 32 + h) * 160 + (act ? t : 0)) * 16;
    until(tB2, GB);
    v4u v;
    for (unsigned spins = 0;; ++spins) {
      CFENCE();
      v = ld_sc(rsws, o);
      if (__all(tagged4(v, t10)) || spins > SPIN_G) break;
      __builtin_amdgcn_s_sleep(TS);
    }
    if (act) *(LAS f4*)(L + L_T4P + t * 16) = f4{untg(v.x), untg(v.y), untg(v.z), untg(v.w)};
  }
  bar();
  if (w == 4) {
    const int e = lane >> 3, c = lane & 7;
    f4 sm = {0, 0, 0, 0};
    if (c < 5) {
#pragma unroll
      for (int u = 0; u < 4; ++u) sm += *(const LAS f4*)(L + L_T4P + ((4 * e + u) * 5 + c) * 16);
    }
#pragma unroll
    for (int k = 0; k < 4; ++k) { float x = sm[k]; x += dpp<0x128>(x); sm[k] = xrow_sum(x); }
    if (lane < 5) {
      const LAS unsigned* vec = (const LAS unsigned*)(L + L_VEC);
      const unsigned g0 = vec[2 * lane], g1 = vec[2 * lane + 1], x0 = vec[20 + 2 * lane], x1 = vec[20 + 2 * lane + 1];
      const float r0 = rbf(blo(x0) + rbf(blo(g0) * rbf(sm[0]))), r1 = rbf(bhi(x0) + rbf(bhi(g0) * rbf(sm[1])));
      const float r2 = rbf(blo(x1) + rbf(blo(g1) * rbf(sm[2]))), r3 = rbf(bhi(x1) + rbf(bhi(g1) * rbf(sm[3])));
      st_sc2(v2u{canonu(pk2(r0, r1)), canonu(pk2(r2, r3))}, rsws, WS_X1B + (unsigned)(b * 320 + 10 * h + 2 * lane) * 4);
      *(LAS f4*)(L + L_X1O + lane * 16) = f4{r0, r1, r2, r3};
    }
  }
  bar();
  const unsigned tB = (unsigned)__builtin_amdgcn_s_memrealtime();
  STAMP(10);
  if (is_mlp) {
    {
      const unsigned o = WS_X1B + (unsigned)w * 1280 + lane * 4;
      unsigned cs[5], sh[5];
#pragma unroll
      for (int u = 0; u < 5; ++u) {
        cs[u] = *(const LAS unsigned*)(L + L_CS + (w * 320 + u * 64 + lane) * 4);
        sh[u] = *(const LAS unsigned*)(L + L_CS + 10240 + (w * 320 + u * 64 + lane) * 4);
      }
      until(tB, GC);
      unsigned xv[5];
      for (unsigned spins = 0;; ++spins) {
        CFENCE();
        bool ok = true;
#pragma unroll
        for (int u = 0; u < 5; ++u) xv[u] = __builtin_amdgcn_raw_buffer_load_b32(rsws, o + u * 256, 0, 16);
#pragma unroll
        for (int u = 0; u < 5; ++u) ok = ok && xv[u] != SENT;
        if (__all(ok) || spins > SPIN_G) break;
        __builtin_amdgcn_s_sleep(TS);
      }
      const float x00 = blo(rfl(xv[0]));
      float s1 = 0.f, s2 = 0.f;
#pragma unroll
      for (int u = 0; u < 5; ++u) {
        const float d0 = blo(xv[u]) - x00, d1 = bhi(xv[u]) - x00;
        s1 += d0 + d1; s2 = fmaf(d0, d0, fmaf(d1, d1, s2));
      }
      s1 = row16_sum(s1); s2 = row16_sum(s2);
      s1 = xrow_sum(s1) * (1.f / DM); s2 = xrow_sum(s2) * (1.f / DM);
      const float rs = __builtin_amdgcn_rsqf(fmaxf(s2 - s1 * s1, 0.f) + 1e-5f), mr = -(x00 + s1) * rs;
#pragma unroll
      for (int u = 0; u < 5; ++u) {
        const float n0 = fmaf(blo(xv[u]), rs, mr), n1 = fmaf(bhi(xv[u]), rs, mr);
        *(LAS unsigned*)(L + L_H2 + w * 1296 + (u * 64 + lane) * 4) = pk2(fmaf(n0, blo(cs[u]), blo(sh[u])), fmaf(n1, bhi(cs[u]), bhi(sh[u])));
      }
    }
    bar();
    STAMP(11);
    {
      const int t = w & 1, kq = w >> 1;
      f4 a = {0, 0, 0, 0};
      const LAS unsigned char* ap = L + L_RT + (160 * kq + 8 * g + tq) * 64 + t * 32 + tp * 8;
      const LAS unsigned char* bp = L + L_H2 + (lane & 7) * 1296 + (160 * kq + 8 * g) * 2;
#pragma unroll
      for (int s = 0; s < 5; ++s) {
        const v4u bb = *(const LAS v4u*)(bp + s * 64);
        const v2u x0 = trread(ap + s * 2048), x1 = trread(ap + s * 2048 + 256);
        a = mfma32(v4u{x0.x, x0.y, x1.x, x1.y}, bb, a);
      }
      if (i16 < 8) *(LAS f4*)(L + L_T4P + (((kq * 2 + t) * 4 + g) * 8 + i16) * 16) = a;
    }
    bar();
    if (tid < 128) {
      const int j = tid >> 4, c = 2 * (tid & 15), t = c >> 4, gg = (c & 15) >> 2, r = c & 3;
      float z0 = 0.f, z1 = 0.f;
#pragma unroll
      for (int kq = 0; kq < 4; ++kq) {
        const LAS float* z = (const LAS float*)(L + L_T4P + (((kq * 2 + t) * 4 + gg) * 8 + j) * 16) + r;
        z0 += z[0]; z1 += z[1];
      }
      *(LAS unsigned*)(L + L_H1 + j * 80 + c * 2) = pk2(silu(rbf(z0)), silu(rbf(z1)));
    }
    bar();
    STAMP(12);
    {
      const v4u bb = *(const LAS v4u*)(L + L_H1 + (lane & 7) * 80 + g * 16);
      const int r0 = 8 * g + tq;
      const LAS unsigned char* ap = L + L_W2 + r0 * 1280 + (tp & 1) * 8;
      const int f = 2 * (r0 & 3);
#pragma unroll
      for (int tt = 0; tt < 5; ++tt) {
        const int ch = (2 * (5 * w + tt) + (tp >> 1)) ^ f;
        const v2u x0 = trread(ap + ch * 16), x1 = trread(ap + 4 * 1280 + ch * 16);
        const f4 y = mfma32(v4u{x0.x, x0.y, x1.x, x1.y}, bb, f4{0, 0, 0, 0});
        if (i16 < 8) *(LAS v4u*)(L + L_RT + i16 * 2576 + (16 * (5 * w + tt) + 4 * g) * 4) = tg4(y, t10);
      }
    }
    bar();
    {
#pragma unroll
      for (int u = 0; u < 3; ++u) {
        const int pc = u * 512 + tid, j = pc / 160, c = pc - 160 * j;
        if (pc < 1280) st_sc(*(const LAS v4u*)(L + L_RT + j * 2576 + c * 16), rsws, WS_T5P + (unsigned)mm * 20480 + (unsigned)pc * 16);
      }
    }
  }
  const unsigned tD = (unsigned)__builtin_amdgcn_s_memrealtime();
  STAMP(13);
  if (w < 5) {
    const bool act = tid < NMLP * 5;
    const int dm = act ? tid / 5 : 0, dc = act ? tid - 5 * dm : 0;
    const unsigned o = WS_T5P + (unsigned)((dm * 8 + b) * 640 + 20 * h + 4 * dc) * 4;
    if (is_mlp) until(tD, GD1); else until(tB, GD2);
    v4u v;
    for (unsigned spins = 0;; ++spins) {
      CFENCE();
      v = ld_sc(rsws, o);
      if (__all(tagged4(v, t10)) || spins > SPIN_G) break;
      __builtin_amdgcn_s_sleep(TS);
    }
    if (act) *(LAS f4*)(L + L_W2 + tid * 16) = f4{untg(v.x), untg(v.y), untg(v.z), untg(v.w)};
  }
  bar();
  if (w == 0) {
    const int e = lane / 5, c = lane - 5 * e;
    f4 sm = {0, 0, 0, 0};
    if (lane < 45) {
#pragma unroll
      for (int u = 0; u < 6; ++u) sm += *(const LAS f4*)(L + L_W2 + ((6 * e + u) * 5 + c) * 16);
      *(LAS f4*)(L + L_W2 + 8192 + lane * 16) = sm;
    }
    asm volatile("s_waitcnt lgkmcnt(0)" ::: "memory");
    if (lane < 5) {
      sm = f4{0, 0, 0, 0};
#pragma unroll
      for (int u = 0; u < 9; ++u) sm += *(const LAS f4*)(L + L_W2 + 8192 + (u * 5 + lane) * 16);
      const LAS unsigned* vec = (const LAS unsigned*)(L + L_VEC);
      const unsigned g0 = vec[10 + 2 * lane], g1 = vec[10 + 2 * lane + 1];
      const f4 x1 = *(const LAS f4*)(L + L_X1O + lane * 16);
      const float r0 = x1[0] + rbf(blo(g0) * rbf(sm[0])), r1 = x1[1] + rbf(bhi(g0) * rbf(sm[1]));
      const float r2 = x1[2] + rbf(blo(g1) * rbf(sm[2])), r3 = x1[3] + rbf(bhi(g1) * rbf(sm[3]));
      *(GAS v2u*)(p.out + b * DM + 20 * h + 4 * lane) = v2u{pk2(r0, r1), pk2(r2, r3)};
    }
  }
  if (id == 0 && tid == 448) {
    const unsigned E = __builtin_amdgcn_raw_buffer_load_b32(rsws, WS_EPOCH, 0, 16);
    __builtin_amdgcn_raw_buffer_store_b32(E + 1u, rsws, WS_EPOCH, 0, 16);
  }
  STAMP(15);
}

}

extern "C" void kernel_launch(void* const* d_in, const int* in_sizes, int n_in, void* d_out, int out_size, void* d_ws,
                              size_t ws_size, hipStream_t stream) {
  Params p;
  p.x = (const u16*)d_in[0];
  p.kc = (const u16*)d_in[1];
  p.vc = (const u16*)d_in[2];
  p.wq = (const u16*)d_in[3];
  p.wo = (const u16*)d_in[4];
  p.w1 = (const u16*)d_in[6];
  p.w2 = (const u16*)d_in[7];
  p.c = (const u16*)d_in[8];
  p.wmod = (const u16*)d_in[9];
  p.out = (u16*)d_out;
  p.ws = (unsigned char*)d_ws;
  hipLaunchKernelGGL(fused_block, dim3(256), dim3(512), 0, stream, p);
}
```

```cpp
#define KAUX 2
#define VAUX 2
#include <hip/hip_runtime.h>
#include <stdint.h>

#define DIAG 0

namespace {

typedef unsigned short u16;
typedef unsigned v4u __attribute__((ext_vector_type(4)));
typedef unsigned v2u __attribute__((ext_vector_type(2)));
typedef float f4 __attribute__((ext_vector_type(4)));
typedef short s4 __attribute__((ext_vector_type(4)));
typedef short s8 __attribute__((ext_vector_type(8)));
typedef __bf16 bf2 __attribute__((ext_vector_type(2)));
typedef __bf16 bf8 __attribute__((ext_vector_type(8)));
typedef __amdgpu_buffer_rsrc_t rsrc_t;
#define LAS __attribute__((address_space(3)))
#define GAS __attribute__((address_space(1)))
#define DI __device__ __forceinline__

constexpr int NB = 8, DM = 640, NH = 32, DH = 128, SK = 4096, WIN = 1024, FF = 1728;
constexpr int NSW = 7;
constexpr int NL = 37;
#define TD_A 8
#define TD_B 8
#define TD_C 16
#define TD_D 8
#define TD_D2 4
#define TS 6
#define ABL_CTRL 0
#define ABL_SLOT 0
#define ABL_ROLE 0
#define ABL_SENT 0
#define LEAD 3
#define MP0 12
#define MPACE 11
#define DELTA 0
#define HEAVY 1
#define ABL_G1 0
#define ABL_G2 0
#define ABL_G3 0
#define ABL_TAIL 0
#define GB 40
#define GC 40
#define GD1 40
#define GD2 190
#define WARM 1
#define INDIC 1
#define ABL_WO 0
#define ABL_W12 0
#define D1 600
#define D2 100
#define PSLEEP 32
#define DPACE 15
#define PACE_ 3
constexpr int PACE = PACE_;
#define NPRE_ 16
constexpr int NPRE = NPRE_;

constexpr int NMLP = 54;
constexpr unsigned WS_EPOCH = 0;
constexpr unsigned WS_INIT = 4096;
constexpr unsigned WS_MODG = 8192;
constexpr unsigned WS_QP = WS_MODG + NB * 1920 * 8;
constexpr unsigned WS_OX = WS_QP + NH * 8 * 8 * 128 * 4;
constexpr unsigned WS_T1P = WS_OX + NH * 8 * 256;
constexpr unsigned WS_X1B = WS_T1P + NB * NH * DM * 4;
constexpr unsigned WS_T5P = WS_X1B + NB * 320 * 4;
constexpr unsigned WS_END = WS_T5P + NMLP * NB * DM * 4;
constexpr unsigned WS_DIAG = 4u << 20;
constexpr unsigned SENT = 0xFFFFFFFFu;
static_assert(WS_END <= WS_DIAG, "workspace map");

constexpr unsigned L_RT = 0;
constexpr unsigned L_QT = 40960;
constexpr unsigned L_W2 = 61440;
constexpr unsigned L_T1 = 102400;
constexpr unsigned L_CS = 122880;
constexpr unsigned L_MISC = 143360;
constexpr unsigned L_FLAG = L_MISC;
constexpr unsigned L_Q = L_MISC + 64;
constexpr unsigned L_STAT = L_Q + 256;
constexpr unsigned L_VEC = L_STAT + 64;
constexpr unsigned L_X1O = L_VEC + 128;
constexpr unsigned L_SC = L_X1O + 128;
constexpr unsigned L_H2 = L_SC;
constexpr unsigned L_HS = L_SC + 8 * 1296;
constexpr unsigned L_MRG = L_HS + 8 * 176;
constexpr unsigned L_OS = L_MRG + 7 * 132 * 4;
constexpr unsigned L_T4P = L_MRG;
constexpr unsigned L_H1 = L_T4P + 4096;
constexpr unsigned L_END = 163840;
static_assert(L_OS + 8 * 272 <= L_END && L_H1 + 8 * 80 <= L_OS + 8 * 272, "LDS map");
static_assert((L_SC % 16) == 0 && (L_HS % 16) == 0 && (L_MRG % 16) == 0 && (L_OS % 16) == 0 && (L_H1 % 16) == 0, "LDS alignment");

constexpr unsigned SPIN_G = 1u << 16;
constexpr unsigned SPIN_L = 1u << 22;

struct Params {
  const u16 *x, *kc, *vc, *wq, *wo, *w1, *w2, *c, *wmod;
  u16* out;
  unsigned char* ws;
};

DI unsigned rfl(unsigned x) { return __builtin_amdgcn_readfirstlane(x); }
DI float asf(unsigned u) { return __uint_as_float(u); }
DI unsigned asu(float f) { return __float_as_uint(f); }
DI unsigned pk2(float lo, float hi) { bf2 v = {(__bf16)lo, (__bf16)hi}; return __builtin_bit_cast(unsigned, v); }
DI float rbf(float f) { return asf(pk2(f, 0.f) << 16); }
DI float blo(unsigned u) { return asf(u << 16); }
DI float bhi(unsigned u) { return asf(u & 0xffff0000u); }
DI float dot2(unsigned a, unsigned b, float c) {
  return __builtin_amdgcn_fdot2_f32_bf16(__builtin_bit_cast(bf2, a), __builtin_bit_cast(bf2, b), c, false);
}
template <int CTRL> DI float dpp(float x) {
  return __builtin_bit_cast(float, __builtin_amdgcn_mov_dpp(__builtin_bit_cast(int, x), CTRL, 0xf, 0xf, true));
}
DI float row16_sum(float x) { x += dpp<0x128>(x); x += dpp<0x124>(x); x += dpp<0x122>(x); x += dpp<0x121>(x); return x; }
DI float oct_sum(float x) { x += dpp<0xB1>(x); x += dpp<0x4E>(x); x += dpp<0x141>(x); return x; }
DI float x16_sum(float x) {
  auto s = __builtin_amdgcn_permlane16_swap(asu(x), asu(x), false, false);
  return asf(s[0]) + asf(s[1]);
}
DI float x32_sum(float x) {
  auto t = __builtin_amdgcn_permlane32_swap(asu(x), asu(x), false, false);
  return asf(t[0]) + asf(t[1]);
}
DI float xrow_sum(float x) { return x32_sum(x16_sum(x)); }
DI float xrow_max(float x) {
  auto s = __builtin_amdgcn_permlane16_swap(asu(x), asu(x), false, false);
  x = fmaxf(asf(s[0]), asf(s[1]));
  auto t = __builtin_amdgcn_permlane32_swap(asu(x), asu(x), false, false);
  return fmaxf(asf(t[0]), asf(t[1]));
}
DI float wave_sum(float x) { return xrow_sum(row16_sum(x)); }
DI f4 mfma32(v4u a, v4u b, f4 c) {
  return __builtin_amdgcn_mfma_f32_16x16x32_bf16(__builtin_bit_cast(bf8, a), __builtin_bit_cast(bf8, b), c, 0, 0, 0);
}
DI f4 mfma16(v2u a, v2u b, f4 c) {
  return __builtin_amdgcn_mfma_f32_16x16x16bf16_1k(__builtin_bit_cast(s4, a), __builtin_bit_cast(s4, b), c, 0, 0, 0);
}
DI v2u trread(const LAS unsigned char* p) {
  return __builtin_bit_cast(v2u, __builtin_amdgcn_ds_read_tr16_b64_v4i16((LAS s4*)p));
}
DI rsrc_t mkrs(const void* p, unsigned bytes) { return __builtin_amdgcn_make_buffer_rsrc((void*)p, 0, (int)bytes, 0x00020000); }
DI v4u ld_nt(rsrc_t r, unsigned vo, unsigned so) { return __builtin_amdgcn_raw_buffer_load_b128(r, vo, so, 2); }
DI v4u ld_k(rsrc_t r, unsigned vo, unsigned so) { return __builtin_amdgcn_raw_buffer_load_b128(r, vo, so, KAUX); }
DI v4u ld_v(rsrc_t r, unsigned vo, unsigned so) { return __builtin_amdgcn_raw_buffer_load_b128(r, vo, so, VAUX); }
DI v4u ld_sc(rsrc_t r, unsigned vo) { return __builtin_amdgcn_raw_buffer_load_b128(r, vo, 0, 16); }
DI v2u ld_sc2(rsrc_t r, unsigned vo) { return __builtin_amdgcn_raw_buffer_load_b64(r, vo, 0, 16); }
DI void st_sc(v4u v, rsrc_t r, unsigned vo) { __builtin_amdgcn_raw_buffer_store_b128(v, r, vo, 0, 16); }
DI void st_sc2(v2u v, rsrc_t r, unsigned vo) { __builtin_amdgcn_raw_buffer_store_b64(v, r, vo, 0, 16); }
DI unsigned canonu(unsigned u) { return u == 0xFFFFFFFFu ? 0x7FFF7FFFu : u; }
DI void dma16(rsrc_t r, unsigned lds, unsigned vo, unsigned so) {
  unsigned keep;
  asm volatile("s_nop 4\n\ts_mov_b32 %0, m0\n\ts_mov_b32 m0, %1\n\ts_nop 0\n\tbuffer_load_dwordx4 %2, %3, %4 offen nt lds\n\ts_mov_b32 m0, %0"
               : "=&s"(keep) : "s"(lds), "v"(vo), "s"(r), "s"(so) : "memory");
}
template <int N> DI void vmwait() { asm volatile("s_waitcnt vmcnt(%0)" :: "n"(N) : "memory"); }
#define VMWAIT(n) vmwait<(n)>()
DI void bar() { asm volatile("s_waitcnt lgkmcnt(0)\n\ts_barrier" ::: "memory"); }
#define CFENCE() asm volatile("" ::: "memory")
#define SCHED() __builtin_amdgcn_sched_barrier(0)
DI unsigned canon(float f) { unsigned u = asu(f); return u == SENT ? 0x7FFFFFFFu : u; }
DI unsigned tg(float f, unsigned t10) { return ((asu(f) + 0x200u) & ~0x3FFu) | t10; }
DI v4u tg4(f4 v, unsigned t10) { return v4u{tg(v[0], t10), tg(v[1], t10), tg(v[2], t10), tg(v[3], t10)}; }
DI bool tagged4(v4u v, unsigned t10) { return (((v.x ^ t10) | (v.y ^ t10) | (v.z ^ t10) | (v.w ^ t10)) & 0x3FFu) == 0u; }
DI float untg(unsigned u) { return asf(u & ~0x3FFu); }
DI bool valid4(v4u v) { return v.x != SENT && v.y != SENT && v.z != SENT && v.w != SENT; }
DI float rcp(float x) { return __builtin_amdgcn_rcpf(x); }
DI float silu(float v) { return v * rcp(1.f + __builtin_amdgcn_exp2f(v * -1.4426950408889634f)); }

#define STAMP(k) do { } while (0)
#define STAMP2(k) do { } while (0)

__global__ void __launch_bounds__(512, 2) fused_block(Params p) {
  __shared__ __attribute__((aligned(16))) unsigned char smem[L_END];
  LAS unsigned char* L = (LAS unsigned char*)smem;
  const unsigned lds0 = (unsigned)(uintptr_t)L;
  const int tid = threadIdx.x, lane = tid & 63;
  const int w = (int)rfl(tid >> 6);
  const int id = blockIdx.x, xcd = id & 7, sl = id >> 3;
  const int h = xcd * 4 + (sl >> 3), b = sl & 7;
  const int g = lane >> 4, i16 = lane & 15;
  const int tq = i16 >> 2, tp = lane & 3;
  const rsrc_t rsws = mkrs(p.ws, WS_END);
  volatile LAS unsigned* flags = (volatile LAS unsigned*)(L + L_FLAG);
  const int mpr = (sl >> 1) * 8 + xcd;
  const bool has_mod = !ABL_ROLE && sl < 16 && mpr < 60;
  const bool crit = has_mod && mpr < 20;
  const int mcol0 = (2 * mpr + (sl & 1)) * 32;
  const int fpr = ((sl - 16) >> 1) * 8 + xcd;
  const bool is_mlp = !ABL_ROLE && sl >= 16 && sl < 24 && fpr < 27;
  const int mm = 2 * fpr + (sl & 1);

  STAMP(0);
  const unsigned tstart = (unsigned)__builtin_amdgcn_s_memrealtime();
  auto sched = [&](int i, bool light) {
    if (DELTA && light) { while ((unsigned)__builtin_amdgcn_s_memrealtime() - tstart < (unsigned)(i * DELTA / 10)) __builtin_amdgcn_s_sleep(1); }
  };

  if (w < NSW) {
    const rsrc_t rsk = mkrs(p.kc + ((size_t)(b * NH + h) * SK + (SK - WIN)) * DH, WIN * DH * 2);
    const rsrc_t rsv = mkrs(p.vc + ((size_t)(b * NH + h) * SK + (SK - WIN)) * DH, WIN * DH * 2);
    const unsigned vo = (unsigned)(lane * 16 + w * 1024);
    v4u kr[NL], vr[NL], cr[2];
    if (has_mod && w < 5) {
      const GAS v4u* cp = (const GAS v4u*)p.c;
      cr[0] = cp[w * 128 + lane];
      cr[1] = cp[w * 128 + 64 + lane];
    }
    auto silu_c = [&]() {
      if (w < 5) {
#pragma unroll
        for (int u = 0; u < 2; ++u) {
          const int q = w * 128 + u * 64 + lane, j = q / 80, k8 = q - 80 * j;
          v4u o;
          o.x = pk2(silu(blo(cr[u].x)), silu(bhi(cr[u].x)));
          o.y = pk2(silu(blo(cr[u].y)), silu(bhi(cr[u].y)));
          o.z = pk2(silu(blo(cr[u].z)), silu(bhi(cr[u].z)));
          o.w = pk2(silu(blo(cr[u].w)), silu(bhi(cr[u].w)));
          *(LAS v4u*)(L + L_SC + j * 1296 + k8 * 16) = o;
        }
      }
    };
    const rsrc_t rsrt = mkrs(p.wmod, DM * 6 * DM * 2);
    const unsigned rt_vo = (unsigned)((lane >> 2) * 7680 + mcol0 * 2 + (lane & 3) * 16);
    auto dma_rt = [&](int m) { dma16(rsrt, lds0 + L_RT + m * 1024, rt_vo, (unsigned)m * 16u * 7680u); };
    if (!crit) {
#pragma unroll
      for (int i = 0; i < PACE; ++i) kr[i] = ld_k(rsk, vo, i * 7168);
    }
    if (has_mod) { for (int m = w; m < 40; m += NSW) dma_rt(m); }
    SCHED();
    bar();
    if (has_mod) {
      silu_c();
      VMWAIT(0);
      asm volatile("s_waitcnt lgkmcnt(0)" ::: "memory");
      if (lane == 0) __hip_atomic_fetch_add((LAS unsigned*)(L + L_FLAG), 1u, __ATOMIC_RELAXED, __HIP_MEMORY_SCOPE_WORKGROUP);
    }
    {
      const rsrc_t rsq = mkrs(p.wq, DM * NH * DH * 2);
      for (int n = w; n < 20; n += NSW) {
        const int rl = 4 * n + g;
        dma16(rsq, lds0 + L_QT + n * 1024, (unsigned)((80 * b + rl) * 8192 + h * 256 + ((i16 ^ (2 * (rl & 7))) * 16)), 0);
      }
    }
    if (crit) {
#pragma unroll
      for (int i = 0; i < PACE; ++i) kr[i] = ld_k(rsk, vo, i * 7168);
    }
    SCHED();
    const rsrc_t rsw1 = mkrs(p.w1, DM * FF * 2);
    const rsrc_t rsw2 = mkrs(p.w2 + (size_t)(is_mlp ? mm : 0) * 32 * DM, 32 * DM * 2);
    auto slot = [&](int s) {
      const int n = 7 * s + w;
      if (n < 40) {
        dma16(rsw1, lds0 + L_RT + n * 1024, (unsigned)((lane >> 2) * (FF * 2) + mm * 64 + (lane & 3) * 16), (unsigned)n * 16u * (FF * 2));
      } else if (n < 80) {
        const int ci = (n - 40) * 64 + lane, row = ci / 80, pos = ci - 80 * row;
        dma16(rsw2, lds0 + L_W2 + (n - 40) * 1024, (unsigned)(row * 1280 + (pos ^ (2 * (row & 3))) * 16), 0);
      }
    };
    const bool heavy = HEAVY == 1 ? (is_mlp || has_mod) : HEAVY == 2 ? is_mlp : false;
#pragma unroll
    for (int i = PACE; i < NL + NPRE; ++i) {
      sched(i, !is_mlp);
      if (heavy) VMWAIT(PACE); else VMWAIT(PACE - 1);
      if (i < NL) kr[i] = ld_k(rsk, vo, i * 7168); else vr[i - NL] = ld_v(rsv, vo, (i - NL) * 7168);
      SCHED();
      if (i == PACE + 4) {
        asm volatile("s_waitcnt lgkmcnt(0)" ::: "memory");
        if (lane == 0) __hip_atomic_fetch_add((LAS unsigned*)(L + L_FLAG) + 2, 1u, __ATOMIC_RELAXED, __HIP_MEMORY_SCOPE_WORKGROUP);
        STAMP(1);
      }
      if (i >= 7 && (i % 4) == 3 && (i - 7) / 4 < 12) { if (is_mlp) slot((i - 7) / 4); SCHED(); }
    }
    STAMP(2);
    {
      unsigned spins = 0;
      while (flags[1] == 0u) { __builtin_amdgcn_s_sleep(1); if (++spins > SPIN_L) break; }
    }
    CFENCE();
    STAMP(3);
    const v4u q4 = *(const LAS v4u*)(L + L_Q + i16 * 16);
    float s[NL];
#pragma unroll
    for (int i = 0; i < NL; ++i) {
      float d = dot2(kr[i].x, q4.x, 0.f);
      d = dot2(kr[i].y, q4.y, d); d = dot2(kr[i].z, q4.z, d); d = dot2(kr[i].w, q4.w, d);
      s[i] = row16_sum(d);
      if ((i & 3) == 3 && NPRE + (i >> 2) < NL) { SCHED(); sched(NL + NPRE + (i >> 2), !is_mlp); if (heavy) VMWAIT(PACE); else VMWAIT(PACE - 1); vr[NPRE + (i >> 2)] = ld_v(rsv, vo, (NPRE + (i >> 2)) * 7168); SCHED(); }
    }
    constexpr int NV1 = NPRE + (NL >> 2);
    if (w >= 4) s[NL - 1] = -INFINITY;
    float m = s[0];
#pragma unroll
    for (int i = 1; i < NL; ++i) m = fmaxf(m, s[i]);
    m = xrow_max(m);
    constexpr float C2 = 0.08838834764831845f * 1.4426950408889634f;
    const float mc = m * C2;
    unsigned pp[NL];
    float l = 0.f;
#pragma unroll
    for (int i = 0; i < NL; ++i) {
      pp[i] = pk2(0.f, __builtin_amdgcn_exp2f(fmaf(s[i], C2, -mc)));
      l += asf(pp[i]);
    }
    l = xrow_sum(l);
    STAMP(4);
    float acc[8];
#pragma unroll
    for (int k = 0; k < 8; ++k) acc[k] = 0.f;
#pragma unroll
    for (int i = 0; i < NL; ++i) {
      const unsigned p1 = pp[i], p0 = p1 >> 16;
      acc[0] = dot2(vr[i].x, p0, acc[0]); acc[1] = dot2(vr[i].x, p1, acc[1]);
      acc[2] = dot2(vr[i].y, p0, acc[2]); acc[3] = dot2(vr[i].y, p1, acc[3]);
      acc[4] = dot2(vr[i].z, p0, acc[4]); acc[5] = dot2(vr[i].z, p1, acc[5]);
      acc[6] = dot2(vr[i].w, p0, acc[6]); acc[7] = dot2(vr[i].w, p1, acc[7]);
      SCHED();
      if (NV1 + i < NL) { sched(NL + NV1 + i, !is_mlp); if (heavy) VMWAIT(PACE); else VMWAIT(PACE - 1); vr[NV1 + i] = ld_v(rsv, vo, (NV1 + i) * 7168); SCHED(); }
      if (NV1 + i == NL - 1 - LEAD) {
        if (lane == 0) __hip_atomic_fetch_add((LAS unsigned*)(L + L_FLAG) + 3, 1u, __ATOMIC_RELAXED, __HIP_MEMORY_SCOPE_WORKGROUP);
      }
    }
    STAMP(5);
#pragma unroll
    for (int k = 0; k < 8; ++k) acc[k] = xrow_sum(acc[k]);
    if (g == 0) {
      LAS float* mr = (LAS float*)(L + L_MRG) + w * 132;
      *(LAS f4*)(mr + 4 + 8 * i16) = f4{acc[0], acc[1], acc[2], acc[3]};
      *(LAS f4*)(mr + 8 + 8 * i16) = f4{acc[4], acc[5], acc[6], acc[7]};
      if (i16 == 0) { mr[0] = m; mr[1] = l; }
    }
  } else {
    __builtin_amdgcn_s_setprio(3);
    if (lane == 0) { flags[0] = 0u; flags[1] = 0u; flags[2] = 0u; flags[3] = 0u; }
    if (ABL_CTRL) {
      bar();
      while ((unsigned)__builtin_amdgcn_s_memrealtime() - tstart < 800) __builtin_amdgcn_s_sleep(8);
      if (lane == 0) flags[1] = 1u;
    } else {
    const unsigned E = __builtin_amdgcn_raw_buffer_load_b32(rsws, WS_EPOCH, 0, 16);
    unsigned warm = 0;
    {
      const int n = sl * 64 + lane, pi = n / 640, row = n - 640 * pi, pr = xcd + 8 * pi;
      if (WARM && pr < 20) warm = __builtin_amdgcn_raw_buffer_load_b32(mkrs(p.wmod, DM * 6 * DM * 2), (unsigned)(row * 7680 + pr * 128), 0, 2);
    }
    const GAS v4u* xp = (const GAS v4u*)p.x;
    v4u xr[10];
    const int sj = lane >> 3, sub = lane & 7;
#pragma unroll
    for (int u = 0; u < 10; ++u) xr[u] = xp[sj * 80 + sub + 8 * u];
    SCHED();
    bar();
    unsigned xh[5];
#pragma unroll
    for (int u = 0; u < 5; ++u) { const int idx = u * 64 + lane, j = idx / 40, m = idx - 40 * j; xh[u] = ((const GAS unsigned*)p.x)[j * 320 + 40 * b + m]; }
    if (!ABL_SENT) {
      const v4u sv = {SENT, SENT, SENT, SENT};
      if (lane < 16) st_sc(sv, rsws, WS_OX + (unsigned)(h * 8 + b) * 256 + lane * 16);
      if (lane < 5) st_sc2(v2u{SENT, SENT}, rsws, WS_X1B + (unsigned)(b * 320 + 10 * h) * 4 + lane * 8);
    }
    STAMP(1);
    {
      float sm = 0.f;
#pragma unroll
      for (int u = 0; u < 10; ++u)
        sm += ((blo(xr[u].x) + bhi(xr[u].x)) + (blo(xr[u].y) + bhi(xr[u].y))) + ((blo(xr[u].z) + bhi(xr[u].z)) + (blo(xr[u].w) + bhi(xr[u].w)));
      const float mean = oct_sum(sm) * (1.f / DM);
      float sq = 0.f;
#pragma unroll
      for (int u = 0; u < 10; ++u) {
#pragma unroll
        for (int k = 0; k < 4; ++k) { const float a = blo(xr[u][k]) - mean, c2 = bhi(xr[u][k]) - mean; sq = fmaf(a, a, sq); sq = fmaf(c2, c2, sq); }
      }
      const float var = oct_sum(sq) * (1.f / DM);
      if (sub == 0) {
        LAS float* st = (LAS float*)(L + L_STAT) + sj * 2;
        st[0] = rbf(mean);
        st[1] = rcp(rbf(__builtin_amdgcn_sqrtf(rbf(rbf(var) + 1e-5f))));
      }
    }
    VMWAIT(0);
    asm volatile("" :: "v"(warm));
    const unsigned T = rfl(E) + 1u;
    const unsigned t10 = T & 0x3FFu;
    if (lane == 0) flags[4] = T;
    if (lane == 0) st_sc2(v2u{T, ~T}, rsws, WS_INIT + (unsigned)(b * 32 + h) * 8);
    STAMP2(9);
    if (has_mod) {
      unsigned spins = 0;
      while (flags[0] < (unsigned)NSW) { __builtin_amdgcn_s_sleep(1); if (++spins > SPIN_L) break; }
      CFENCE();
      STAMP(2);
      f4 a0 = {0, 0, 0, 0}, a1 = {0, 0, 0, 0};
      const LAS unsigned char* ap = L + L_RT + (8 * g + tq) * 64 + tp * 8;
      const LAS unsigned char* bp = L + L_SC + (lane & 7) * 1296 + g * 16;
#pragma unroll 2
      for (int s = 0; s < 20; ++s) {
        const v4u bb = *(const LAS v4u*)(bp + s * 64);
        const v2u x0 = trread(ap + s * 2048), x1 = trread(ap + s * 2048 + 256);
        const v2u y0 = trread(ap + s * 2048 + 32), y1 = trread(ap + s * 2048 + 256 + 32);
        a0 = mfma32(v4u{x0.x, x0.y, x1.x, x1.y}, bb, a0);
        a1 = mfma32(v4u{y0.x, y0.y, y1.x, y1.y}, bb, a1);
      }
      if (i16 < 8) {
        const unsigned o = WS_MODG + (unsigned)(i16 * 1920 + (mcol0 + 4 * g) / 2) * 8;
        st_sc(v4u{pk2(a0[0], a0[1]), T, pk2(a0[2], a0[3]), T}, rsws, o);
        st_sc(v4u{pk2(a1[0], a1[1]), T, pk2(a1[2], a1[3]), T}, rsws, o + 64);
      }
    }
    STAMP(3);
    {
      v2u sa[5], ca[5], fl[4];
      if (D1) { while ((unsigned)__builtin_amdgcn_s_memrealtime() - tstart < D1) __builtin_amdgcn_s_sleep(2); }
      STAMP2(10);
      if (ABL_G1) { for (int u = 0; u < 5; ++u) { sa[u] = v2u{0u, T}; ca[u] = v2u{0u, T}; } }
      for (unsigned spins = 0; !ABL_G1; ++spins) {
        CFENCE();
        bool ok = true;
#pragma unroll
        for (int u = 0; u < 5; ++u) {
          const int idx = u * 64 + lane, j = idx / 40, m = idx - 40 * j;
          const unsigned o = WS_MODG + (unsigned)(j * 1920 + 40 * b + m) * 8;
          sa[u] = ld_sc2(rsws, o);
          ca[u] = ld_sc2(rsws, o + 320 * 8);
        }
#pragma unroll
        for (int u = 0; u < 4; ++u) fl[u] = ld_sc2(rsws, WS_INIT + (unsigned)(u * 64 + lane) * 8);
#pragma unroll
        for (int u = 0; u < 5; ++u) ok = ok && sa[u].y == T && ca[u].y == T;
#pragma unroll
        for (int u = 0; u < 4; ++u) ok = ok && fl[u].x == T && fl[u].y == ~T;
        if (__all(ok) || spins > SPIN_G) break;
        __builtin_amdgcn_s_sleep(PSLEEP);
      }
      STAMP2(11);
      asm volatile("s_waitcnt lgkmcnt(0)" ::: "memory");
#pragma unroll
      for (int u = 0; u < 5; ++u) {
        const int idx = u * 64 + lane, j = idx / 40, m = idx - 40 * j;
        const float mean = ((LAS float*)(L + L_STAT))[2 * j], den = ((LAS float*)(L + L_STAT))[2 * j + 1];
        const float n0 = rbf(rbf(blo(xh[u]) - mean) * den), n1 = rbf(rbf(bhi(xh[u]) - mean) * den);
        const float h0 = rbf(n0 * rbf(1.f + blo(ca[u].x))) + blo(sa[u].x), h1 = rbf(n1 * rbf(1.f + bhi(ca[u].x))) + bhi(sa[u].x);
        *(LAS unsigned*)(L + L_HS + j * 176 + m * 4) = pk2(h0, h1);
      }
    }
    STAMP(4);
    {
      unsigned spins = 0;
      while (flags[2] < (unsigned)NSW) { __builtin_amdgcn_s_sleep(1); if (++spins > SPIN_L) break; }
      CFENCE();
    }
    STAMP2(12);
    {
      f4 qa[8];
#pragma unroll
      for (int t = 0; t < 8; ++t) qa[t] = f4{0, 0, 0, 0};
#pragma unroll
      for (int s = 0; s < 5; ++s) {
        const v2u bb = *(const LAS v2u*)(L + L_HS + (lane & 7) * 176 + s * 32 + g * 8);
        const int row = 16 * s + 4 * g + tq;
        const LAS unsigned char* rp = L + L_QT + row * 256 + (tp & 1) * 8;
        const int sw = 2 * (row & 7);
#pragma unroll
        for (int t = 0; t < 8; ++t) qa[t] = mfma16(trread(rp + (((2 * t + (tp >> 1)) ^ sw) * 16)), bb, qa[t]);
      }
      if (i16 < 8 && !ABL_G2) {
        const unsigned o = WS_QP + (unsigned)(((h * 8 + b) * 8 + i16) * 128 + 4 * g) * 4;
#pragma unroll
        for (int t = 0; t < 8; ++t) st_sc(tg4(qa[t], t10), rsws, o + t * 64);
      }
    }
    STAMP(5);
    const unsigned tpub = (unsigned)__builtin_amdgcn_s_memrealtime();
    {
      v4u qv[4];
      if (D2) { while ((unsigned)__builtin_amdgcn_s_memrealtime() - tpub < D2) __builtin_amdgcn_s_sleep(2); }
      STAMP2(13);
      if (ABL_G2) { for (int u = 0; u < 4; ++u) qv[u] = v4u{t10, t10, t10, t10}; }
      for (unsigned spins = 0; !ABL_G2; ++spins) {
        CFENCE();
        bool ok = true;
#pragma unroll
        for (int u = 0; u < 4; ++u) qv[u] = ld_sc(rsws, WS_QP + (unsigned)(((h * 8 + 2 * u + (lane >> 5)) * 8 + b) * 128 + 4 * (lane & 31)) * 4);
#pragma unroll
        for (int u = 0; u < 4; ++u) ok = ok && tagged4(qv[u], t10);
        if (__all(ok) || spins > SPIN_G) break;
        __builtin_amdgcn_s_sleep(PSLEEP);
      }
      f4 qs;
#pragma unroll
      for (int k = 0; k < 4; ++k) qs[k] = x32_sum((untg(qv[0][k]) + untg(qv[1][k])) + (untg(qv[2][k]) + untg(qv[3][k])));
      if (lane < 32) *(LAS v2u*)(L + L_Q + lane * 8) = v2u{pk2(qs[0], qs[1]), pk2(qs[2], qs[3])};
      asm volatile("s_waitcnt lgkmcnt(0)" ::: "memory");
      if (lane == 0) flags[1] = 1u;
    }
    STAMP(6);
    if (!ABL_G3) {
      const unsigned go = WS_MODG + (unsigned)(b * 1920 + (lane < 10 ? 640 + 10 * h + lane : 1600 + 10 * h + (lane < 20 ? lane - 10 : 0))) * 8;
      const unsigned xv = ((const GAS unsigned*)p.x)[b * 320 + 10 * h + (lane < 10 ? lane : 0)];
      v2u gv;
      for (unsigned spins = 0;; ++spins) {
        CFENCE();
        gv = ld_sc2(rsws, go);
        if (__all(gv.y == T) || spins > SPIN_G) break;
        __builtin_amdgcn_s_sleep(32);
      }
      if (lane < 20) *(LAS unsigned*)(L + L_VEC + lane * 4) = gv.x;
      if (lane < 10) *(LAS unsigned*)(L + L_VEC + 80 + lane * 4) = xv;
      if (is_mlp) {
        for (int j = 0; j < 8; ++j) {
          v4u cv[5];
          for (unsigned spins = 0;; ++spins) {
            CFENCE();
            bool ok = true;
#pragma unroll
            for (int u = 0; u < 5; ++u) cv[u] = ld_sc(rsws, WS_MODG + (unsigned)(j * 1920 + 960) * 8 + (unsigned)(u * 64 + lane) * 16);
#pragma unroll
            for (int u = 0; u < 5; ++u) ok = ok && cv[u].y == T && cv[u].w == T;
            if (__all(ok) || spins > SPIN_G) break;
            __builtin_amdgcn_s_sleep(32);
          }
#pragma unroll
          for (int u = 0; u < 5; ++u) {
            const int pc = u * 64 + lane;
            const unsigned o = (pc < 160 ? 10240u + (unsigned)(j * 320 + 2 * pc) * 4 : (unsigned)(j * 320 + 2 * (pc - 160)) * 4);
            const v2u sc1 = {pk2(1.f + blo(cv[u].x), 1.f + bhi(cv[u].x)), pk2(1.f + blo(cv[u].z), 1.f + bhi(cv[u].z))};
            *(LAS v2u*)(L + L_CS + o) = pc < 160 ? v2u{cv[u].x, cv[u].z} : sc1;
          }
        }
      }
    }
    STAMP(7);
    {
      unsigned spins = 0;
      while (flags[3] < (unsigned)NSW) { __builtin_amdgcn_s_sleep(1); if (++spins > SPIN_L) break; }
      CFENCE();
    }
    {
      const rsrc_t rswo = mkrs(p.wo + (size_t)h * DH * DM, DH * DM * 2);
      for (int n = 0; n < 20; ++n) {
        const int ci = n * 64 + lane, row = ci / 10, ch = ci - 10 * row;
        dma16(rswo, lds0 + L_T1 + n * 1024, (unsigned)(row * 1280 + b * 160 + ch * 16), 0);
      }
    }
    }
  }

  bar();
  STAMP(8);
  if (ABL_CTRL || ABL_TAIL) {
    STAMP(15);
    return;
  }
  const unsigned t10 = flags[4] & 0x3FFu;
  auto until = [&](unsigned t0, unsigned ticks) { while ((unsigned)__builtin_amdgcn_s_memrealtime() - t0 < ticks) __builtin_amdgcn_s_sleep(1); };
  if (w == 0) {
    if (lane < 32) {
      const LAS float* mr = (const LAS float*)(L + L_MRG);
      float M = mr[0];
#pragma unroll
      for (int u = 1; u < NSW; ++u) M = fmaxf(M, mr[u * 132]);
      constexpr float C2 = 0.08838834764831845f * 1.4426950408889634f;
      float Lsum = 0.f;
      f4 o = {0, 0, 0, 0};
#pragma unroll
      for (int u = 0; u < NSW; ++u) {
        const float f = __builtin_amdgcn_exp2f((mr[u * 132] - M) * C2);
        Lsum = fmaf(f, mr[u * 132 + 1], Lsum);
        o += f * *(const LAS f4*)(mr + u * 132 + 4 + 4 * lane);
      }
      const float inv = rcp(Lsum);
      st_sc2(v2u{canonu(pk2(o[0] * inv, o[1] * inv)), canonu(pk2(o[2] * inv, o[3] * inv))}, rsws, WS_OX + (unsigned)(h * 8 + b) * 256 + lane * 8);
    }
  } else if (w < 3) {
    const int t = tid - 64;
    __builtin_amdgcn_s_sleep(TD_A);
    v4u v;
    for (unsigned spins = 0;; ++spins) {
      CFENCE();
      v = ld_sc(rsws, WS_OX + (unsigned)h * 2048 + t * 16);
      if (__all(valid4(v)) || spins > SPIN_G) break;
      __builtin_amdgcn_s_sleep(TS);
    }
    *(LAS v4u*)(L + L_OS + (t >> 4) * 272 + (t & 15) * 16) = v;
  } else if (w == 7) {
    VMWAIT(0);
  }
  bar();
  const unsigned tB2 = (unsigned)__builtin_amdgcn_s_memrealtime();
  STAMP(9);
  if (w < 4) {
    const LAS unsigned char* bp = L + L_OS + (lane & 7) * 272 + g * 16;
    v4u bb[4];
#pragma unroll
    for (int s = 0; s < 4; ++s) bb[s] = *(const LAS v4u*)(bp + s * 64);
    for (int t = w; t < 5; t += 4) {
      f4 y = {0, 0, 0, 0};
      const LAS unsigned char* ap = L + L_T1 + (8 * g + tq) * 160 + (16 * t + 4 * tp) * 2;
#pragma unroll
      for (int s = 0; s < 4; ++s) {
        const v2u x0 = trread(ap + s * 32 * 160), x1 = trread(ap + s * 32 * 160 + 4 * 160);
        y = mfma32(v4u{x0.x, x0.y, x1.x, x1.y}, bb[s], y);
      }
      const int cc = 4 * t + g;
      if (i16 < 8) st_sc(tg4(y, t10), rsws,
                         WS_T1P + (unsigned)((((i16 * 32 + 4 * b + cc / 5) * 32 + h) * 5 + cc % 5) * 16));
    }
  } else if (w < 7) {
    const int t = tid - 256;
    const bool act = t < 160;
    const unsigned o = WS_T1P + (unsigned)((b * 32 + h) * 160 + (act ? t : 0)) * 16;
    until(tB2, GB);
    v4u v;
    for (unsigned spins = 0;; ++spins) {
      CFENCE();
      v = ld_sc(rsws, o);
      if (__all(tagged4(v, t10)) || spins > SPIN_G) break;
      __builtin_amdgcn_s_sleep(TS);
    }
    if (act) *(LAS f4*)(L + L_T4P + t * 16) = f4{untg(v.x), untg(v.y), untg(v.z), untg(v.w)};
  }
  bar();
  if (w == 4) {
    const int e = lane >> 3, c = lane & 7;
    f4 sm = {0, 0, 0, 0};
    if (c < 5) {
#pragma unroll
      for (int u = 0; u < 4; ++u) sm += *(const LAS f4*)(L + L_T4P + ((4 * e + u) * 5 + c) * 16);
    }
#pragma unroll
    for (int k = 0; k < 4; ++k) { float x = sm[k]; x += dpp<0x128>(x); sm[k] = xrow_sum(x); }
    if (lane < 5) {
      const LAS unsigned* vec = (const LAS unsigned*)(L + L_VEC);
      const unsigned g0 = vec[2 * lane], g1 = vec[2 * lane + 1], x0 = vec[20 + 2 * lane], x1 = vec[20 + 2 * lane + 1];
      const float r0 = rbf(blo(x0) + rbf(blo(g0) * rbf(sm[0]))), r1 = rbf(bhi(x0) + rbf(bhi(g0) * rbf(sm[1])));
      const float r2 = rbf(blo(x1) + rbf(blo(g1) * rbf(sm[2]))), r3 = rbf(bhi(x1) + rbf(bhi(g1) * rbf(sm[3])));
      st_sc2(v2u{canonu(pk2(r0, r1)), canonu(pk2(r2, r3))}, rsws, WS_X1B + (unsigned)(b * 320 + 10 * h + 2 * lane) * 4);
      *(LAS f4*)(L + L_X1O + lane * 16) = f4{r0, r1, r2, r3};
    }
  }
  bar();
  const unsigned tB = (unsigned)__builtin_amdgcn_s_memrealtime();
  STAMP(10);
  if (is_mlp) {
    {
      const unsigned o = WS_X1B + (unsigned)w * 1280 + lane * 4;
      unsigned cs[5], sh[5];
#pragma unroll
      for (int u = 0; u < 5; ++u) {
        cs[u] = *(const LAS unsigned*)(L + L_CS + (w * 320 + u * 64 + lane) * 4);
        sh[u] = *(const LAS unsigned*)(L + L_CS + 10240 + (w * 320 + u * 64 + lane) * 4);
      }
      until(tB, GC);
      unsigned xv[5];
      for (unsigned spins = 0;; ++spins) {
        CFENCE();
        bool ok = true;
#pragma unroll
        for (int u = 0; u < 5; ++u) xv[u] = __builtin_amdgcn_raw_buffer_load_b32(rsws, o + u * 256, 0, 16);
#pragma unroll
        for (int u = 0; u < 5; ++u) ok = ok && xv[u] != SENT;
        if (__all(ok) || spins > SPIN_G) break;
        __builtin_amdgcn_s_sleep(TS);
      }
      const float x00 = blo(rfl(xv[0]));
      float s1 = 0.f, s2 = 0.f;
#pragma unroll
      for (int u = 0; u < 5; ++u) {
        const float d0 = blo(xv[u]) - x00, d1 = bhi(xv[u]) - x00;
        s1 += d0 + d1; s2 = fmaf(d0, d0, fmaf(d1, d1, s2));
      }
      s1 = row16_sum(s1); s2 = row16_sum(s2);
      s1 = xrow_sum(s1) * (1.f / DM); s2 = xrow_sum(s2) * (1.f / DM);
      const float rs = __builtin_amdgcn_rsqf(fmaxf(s2 - s1 * s1, 0.f) + 1e-5f), mr = -(x00 + s1) * rs;
#pragma unroll
      for (int u = 0; u < 5; ++u) {
        const float n0 = fmaf(blo(xv[u]), rs, mr), n1 = fmaf(bhi(xv[u]), rs, mr);
        *(LAS unsigned*)(L + L_H2 + w * 1296 + (u * 64 + lane) * 4) = pk2(fmaf(n0, blo(cs[u]), blo(sh[u])), fmaf(n1, bhi(cs[u]), bhi(sh[u])));
      }
    }
    bar();
    STAMP(11);
    {
      const int t = w & 1, kq = w >> 1;
      f4 a = {0, 0, 0, 0};
      const LAS unsigned char* ap = L + L_RT + (160 * kq + 8 * g + tq) * 64 + t * 32 + tp * 8;
      const LAS unsigned char* bp = L + L_H2 + (lane & 7) * 1296 + (160 * kq + 8 * g) * 2;
#pragma unroll
      for (int s = 0; s < 5; ++s) {
        const v4u bb = *(const LAS v4u*)(bp + s * 64);
        const v2u x0 = trread(ap + s * 2048), x1 = trread(ap + s * 2048 + 256);
        a = mfma32(v4u{x0.x, x0.y, x1.x, x1.y}, bb, a);
      }
      if (i16 < 8) *(LAS f4*)(L + L_T4P + (((kq * 2 + t) * 4 + g) * 8 + i16) * 16) = a;
    }
    bar();
    if (tid < 128) {
      const int j = tid >> 4, c = 2 * (tid & 15), t = c >> 4, gg = (c & 15) >> 2, r = c & 3;
      float z0 = 0.f, z1 = 0.f;
#pragma unroll
      for (int kq = 0; kq < 4; ++kq) {
        const LAS float* z = (const LAS float*)(L + L_T4P + (((kq * 2 + t) * 4 + gg) * 8 + j) * 16) + r;
        z0 += z[0]; z1 += z[1];
      }
      *(LAS unsigned*)(L + L_H1 + j * 80 + c * 2) = pk2(silu(rbf(z0)), silu(rbf(z1)));
    }
    bar();
    STAMP(12);
    {
      const v4u bb = *(const LAS v4u*)(L + L_H1 + (lane & 7) * 80 + g * 16);
      const int r0 = 8 * g + tq;
      const LAS unsigned char* ap = L + L_W2 + r0 * 1280 + (tp & 1) * 8;
      const int f = 2 * (r0 & 3);
#pragma unroll
      for (int tt = 0; tt < 5; ++tt) {
        const int ch = (2 * (5 * w + tt) + (tp >> 1)) ^ f;
        const v2u x0 = trread(ap + ch * 16), x1 = trread(ap + 4 * 1280 + ch * 16);
        const f4 y = mfma32(v4u{x0.x, x0.y, x1.x, x1.y}, bb, f4{0, 0, 0, 0});
        if (i16 < 8) *(LAS v4u*)(L + L_RT + i16 * 2576 + (16 * (5 * w + tt) + 4 * g) * 4) = tg4(y, t10);
      }
    }
    bar();
    {
#pragma unroll
      for (int u = 0; u < 3; ++u) {
        const int pc = u * 512 + tid, j = pc / 160, c = pc - 160 * j;
        if (pc < 1280) st_sc(*(const LAS v4u*)(L + L_RT + j * 2576 + c * 16), rsws, WS_T5P + (unsigned)mm * 20480 + (unsigned)pc * 16);
      }
    }
  }
  const unsigned tD = (unsigned)__builtin_amdgcn_s_memrealtime();
  STAMP(13);
  if (w < 5) {
    const bool act = tid < NMLP * 5;
    const int dm = act ? tid / 5 : 0, dc = act ? tid - 5 * dm : 0;
    const unsigned o = WS_T5P + (unsigned)((dm * 8 + b) * 640 + 20 * h + 4 * dc) * 4;
    if (is_mlp) until(tD, GD1); else until(tB, GD2);
    v4u v;
    for (unsigned spins = 0;; ++spins) {
      CFENCE();
      v = ld_sc(rsws, o);
      if (__all(tagged4(v, t10)) || spins > SPIN_G) break;
      __builtin_amdgcn_s_sleep(TS);
    }
    if (act) *(LAS f4*)(L + L_W2 + tid * 16) = f4{untg(v.x), untg(v.y), untg(v.z), untg(v.w)};
  }
  bar();
  if (w == 0) {
    const int e = lane / 5, c = lane - 5 * e;
    f4 sm = {0, 0, 0, 0};
    if (lane < 45) {
#pragma unroll
      for (int u = 0; u < 6; ++u) sm += *(const LAS f4*)(L + L_W2 + ((6 * e + u) * 5 + c) * 16);
      *(LAS f4*)(L + L_W2 + 8192 + lane * 16) = sm;
    }
    asm volatile("s_waitcnt lgkmcnt(0)" ::: "memory");
    if (lane < 5) {
      sm = f4{0, 0, 0, 0};
#pragma unroll
      for (int u = 0; u < 9; ++u) sm += *(const LAS f4*)(L + L_W2 + 8192 + (u * 5 + lane) * 16);
      const LAS unsigned* vec = (const LAS unsigned*)(L + L_VEC);
      const unsigned g0 = vec[10 + 2 * lane], g1 = vec[10 + 2 * lane + 1];
      const f4 x1 = *(const LAS f4*)(L + L_X1O + lane * 16);
      const float r0 = x1[0] + rbf(blo(g0) * rbf(sm[0])), r1 = x1[1] + rbf(bhi(g0) * rbf(sm[1]));
      const float r2 = x1[2] + rbf(blo(g1) * rbf(sm[2])), r3 = x1[3] + rbf(bhi(g1) * rbf(sm[3]));
      *(GAS v2u*)(p.out + b * DM + 20 * h + 4 * lane) = v2u{pk2(r0, r1), pk2(r2, r3)};
    }
  }
  if (id == 0 && tid == 448) {
    const unsigned E = __builtin_amdgcn_raw_buffer_load_b32(rsws, WS_EPOCH, 0, 16);
    __builtin_amdgcn_raw_buffer_store_b32(E + 1u, rsws, WS_EPOCH, 0, 16);
  }
  STAMP(15);
}

}

extern "C" void kernel_launch(void* const* d_in, const int* in_sizes, int n_in, void* d_out, int out_size, void* d_ws,
                              size_t ws_size, hipStream_t stream) {
  Params p;
  p.x = (const u16*)d_in[0];
  p.kc = (const u16*)d_in[1];
  p.vc = (const u16*)d_in[2];
  p.wq = (const u16*)d_in[3];
  p.wo = (const u16*)d_in[4];
  p.w1 = (const u16*)d_in[6];
  p.w2 = (const u16*)d_in[7];
  p.c = (const u16*)d_in[8];
  p.wmod = (const u16*)d_in[9];
  p.out = (u16*)d_out;
  p.ws = (unsigned char*)d_ws;
  hipLaunchKernelGGL(fused_block, dim3(256), dim3(512), 0, stream, p);
}
```

```cpp
#define DIAG 1
#define DIAG_PRINT 1
#include <stdio.h>
#include <hip/hip_runtime.h>
#include <stdint.h>

namespace {

typedef unsigned short u16;
typedef unsigned v4u __attribute__((ext_vector_type(4)));
typedef unsigned v2u __attribute__((ext_vector_type(2)));
typedef float f4 __attribute__((ext_vector_type(4)));
typedef short s4 __attribute__((ext_vector_type(4)));
typedef short s8 __attribute__((ext_vector_type(8)));
typedef __bf16 bf2 __attribute__((ext_vector_type(2)));
typedef __bf16 bf8 __attribute__((ext_vector_type(8)));
typedef __amdgpu_buffer_rsrc_t rsrc_t;
#define LAS __attribute__((address_space(3)))
#define GAS __attribute__((address_space(1)))
#define DI __device__ __forceinline__

constexpr int NB = 8, DM = 640, NH = 32, DH = 128, SK = 4096, WIN = 1024, FF = 1728;
constexpr int NSW = 7;
constexpr int NL = 37;
#define TD_A 8
#define TD_B 8
#define TD_C 16
#define TD_D 8
#define TD_D2 4
#define TS 6
#define ABL_CTRL 0
#define ABL_SLOT 0
#define ABL_ROLE 0
#define ABL_SENT 0
#define LEAD 3
#define MP0 12
#define MPACE 11
#define DELTA 0
#define HEAVY 1
#define ABL_G1 0
#define ABL_G2 0
#define ABL_G3 0
#define ABL_TAIL 0
#define GB 40
#define GC 40
#define GD1 40
#define GD2 190
#define WARM 1
#define INDIC 1
#define ABL_WO 0
#define ABL_W12 0
#define D1 600
#define D2 100
#define PSLEEP 32
#define DPACE 15
#define PACE_ 3
constexpr int PACE = PACE_;
#define NPRE_ 16
constexpr int NPRE = NPRE_;

constexpr int NMLP = 54;
constexpr unsigned WS_EPOCH = 0;
constexpr unsigned WS_INIT = 4096;
constexpr unsigned WS_MODG = 8192;
constexpr unsigned WS_QP = WS_MODG + NB * 1920 * 8;
constexpr unsigned WS_OX = WS_QP + NH * 8 * 8 * 128 * 4;
constexpr unsigned WS_T1P = WS_OX + NH * 8 * 256;
constexpr unsigned WS_X1B = WS_T1P + NB * NH * DM * 4;
constexpr unsigned WS_T5P = WS_X1B + NB * 320 * 4;
constexpr unsigned WS_END = WS_T5P + NMLP * NB * DM * 4;
constexpr unsigned WS_DIAG = 4u << 20;
constexpr unsigned SENT = 0xFFFFFFFFu;
static_assert(WS_END <= WS_DIAG, "workspace map");

constexpr unsigned L_RT = 0;
constexpr unsigned L_QT = 40960;
constexpr unsigned L_W2 = 61440;
constexpr unsigned L_T1 = 102400;
constexpr unsigned L_CS = 122880;
constexpr unsigned L_MISC = 143360;
constexpr unsigned L_FLAG = L_MISC;
constexpr unsigned L_Q = L_MISC + 64;
constexpr unsigned L_STAT = L_Q + 256;
constexpr unsigned L_VEC = L_STAT + 64;
constexpr unsigned L_X1O = L_VEC + 128;
constexpr unsigned L_SC = L_X1O + 128;
constexpr unsigned L_H2 = L_SC;
constexpr unsigned L_HS = L_SC + 8 * 1296;
constexpr unsigned L_MRG = L_HS + 8 * 176;
constexpr unsigned L_OS = L_MRG + 7 * 132 * 4;
constexpr unsigned L_T4P = L_MRG;
constexpr unsigned L_H1 = L_T4P + 4096;
constexpr unsigned L_END = 163840;
static_assert(L_OS + 8 * 272 <= L_END && L_H1 + 8 * 80 <= L_OS + 8 * 272, "LDS map");
static_assert((L_SC % 16) == 0 && (L_HS % 16) == 0 && (L_MRG % 16) == 0 && (L_OS % 16) == 0 && (L_H1 % 16) == 0, "LDS alignment");

constexpr unsigned SPIN_G = 1u << 16;
constexpr unsigned SPIN_L = 1u << 22;

struct Params {
  const u16 *x, *kc, *vc, *wq, *wo, *w1, *w2, *c, *wmod;
  u16* out;
  unsigned char* ws;
};

DI unsigned rfl(unsigned x) { return __builtin_amdgcn_readfirstlane(x); }
DI float asf(unsigned u) { return __uint_as_float(u); }
DI unsigned asu(float f) { return __float_as_uint(f); }
DI unsigned pk2(float lo, float hi) { bf2 v = {(__bf16)lo, (__bf16)hi}; return __builtin_bit_cast(unsigned, v); }
DI float rbf(float f) { return asf(pk2(f, 0.f) << 16); }
DI float blo(unsigned u) { return asf(u << 16); }
DI float bhi(unsigned u) { return asf(u & 0xffff0000u); }
DI float dot2(unsigned a, unsigned b, float c) {
  return __builtin_amdgcn_fdot2_f32_bf16(__builtin_bit_cast(bf2, a), __builtin_bit_cast(bf2, b), c, false);
}
template <int CTRL> DI float dpp(float x) {
  return __builtin_bit_cast(float, __builtin_amdgcn_mov_dpp(__builtin_bit_cast(int, x), CTRL, 0xf, 0xf, true));
}
DI float row16_sum(float x) { x += dpp<0x128>(x); x += dpp<0x124>(x); x += dpp<0x122>(x); x += dpp<0x121>(x); return x; }
DI float oct_sum(float x) { x += dpp<0xB1>(x); x += dpp<0x4E>(x); x += dpp<0x141>(x); return x; }
DI float x16_sum(float x) {
  auto s = __builtin_amdgcn_permlane16_swap(asu(x), asu(x), false, false);
  return asf(s[0]) + asf(s[1]);
}
DI float x32_sum(float x) {
  auto t = __builtin_amdgcn_permlane32_swap(asu(x), asu(x), false, false);
  return asf(t[0]) + asf(t[1]);
}
DI float xrow_sum(float x) { return x32_sum(x16_sum(x)); }
DI float xrow_max(float x) {
  auto s = __builtin_amdgcn_permlane16_swap(asu(x), asu(x), false, false);
  x = fmaxf(asf(s[0]), asf(s[1]));
  auto t = __builtin_amdgcn_permlane32_swap(asu(x), asu(x), false, false);
  return fmaxf(asf(t[0]), asf(t[1]));
}
DI float wave_sum(float x) { return xrow_sum(row16_sum(x)); }
DI f4 mfma32(v4u a, v4u b, f4 c) {
  return __builtin_amdgcn_mfma_f32_16x16x32_bf16(__builtin_bit_cast(bf8, a), __builtin_bit_cast(bf8, b), c, 0, 0, 0);
}
DI f4 mfma16(v2u a, v2u b, f4 c) {
  return __builtin_amdgcn_mfma_f32_16x16x16bf16_1k(__builtin_bit_cast(s4, a), __builtin_bit_cast(s4, b), c, 0, 0, 0);
}
DI v2u trread(const LAS unsigned char* p) {
  return __builtin_bit_cast(v2u, __builtin_amdgcn_ds_read_tr16_b64_v4i16((LAS s4*)p));
}
DI rsrc_t mkrs(const void* p, unsigned bytes) { return __builtin_amdgcn_make_buffer_rsrc((void*)p, 0, (int)bytes, 0x00020000); }
DI v4u ld_nt(rsrc_t r, unsigned vo, unsigned so) { return __builtin_amdgcn_raw_buffer_load_b128(r, vo, so, 2); }
#define KAUX 2
#define VAUX 2
DI v4u ld_k(rsrc_t r, unsigned vo, unsigned so) { return __builtin_amdgcn_raw_buffer_load_b128(r, vo, so, KAUX); }
DI v4u ld_v(rsrc_t r, unsigned vo, unsigned so) { return __builtin_amdgcn_raw_buffer_load_b128(r, vo, so, VAUX); }
DI v4u ld_sc(rsrc_t r, unsigned vo) { return __builtin_amdgcn_raw_buffer_load_b128(r, vo, 0, 16); }
DI v2u ld_sc2(rsrc_t r, unsigned vo) { return __builtin_amdgcn_raw_buffer_load_b64(r, vo, 0, 16); }
DI void st_sc(v4u v, rsrc_t r, unsigned vo) { __builtin_amdgcn_raw_buffer_store_b128(v, r, vo, 0, 16); }
DI void st_sc2(v2u v, rsrc_t r, unsigned vo) { __builtin_amdgcn_raw_buffer_store_b64(v, r, vo, 0, 16); }
DI unsigned canonu(unsigned u) { return u == 0xFFFFFFFFu ? 0x7FFF7FFFu : u; }
DI void dma16(rsrc_t r, unsigned lds, unsigned vo, unsigned so) {
  unsigned keep;
  asm volatile("s_nop 4\n\ts_mov_b32 %0, m0\n\ts_mov_b32 m0, %1\n\ts_nop 0\n\tbuffer_load_dwordx4 %2, %3, %4 offen nt lds\n\ts_mov_b32 m0, %0"
               : "=&s"(keep) : "s"(lds), "v"(vo), "s"(r), "s"(so) : "memory");
}
template <int N> DI void vmwait() { asm volatile("s_waitcnt vmcnt(%0)" :: "n"(N) : "memory"); }
#define VMWAIT(n) vmwait<(n)>()
DI void bar() { asm volatile("s_waitcnt lgkmcnt(0)\n\ts_barrier" ::: "memory"); }
#define CFENCE() asm volatile("" ::: "memory")
#define SCHED() __builtin_amdgcn_sched_barrier(0)
DI unsigned canon(float f) { unsigned u = asu(f); return u == SENT ? 0x7FFFFFFFu : u; }
DI unsigned tg(float f, unsigned t10) { return ((asu(f) + 0x200u) & ~0x3FFu) | t10; }
DI v4u tg4(f4 v, unsigned t10) { return v4u{tg(v[0], t10), tg(v[1], t10), tg(v[2], t10), tg(v[3], t10)}; }
DI bool tagged4(v4u v, unsigned t10) { return (((v.x ^ t10) | (v.y ^ t10) | (v.z ^ t10) | (v.w ^ t10)) & 0x3FFu) == 0u; }
DI float untg(unsigned u) { return asf(u & ~0x3FFu); }
DI bool valid4(v4u v) { return v.x != SENT && v.y != SENT && v.z != SENT && v.w != SENT; }
DI float rcp(float x) { return __builtin_amdgcn_rcpf(x); }
DI float silu(float v) { return v * rcp(1.f + __builtin_amdgcn_exp2f(v * -1.4426950408889634f)); }

#define STAMP(k) do { if (DIAG == 1 || (k) <= 7 || (k) == 15) ts[k] = (unsigned)__builtin_amdgcn_s_memrealtime(); } while (0)
#define STAMP2(k) do { if (DIAG == 2) ts[k] = (unsigned)__builtin_amdgcn_s_memrealtime(); } while (0)

__global__ void __launch_bounds__(512, 2) fused_block(Params p) {
  __shared__ __attribute__((aligned(16))) unsigned char smem[L_END];
  LAS unsigned char* L = (LAS unsigned char*)smem;
  const unsigned lds0 = (unsigned)(uintptr_t)L;
  const int tid = threadIdx.x, lane = tid & 63;
  const int w = (int)rfl(tid >> 6);
  const int id = blockIdx.x, xcd = id & 7, sl = id >> 3;
  const int h = xcd * 4 + (sl >> 3), b = sl & 7;
  const int g = lane >> 4, i16 = lane & 15;
  const int tq = i16 >> 2, tp = lane & 3;
  const rsrc_t rsws = mkrs(p.ws, WS_END);
  volatile LAS unsigned* flags = (volatile LAS unsigned*)(L + L_FLAG);
  const int mpr = (sl >> 1) * 8 + xcd;
  const bool has_mod = !ABL_ROLE && sl < 16 && mpr < 60;
  const bool crit = has_mod && mpr < 20;
  const int mcol0 = (2 * mpr + (sl & 1)) * 32;
  const int fpr = ((sl - 16) >> 1) * 8 + xcd;
  const bool is_mlp = !ABL_ROLE && sl >= 16 && sl < 24 && fpr < 27;
  const int mm = 2 * fpr + (sl & 1);

  unsigned ts[16];
#pragma unroll
  for (int k = 0; k < 16; ++k) ts[k] = 0;
  STAMP(0);
  const unsigned tstart = (unsigned)__builtin_amdgcn_s_memrealtime();
  auto sched = [&](int i, bool light) {
    if (DELTA && light) { while ((unsigned)__builtin_amdgcn_s_memrealtime() - tstart < (unsigned)(i * DELTA / 10)) __builtin_amdgcn_s_sleep(1); }
  };

  if (w < NSW) {
    const rsrc_t rsk = mkrs(p.kc + ((size_t)(b * NH + h) * SK + (SK - WIN)) * DH, WIN * DH * 2);
    const rsrc_t rsv = mkrs(p.vc + ((size_t)(b * NH + h) * SK + (SK - WIN)) * DH, WIN * DH * 2);
    const unsigned vo = (unsigned)(lane * 16 + w * 1024);
    v4u kr[NL], vr[NL], cr[2];
    if (has_mod && w < 5) {
      const GAS v4u* cp = (const GAS v4u*)p.c;
      cr[0] = cp[w * 128 + lane];
      cr[1] = cp[w * 128 + 64 + lane];
    }
    auto silu_c = [&]() {
      if (w < 5) {
#pragma unroll
        for (int u = 0; u < 2; ++u) {
          const int q = w * 128 + u * 64 + lane, j = q / 80, k8 = q - 80 * j;
          v4u o;
          o.x = pk2(silu(blo(cr[u].x)), silu(bhi(cr[u].x)));
          o.y = pk2(silu(blo(cr[u].y)), silu(bhi(cr[u].y)));
          o.z = pk2(silu(blo(cr[u].z)), silu(bhi(cr[u].z)));
          o.w = pk2(silu(blo(cr[u].w)), silu(bhi(cr[u].w)));
          *(LAS v4u*)(L + L_SC + j * 1296 + k8 * 16) = o;
        }
      }
    };
    const rsrc_t rsrt = mkrs(p.wmod, DM * 6 * DM * 2);
    const unsigned rt_vo = (unsigned)((lane >> 2) * 7680 + mcol0 * 2 + (lane & 3) * 16);
    auto dma_rt = [&](int m) { dma16(rsrt, lds0 + L_RT + m * 1024, rt_vo, (unsigned)m * 16u * 7680u); };
    if (!crit) {
#pragma unroll
      for (int i = 0; i < PACE; ++i) kr[i] = ld_k(rsk, vo, i * 7168);
    }
    if (has_mod) { for (int m = w; m < 40; m += NSW) dma_rt(m); }
    SCHED();
    bar();
    if (has_mod) {
      silu_c();
      VMWAIT(0);
      asm volatile("s_waitcnt lgkmcnt(0)" ::: "memory");
      if (lane == 0) __hip_atomic_fetch_add((LAS unsigned*)(L + L_FLAG), 1u, __ATOMIC_RELAXED, __HIP_MEMORY_SCOPE_WORKGROUP);
    }
    {
      const rsrc_t rsq = mkrs(p.wq, DM * NH * DH * 2);
      for (int n = w; n < 20; n += NSW) {
        const int rl = 4 * n + g;
        dma16(rsq, lds0 + L_QT + n * 1024, (unsigned)((80 * b + rl) * 8192 + h * 256 + ((i16 ^ (2 * (rl & 7))) * 16)), 0);
      }
    }
    if (crit) {
#pragma unroll
      for (int i = 0; i < PACE; ++i) kr[i] = ld_k(rsk, vo, i * 7168);
    }
    SCHED();
    const rsrc_t rsw1 = mkrs(p.w1, DM * FF * 2);
    const rsrc_t rsw2 = mkrs(p.w2 + (size_t)(is_mlp ? mm : 0) * 32 * DM, 32 * DM * 2);
    auto slot = [&](int s) {
      const int n = 7 * s + w;
      if (n < 40) {
        dma16(rsw1, lds0 + L_RT + n * 1024, (unsigned)((lane >> 2) * (FF * 2) + mm * 64 + (lane & 3) * 16), (unsigned)n * 16u * (FF * 2));
      } else if (n < 80) {
        const int ci = (n - 40) * 64 + lane, row = ci / 80, pos = ci - 80 * row;
        dma16(rsw2, lds0 + L_W2 + (n - 40) * 1024, (unsigned)(row * 1280 + (pos ^ (2 * (row & 3))) * 16), 0);
      }
    };
    const bool heavy = HEAVY == 1 ? (is_mlp || has_mod) : HEAVY == 2 ? is_mlp : false;
#pragma unroll
    for (int i = PACE; i < NL + NPRE; ++i) {
      sched(i, !is_mlp);
      if (heavy) VMWAIT(PACE); else VMWAIT(PACE - 1);
      if (i < NL) kr[i] = ld_k(rsk, vo, i * 7168); else vr[i - NL] = ld_v(rsv, vo, (i - NL) * 7168);
      SCHED();
      if (i == PACE + 4) {
        asm volatile("s_waitcnt lgkmcnt(0)" ::: "memory");
        if (lane == 0) __hip_atomic_fetch_add((LAS unsigned*)(L + L_FLAG) + 2, 1u, __ATOMIC_RELAXED, __HIP_MEMORY_SCOPE_WORKGROUP);
        STAMP(1);
      }
      if (i >= 7 && (i % 4) == 3 && (i - 7) / 4 < 12) { if (is_mlp) slot((i - 7) / 4); SCHED(); }
    }
    STAMP(2);
    {
      unsigned spins = 0;
      while (flags[1] == 0u) { __builtin_amdgcn_s_sleep(1); if (++spins > SPIN_L) break; }
    }
    CFENCE();
    STAMP(3);
    const v4u q4 = *(const LAS v4u*)(L + L_Q + i16 * 16);
    float s[NL];
#pragma unroll
    for (int i = 0; i < NL; ++i) {
      float d = dot2(kr[i].x, q4.x, 0.f);
      d = dot2(kr[i].y, q4.y, d); d = dot2(kr[i].z, q4.z, d); d = dot2(kr[i].w, q4.w, d);
      s[i] = row16_sum(d);
      if ((i & 3) == 3 && NPRE + (i >> 2) < NL) { SCHED(); sched(NL + NPRE + (i >> 2), !is_mlp); if (heavy) VMWAIT(PACE); else VMWAIT(PACE - 1); vr[NPRE + (i >> 2)] = ld_v(rsv, vo, (NPRE + (i >> 2)) * 7168); SCHED(); }
    }
    constexpr int NV1 = NPRE + (NL >> 2);
    if (w >= 4) s[NL - 1] = -INFINITY;
    float m = s[0];
#pragma unroll
    for (int i = 1; i < NL; ++i) m = fmaxf(m, s[i]);
    m = xrow_max(m);
    constexpr float C2 = 0.08838834764831845f * 1.4426950408889634f;
    const float mc = m * C2;
    unsigned pp[NL];
    float l = 0.f;
#pragma unroll
    for (int i = 0; i < NL; ++i) {
      pp[i] = pk2(0.f, __builtin_amdgcn_exp2f(fmaf(s[i], C2, -mc)));
      l += asf(pp[i]);
    }
    l = xrow_sum(l);
    STAMP(4);
    float acc[8];
#pragma unroll
    for (int k = 0; k < 8; ++k) acc[k] = 0.f;
#pragma unroll
    for (int i = 0; i < NL; ++i) {
      const unsigned p1 = pp[i], p0 = p1 >> 16;
      acc[0] = dot2(vr[i].x, p0, acc[0]); acc[1] = dot2(vr[i].x, p1, acc[1]);
      acc[2] = dot2(vr[i].y, p0, acc[2]); acc[3] = dot2(vr[i].y, p1, acc[3]);
      acc[4] = dot2(vr[i].z, p0, acc[4]); acc[5] = dot2(vr[i].z, p1, acc[5]);
      acc[6] = dot2(vr[i].w, p0, acc[6]); acc[7] = dot2(vr[i].w, p1, acc[7]);
      SCHED();
      if (NV1 + i < NL) { sched(NL + NV1 + i, !is_mlp); if (heavy) VMWAIT(PACE); else VMWAIT(PACE - 1); vr[NV1 + i] = ld_v(rsv, vo, (NV1 + i) * 7168); SCHED(); }
      if (NV1 + i == NL - 1 - LEAD) {
        if (lane == 0) __hip_atomic_fetch_add((LAS unsigned*)(L + L_FLAG) + 3, 1u, __ATOMIC_RELAXED, __HIP_MEMORY_SCOPE_WORKGROUP);
      }
    }
    STAMP(5);
#pragma unroll
    for (int k = 0; k < 8; ++k) acc[k] = xrow_sum(acc[k]);
    if (g == 0) {
      LAS float* mr = (LAS float*)(L + L_MRG) + w * 132;
      *(LAS f4*)(mr + 4 + 8 * i16) = f4{acc[0], acc[1], acc[2], acc[3]};
      *(LAS f4*)(mr + 8 + 8 * i16) = f4{acc[4], acc[5], acc[6], acc[7]};
      if (i16 == 0) { mr[0] = m; mr[1] = l; }
    }
  } else {
    __builtin_amdgcn_s_setprio(3);
    if (lane == 0) { flags[0] = 0u; flags[1] = 0u; flags[2] = 0u; flags[3] = 0u; }
    if (ABL_CTRL) {
      bar();
      while ((unsigned)__builtin_amdgcn_s_memrealtime() - tstart < 800) __builtin_amdgcn_s_sleep(8);
      if (lane == 0) flags[1] = 1u;
    } else {
    const unsigned E = __builtin_amdgcn_raw_buffer_load_b32(rsws, WS_EPOCH, 0, 16);
    unsigned warm = 0;
    {
      const int n = sl * 64 + lane, pi = n / 640, row = n - 640 * pi, pr = xcd + 8 * pi;
      if (WARM && pr < 20) warm = __builtin_amdgcn_raw_buffer_load_b32(mkrs(p.wmod, DM * 6 * DM * 2), (unsigned)(row * 7680 + pr * 128), 0, 2);
    }
    const GAS v4u* xp = (const GAS v4u*)p.x;
    v4u xr[10];
    const int sj = lane >> 3, sub = lane & 7;
#pragma unroll
    for (int u = 0; u < 10; ++u) xr[u] = xp[sj * 80 + sub + 8 * u];
    SCHED();
    bar();
    unsigned xh[5];
#pragma unroll
    for (int u = 0; u < 5; ++u) { const int idx = u * 64 + lane, j = idx / 40, m = idx - 40 * j; xh[u] = ((const GAS unsigned*)p.x)[j * 320 + 40 * b + m]; }
    if (!ABL_SENT) {
      const v4u sv = {SENT, SENT, SENT, SENT};
      if (lane < 16) st_sc(sv, rsws, WS_OX + (unsigned)(h * 8 + b) * 256 + lane * 16);
      if (lane < 5) st_sc2(v2u{SENT, SENT}, rsws, WS_X1B + (unsigned)(b * 320 + 10 * h) * 4 + lane * 8);
    }
    STAMP(1);
    {
      float sm = 0.f;
#pragma unroll
      for (int u = 0; u < 10; ++u)
        sm += ((blo(xr[u].x) + bhi(xr[u].x)) + (blo(xr[u].y) + bhi(xr[u].y))) + ((blo(xr[u].z) + bhi(xr[u].z)) + (blo(xr[u].w) + bhi(xr[u].w)));
      const float mean = oct_sum(sm) * (1.f / DM);
      float sq = 0.f;
#pragma unroll
      for (int u = 0; u < 10; ++u) {
#pragma unroll
        for (int k = 0; k < 4; ++k) { const float a = blo(xr[u][k]) - mean, c2 = bhi(xr[u][k]) - mean; sq = fmaf(a, a, sq); sq = fmaf(c2, c2, sq); }
      }
      const float var = oct_sum(sq) * (1.f / DM);
      if (sub == 0) {
        LAS float* st = (LAS float*)(L + L_STAT) + sj * 2;
        st[0] = rbf(mean);
        st[1] = rcp(rbf(__builtin_amdgcn_sqrtf(rbf(rbf(var) + 1e-5f))));
      }
    }
    VMWAIT(0);
    asm volatile("" :: "v"(warm));
    const unsigned T = rfl(E) + 1u;
    const unsigned t10 = T & 0x3FFu;
    if (lane == 0) flags[4] = T;
    if (lane == 0) st_sc2(v2u{T, ~T}, rsws, WS_INIT + (unsigned)(b * 32 + h) * 8);
    STAMP2(9);
    if (has_mod) {
      unsigned spins = 0;
      while (flags[0] < (unsigned)NSW) { __builtin_amdgcn_s_sleep(1); if (++spins > SPIN_L) break; }
      CFENCE();
      STAMP(2);
      f4 a0 = {0, 0, 0, 0}, a1 = {0, 0, 0, 0};
      const LAS unsigned char* ap = L + L_RT + (8 * g + tq) * 64 + tp * 8;
      const LAS unsigned char* bp = L + L_SC + (lane & 7) * 1296 + g * 16;
#pragma unroll 2
      for (int s = 0; s < 20; ++s) {
        const v4u bb = *(const LAS v4u*)(bp + s * 64);
        const v2u x0 = trread(ap + s * 2048), x1 = trread(ap + s * 2048 + 256);
        const v2u y0 = trread(ap + s * 2048 + 32), y1 = trread(ap + s * 2048 + 256 + 32);
        a0 = mfma32(v4u{x0.x, x0.y, x1.x, x1.y}, bb, a0);
        a1 = mfma32(v4u{y0.x, y0.y, y1.x, y1.y}, bb, a1);
      }
      if (i16 < 8) {
        const unsigned o = WS_MODG + (unsigned)(i16 * 1920 + (mcol0 + 4 * g) / 2) * 8;
        st_sc(v4u{pk2(a0[0], a0[1]), T, pk2(a0[2], a0[3]), T}, rsws, o);
        st_sc(v4u{pk2(a1[0], a1[1]), T, pk2(a1[2], a1[3]), T}, rsws, o + 64);
      }
    }
    STAMP(3);
    {
      v2u sa[5], ca[5], fl[4];
      if (D1) { while ((unsigned)__builtin_amdgcn_s_memrealtime() - tstart < D1) __builtin_amdgcn_s_sleep(2); }
      STAMP2(10);
      if (ABL_G1) { for (int u = 0; u < 5; ++u) { sa[u] = v2u{0u, T}; ca[u] = v2u{0u, T}; } }
      for (unsigned spins = 0; !ABL_G1; ++spins) {
        CFENCE();
        bool ok = true;
#pragma unroll
        for (int u = 0; u < 5; ++u) {
          const int idx = u * 64 + lane, j = idx / 40, m = idx - 40 * j;
          const unsigned o = WS_MODG + (unsigned)(j * 1920 + 40 * b + m) * 8;
          sa[u] = ld_sc2(rsws, o);
          ca[u] = ld_sc2(rsws, o + 320 * 8);
        }
#pragma unroll
        for (int u = 0; u < 4; ++u) fl[u] = ld_sc2(rsws, WS_INIT + (unsigned)(u * 64 + lane) * 8);
#pragma unroll
        for (int u = 0; u < 5; ++u) ok = ok && sa[u].y == T && ca[u].y == T;
#pragma unroll
        for (int u = 0; u < 4; ++u) ok = ok && fl[u].x == T && fl[u].y == ~T;
        if (__all(ok) || spins > SPIN_G) break;
        __builtin_amdgcn_s_sleep(PSLEEP);
      }
      STAMP2(11);
      asm volatile("s_waitcnt lgkmcnt(0)" ::: "memory");
#pragma unroll
      for (int u = 0; u < 5; ++u) {
        const int idx = u * 64 + lane, j = idx / 40, m = idx - 40 * j;
        const float mean = ((LAS float*)(L + L_STAT))[2 * j], den = ((LAS float*)(L + L_STAT))[2 * j + 1];
        const float n0 = rbf(rbf(blo(xh[u]) - mean) * den), n1 = rbf(rbf(bhi(xh[u]) - mean) * den);
        const float h0 = rbf(n0 * rbf(1.f + blo(ca[u].x))) + blo(sa[u].x), h1 = rbf(n1 * rbf(1.f + bhi(ca[u].x))) + bhi(sa[u].x);
        *(LAS unsigned*)(L + L_HS + j * 176 + m * 4) = pk2(h0, h1);
      }
    }
    STAMP(4);
    {
      unsigned spins = 0;
      while (flags[2] < (unsigned)NSW) { __builtin_amdgcn_s_sleep(1); if (++spins > SPIN_L) break; }
      CFENCE();
    }
    STAMP2(12);
    {
      f4 qa[8];
#pragma unroll
      for (int t = 0; t < 8; ++t) qa[t] = f4{0, 0, 0, 0};
#pragma unroll
      for (int s = 0; s < 5; ++s) {
        const v2u bb = *(const LAS v2u*)(L + L_HS + (lane & 7) * 176 + s * 32 + g * 8);
        const int row = 16 * s + 4 * g + tq;
        const LAS unsigned char* rp = L + L_QT + row * 256 + (tp & 1) * 8;
        const int sw = 2 * (row & 7);
#pragma unroll
        for (int t = 0; t < 8; ++t) qa[t] = mfma16(trread(rp + (((2 * t + (tp >> 1)) ^ sw) * 16)), bb, qa[t]);
      }
      if (i16 < 8 && !ABL_G2) {
        const unsigned o = WS_QP + (unsigned)(((h * 8 + b) * 8 + i16) * 128 + 4 * g) * 4;
#pragma unroll
        for (int t = 0; t < 8; ++t) st_sc(tg4(qa[t], t10), rsws, o + t * 64);
      }
    }
    STAMP(5);
    const unsigned tpub = (unsigned)__builtin_amdgcn_s_memrealtime();
    {
      v4u qv[4];
      if (D2) { while ((unsigned)__builtin_amdgcn_s_memrealtime() - tpub < D2) __builtin_amdgcn_s_sleep(2); }
      STAMP2(13);
      if (ABL_G2) { for (int u = 0; u < 4; ++u) qv[u] = v4u{t10, t10, t10, t10}; }
      for (unsigned spins = 0; !ABL_G2; ++spins) {
        CFENCE();
        bool ok = true;
#pragma unroll
        for (int u = 0; u < 4; ++u) qv[u] = ld_sc(rsws, WS_QP + (unsigned)(((h * 8 + 2 * u + (lane >> 5)) * 8 + b) * 128 + 4 * (lane & 31)) * 4);
#pragma unroll
        for (int u = 0; u < 4; ++u) ok = ok && tagged4(qv[u], t10);
        if (__all(ok) || spins > SPIN_G) break;
        __builtin_amdgcn_s_sleep(PSLEEP);
      }
      f4 qs;
#pragma unroll
      for (int k = 0; k < 4; ++k) qs[k] = x32_sum((untg(qv[0][k]) + untg(qv[1][k])) + (untg(qv[2][k]) + untg(qv[3][k])));
      if (lane < 32) *(LAS v2u*)(L + L_Q + lane * 8) = v2u{pk2(qs[0], qs[1]), pk2(qs[2], qs[3])};
      asm volatile("s_waitcnt lgkmcnt(0)" ::: "memory");
      if (lane == 0) flags[1] = 1u;
    }
    STAMP(6);
    if (!ABL_G3) {
      const unsigned go = WS_MODG + (unsigned)(b * 1920 + (lane < 10 ? 640 + 10 * h + lane : 1600 + 10 * h + (lane < 20 ? lane - 10 : 0))) * 8;
      const unsigned xv = ((const GAS unsigned*)p.x)[b * 320 + 10 * h + (lane < 10 ? lane : 0)];
      v2u gv;
      for (unsigned spins = 0;; ++spins) {
        CFENCE();
        gv = ld_sc2(rsws, go);
        if (__all(gv.y == T) || spins > SPIN_G) break;
        __builtin_amdgcn_s_sleep(32);
      }
      if (lane < 20) *(LAS unsigned*)(L + L_VEC + lane * 4) = gv.x;
      if (lane < 10) *(LAS unsigned*)(L + L_VEC + 80 + lane * 4) = xv;
      if (is_mlp) {
        for (int j = 0; j < 8; ++j) {
          v4u cv[5];
          for (unsigned spins = 0;; ++spins) {
            CFENCE();
            bool ok = true;
#pragma unroll
            for (int u = 0; u < 5; ++u) cv[u] = ld_sc(rsws, WS_MODG + (unsigned)(j * 1920 + 960) * 8 + (unsigned)(u * 64 + lane) * 16);
#pragma unroll
            for (int u = 0; u < 5; ++u) ok = ok && cv[u].y == T && cv[u].w == T;
            if (__all(ok) || spins > SPIN_G) break;
            __builtin_amdgcn_s_sleep(32);
          }
#pragma unroll
          for (int u = 0; u < 5; ++u) {
            const int pc = u * 64 + lane;
            const unsigned o = (pc < 160 ? 10240u + (unsigned)(j * 320 + 2 * pc) * 4 : (unsigned)(j * 320 + 2 * (pc - 160)) * 4);
            const v2u sc1 = {pk2(1.f + blo(cv[u].x), 1.f + bhi(cv[u].x)), pk2(1.f + blo(cv[u].z), 1.f + bhi(cv[u].z))};
            *(LAS v2u*)(L + L_CS + o) = pc < 160 ? v2u{cv[u].x, cv[u].z} : sc1;
          }
        }
      }
    }
    STAMP(7);
    {
      unsigned spins = 0;
      while (flags[3] < (unsigned)NSW) { __builtin_amdgcn_s_sleep(1); if (++spins > SPIN_L) break; }
      CFENCE();
    }
    {
      const rsrc_t rswo = mkrs(p.wo + (size_t)h * DH * DM, DH * DM * 2);
      for (int n = 0; n < 20; ++n) {
        const int ci = n * 64 + lane, row = ci / 10, ch = ci - 10 * row;
        dma16(rswo, lds0 + L_T1 + n * 1024, (unsigned)(row * 1280 + b * 160 + ch * 16), 0);
      }
    }
    }
  }

  bar();
  STAMP(8);
  if (ABL_CTRL || ABL_TAIL) {
    STAMP(15);
    if (lane == 0) {
#pragma unroll
      for (int k = 0; k < 16; ++k) ((GAS unsigned long long*)(p.ws + WS_DIAG))[(blockIdx.x * 8 + w) * 16 + k] = ts[k] ? (unsigned long long)ts[k] + (1ull << 32) : 0ull;
    }
    return;
  }
  const unsigned t10 = flags[4] & 0x3FFu;
  auto until = [&](unsigned t0, unsigned ticks) { while ((unsigned)__builtin_amdgcn_s_memrealtime() - t0 < ticks) __builtin_amdgcn_s_sleep(1); };
  if (w == 0) {
    if (lane < 32) {
      const LAS float* mr = (const LAS float*)(L + L_MRG);
      float M = mr[0];
#pragma unroll
      for (int u = 1; u < NSW; ++u) M = fmaxf(M, mr[u * 132]);
      constexpr float C2 = 0.08838834764831845f * 1.4426950408889634f;
      float Lsum = 0.f;
      f4 o = {0, 0, 0, 0};
#pragma unroll
      for (int u = 0; u < NSW; ++u) {
        const float f = __builtin_amdgcn_exp2f((mr[u * 132] - M) * C2);
        Lsum = fmaf(f, mr[u * 132 + 1], Lsum);
        o += f * *(const LAS f4*)(mr + u * 132 + 4 + 4 * lane);
      }
      const float inv = rcp(Lsum);
      st_sc2(v2u{canonu(pk2(o[0] * inv, o[1] * inv)), canonu(pk2(o[2] * inv, o[3] * inv))}, rsws, WS_OX + (unsigned)(h * 8 + b) * 256 + lane * 8);
    }
  } else if (w < 3) {
    const int t = tid - 64;
    __builtin_amdgcn_s_sleep(TD_A);
    v4u v;
    for (unsigned spins = 0;; ++spins) {
      CFENCE();
      v = ld_sc(rsws, WS_OX + (unsigned)h * 2048 + t * 16);
      if (__all(valid4(v)) || spins > SPIN_G) break;
      __builtin_amdgcn_s_sleep(TS);
    }
    *(LAS v4u*)(L + L_OS + (t >> 4) * 272 + (t & 15) * 16) = v;
  } else if (w == 7) {
    VMWAIT(0);
  }
  bar();
  const unsigned tB2 = (unsigned)__builtin_amdgcn_s_memrealtime();
  STAMP(9);
  if (w < 4) {
    const LAS unsigned char* bp = L + L_OS + (lane & 7) * 272 + g * 16;
    v4u bb[4];
#pragma unroll
    for (int s = 0; s < 4; ++s) bb[s] = *(const LAS v4u*)(bp + s * 64);
    for (int t = w; t < 5; t += 4) {
      f4 y = {0, 0, 0, 0};
      const LAS unsigned char* ap = L + L_T1 + (8 * g + tq) * 160 + (16 * t + 4 * tp) * 2;
#pragma unroll
      for (int s = 0; s < 4; ++s) {
        const v2u x0 = trread(ap + s * 32 * 160), x1 = trread(ap + s * 32 * 160 + 4 * 160);
        y = mfma32(v4u{x0.x, x0.y, x1.x, x1.y}, bb[s], y);
      }
      const int cc = 4 * t + g;
      if (i16 < 8) st_sc(tg4(y, t10), rsws,
                         WS_T1P + (unsigned)((((i16 * 32 + 4 * b + cc / 5) * 32 + h) * 5 + cc % 5) * 16));
    }
  } else if (w < 7) {
    const int t = tid - 256;
    const bool act = t < 160;
    const unsigned o = WS_T1P + (unsigned)((b * 32 + h) * 160 + (act ? t : 0)) * 16;
    until(tB2, GB);
    v4u v;
    for (unsigned spins = 0;; ++spins) {
      CFENCE();
      v = ld_sc(rsws, o);
      if (__all(tagged4(v, t10)) || spins > SPIN_G) break;
      __builtin_amdgcn_s_sleep(TS);
    }
    if (act) *(LAS f4*)(L + L_T4P + t * 16) = f4{untg(v.x), untg(v.y), untg(v.z), untg(v.w)};
  }
  bar();
  if (w == 4) {
    const int e = lane >> 3, c = lane & 7;
    f4 sm = {0, 0, 0, 0};
    if (c < 5) {
#pragma unroll
      for (int u = 0; u < 4; ++u) sm += *(const LAS f4*)(L + L_T4P + ((4 * e + u) * 5 + c) * 16);
    }
#pragma unroll
    for (int k = 0; k < 4; ++k) { float x = sm[k]; x += dpp<0x128>(x); sm[k] = xrow_sum(x); }
    if (lane < 5) {
      const LAS unsigned* vec = (const LAS unsigned*)(L + L_VEC);
      const unsigned g0 = vec[2 * lane], g1 = vec[2 * lane + 1], x0 = vec[20 + 2 * lane], x1 = vec[20 + 2 * lane + 1];
      const float r0 = rbf(blo(x0) + rbf(blo(g0) * rbf(sm[0]))), r1 = rbf(bhi(x0) + rbf(bhi(g0) * rbf(sm[1])));
      const float r2 = rbf(blo(x1) + rbf(blo(g1) * rbf(sm[2]))), r3 = rbf(bhi(x1) + rbf(bhi(g1) * rbf(sm[3])));
      st_sc2(v2u{canonu(pk2(r0, r1)), canonu(pk2(r2, r3))}, rsws, WS_X1B + (unsigned)(b * 320 + 10 * h + 2 * lane) * 4);
      *(LAS f4*)(L + L_X1O + lane * 16) = f4{r0, r1, r2, r3};
    }
  }
  bar();
  const unsigned tB = (unsigned)__builtin_amdgcn_s_memrealtime();
  STAMP(10);
  if (is_mlp) {
    {
      const unsigned o = WS_X1B + (unsigned)w * 1280 + lane * 4;
      unsigned cs[5], sh[5];
#pragma unroll
      for (int u = 0; u < 5; ++u) {
        cs[u] = *(const LAS unsigned*)(L + L_CS + (w * 320 + u * 64 + lane) * 4);
        sh[u] = *(const LAS unsigned*)(L + L_CS + 10240 + (w * 320 + u * 64 + lane) * 4);
      }
      until(tB, GC);
      unsigned xv[5];
      for (unsigned spins = 0;; ++spins) {
        CFENCE();
        bool ok = true;
#pragma unroll
        for (int u = 0; u < 5; ++u) xv[u] = __builtin_amdgcn_raw_buffer_load_b32(rsws, o + u * 256, 0, 16);
#pragma unroll
        for (int u = 0; u < 5; ++u) ok = ok && xv[u] != SENT;
        if (__all(ok) || spins > SPIN_G) break;
        __builtin_amdgcn_s_sleep(TS);
      }
      const float x00 = blo(rfl(xv[0]));
      float s1 = 0.f, s2 = 0.f;
#pragma unroll
      for (int u = 0; u < 5; ++u) {
        const float d0 = blo(xv[u]) - x00, d1 = bhi(xv[u]) - x00;
        s1 += d0 + d1; s2 = fmaf(d0, d0, fmaf(d1, d1, s2));
      }
      s1 = row16_sum(s1); s2 = row16_sum(s2);
      s1 = xrow_sum(s1) * (1.f / DM); s2 = xrow_sum(s2) * (1.f / DM);
      const float rs = __builtin_amdgcn_rsqf(fmaxf(s2 - s1 * s1, 0.f) + 1e-5f), mr = -(x00 + s1) * rs;
#pragma unroll
      for (int u = 0; u < 5; ++u) {
        const float n0 = fmaf(blo(xv[u]), rs, mr), n1 = fmaf(bhi(xv[u]), rs, mr);
        *(LAS unsigned*)(L + L_H2 + w * 1296 + (u * 64 + lane) * 4) = pk2(fmaf(n0, blo(cs[u]), blo(sh[u])), fmaf(n1, bhi(cs[u]), bhi(sh[u])));
      }
    }
    bar();
    STAMP(11);
    {
      const int t = w & 1, kq = w >> 1;
      f4 a = {0, 0, 0, 0};
      const LAS unsigned char* ap = L + L_RT + (160 * kq + 8 * g + tq) * 64 + t * 32 + tp * 8;
      const LAS unsigned char* bp = L + L_H2 + (lane & 7) * 1296 + (160 * kq + 8 * g) * 2;
#pragma unroll
      for (int s = 0; s < 5; ++s) {
        const v4u bb = *(const LAS v4u*)(bp + s * 64);
        const v2u x0 = trread(ap + s * 2048), x1 = trread(ap + s * 2048 + 256);
        a = mfma32(v4u{x0.x, x0.y, x1.x, x1.y}, bb, a);
      }
      if (i16 < 8) *(LAS f4*)(L + L_T4P + (((kq * 2 + t) * 4 + g) * 8 + i16) * 16) = a;
    }
    bar();
    if (tid < 128) {
      const int j = tid >> 4, c = 2 * (tid & 15), t = c >> 4, gg = (c & 15) >> 2, r = c & 3;
      float z0 = 0.f, z1 = 0.f;
#pragma unroll
      for (int kq = 0; kq < 4; ++kq) {
        const LAS float* z = (const LAS float*)(L + L_T4P + (((kq * 2 + t) * 4 + gg) * 8 + j) * 16) + r;
        z0 += z[0]; z1 += z[1];
      }
      *(LAS unsigned*)(L + L_H1 + j * 80 + c * 2) = pk2(silu(rbf(z0)), silu(rbf(z1)));
    }
    bar();
    STAMP(12);
    {
      const v4u bb = *(const LAS v4u*)(L + L_H1 + (lane & 7) * 80 + g * 16);
      const int r0 = 8 * g + tq;
      const LAS unsigned char* ap = L + L_W2 + r0 * 1280 + (tp & 1) * 8;
      const int f = 2 * (r0 & 3);
#pragma unroll
      for (int tt = 0; tt < 5; ++tt) {
        const int ch = (2 * (5 * w + tt) + (tp >> 1)) ^ f;
        const v2u x0 = trread(ap + ch * 16), x1 = trread(ap + 4 * 1280 + ch * 16);
        const f4 y = mfma32(v4u{x0.x, x0.y, x1.x, x1.y}, bb, f4{0, 0, 0, 0});
        if (i16 < 8) *(LAS v4u*)(L + L_RT + i16 * 2576 + (16 * (5 * w + tt) + 4 * g) * 4) = tg4(y, t10);
      }
    }
    bar();
    {
#pragma unroll
      for (int u = 0; u < 3; ++u) {
        const int pc = u * 512 + tid, j = pc / 160, c = pc - 160 * j;
        if (pc < 1280) st_sc(*(const LAS v4u*)(L + L_RT + j * 2576 + c * 16), rsws, WS_T5P + (unsigned)mm * 20480 + (unsigned)pc * 16);
      }
    }
  }
  const unsigned tD = (unsigned)__builtin_amdgcn_s_memrealtime();
  STAMP(13);
  if (w < 5) {
    const bool act = tid < NMLP * 5;
    const int dm = act ? tid / 5 : 0, dc = act ? tid - 5 * dm : 0;
    const unsigned o = WS_T5P + (unsigned)((dm * 8 + b) * 640 + 20 * h + 4 * dc) * 4;
    if (is_mlp) until(tD, GD1); else until(tB, GD2);
    v4u v;
    for (unsigned spins = 0;; ++spins) {
      CFENCE();
      v = ld_sc(rsws, o);
      if (__all(tagged4(v, t10)) || spins > SPIN_G) break;
      __builtin_amdgcn_s_sleep(TS);
    }
    if (act) *(LAS f4*)(L + L_W2 + tid * 16) = f4{untg(v.x), untg(v.y), untg(v.z), untg(v.w)};
  }
  bar();
  if (w == 0) {
    const int e = lane / 5, c = lane - 5 * e;
    f4 sm = {0, 0, 0, 0};
    if (lane < 45) {
#pragma unroll
      for (int u = 0; u < 6; ++u) sm += *(const LAS f4*)(L + L_W2 + ((6 * e + u) * 5 + c) * 16);
      *(LAS f4*)(L + L_W2 + 8192 + lane * 16) = sm;
    }
    asm volatile("s_waitcnt lgkmcnt(0)" ::: "memory");
    if (lane < 5) {
      sm = f4{0, 0, 0, 0};
#pragma unroll
      for (int u = 0; u < 9; ++u) sm += *(const LAS f4*)(L + L_W2 + 8192 + (u * 5 + lane) * 16);
      const LAS unsigned* vec = (const LAS unsigned*)(L + L_VEC);
      const unsigned g0 = vec[10 + 2 * lane], g1 = vec[10 + 2 * lane + 1];
      const f4 x1 = *(const LAS f4*)(L + L_X1O + lane * 16);
      const float r0 = x1[0] + rbf(blo(g0) * rbf(sm[0])), r1 = x1[1] + rbf(bhi(g0) * rbf(sm[1]));
      const float r2 = x1[2] + rbf(blo(g1) * rbf(sm[2])), r3 = x1[3] + rbf(bhi(g1) * rbf(sm[3]));
      *(GAS v2u*)(p.out + b * DM + 20 * h + 4 * lane) = v2u{pk2(r0, r1), pk2(r2, r3)};
    }
  }
  if (id == 0 && tid == 448) {
    const unsigned E = __builtin_amdgcn_raw_buffer_load_b32(rsws, WS_EPOCH, 0, 16);
    __builtin_amdgcn_raw_buffer_store_b32(E + 1u, rsws, WS_EPOCH, 0, 16);
  }
  STAMP(15);
  if (lane == 0 && (id % 37 == 0) && (w == 0 || w == 7)) {
    printf("WG %d w %d start %u: %d %d %d %d %d %d %d %d %d %d %d %d %d %d\n", id, w, ts[0], (int)(ts[1] - ts[0]), (int)(ts[2] - ts[0]), (int)(ts[3] - ts[0]), (int)(ts[4] - ts[0]), (int)(ts[5] - ts[0]),
           (int)(ts[6] - ts[0]), (int)(ts[7] - ts[0]), (int)(ts[8] - ts[0]), (int)(ts[9] - ts[0]), (int)(ts[10] - ts[0]), (int)(ts[11] - ts[0]), (int)(ts[12] - ts[0]), (int)(ts[13] - ts[0]), (int)(ts[15] - ts[0]));
  }
}

}

extern "C" void kernel_launch(void* const* d_in, const int* in_sizes, int n_in, void* d_out, int out_size, void* d_ws,
                              size_t ws_size, hipStream_t stream) {
  Params p;
  p.x = (const u16*)d_in[0];
  p.kc = (const u16*)d_in[1];
  p.vc = (const u16*)d_in[2];
  p.wq = (const u16*)d_in[3];
  p.wo = (const u16*)d_in[4];
  p.w1 = (const u16*)d_in[6];
  p.w2 = (const u16*)d_in[7];
  p.c = (const u16*)d_in[8];
  p.wmod = (const u16*)d_in[9];
  p.out = (u16*)d_out;
  p.ws = (unsigned char*)d_ws;
  hipLaunchKernelGGL(fused_block, dim3(256), dim3(512), 0, stream, p);
}
```

```cpp
#include <hip/hip_runtime.h>
#include <stdint.h>

#define DIAG 0

namespace {

typedef unsigned short u16;
typedef unsigned v4u __attribute__((ext_vector_type(4)));
typedef unsigned v2u __attribute__((ext_vector_type(2)));
typedef float f4 __attribute__((ext_vector_type(4)));
typedef short s4 __attribute__((ext_vector_type(4)));
typedef short s8 __attribute__((ext_vector_type(8)));
typedef __bf16 bf2 __attribute__((ext_vector_type(2)));
typedef __bf16 bf8 __attribute__((ext_vector_type(8)));
typedef __amdgpu_buffer_rsrc_t rsrc_t;
#define LAS __attribute__((address_space(3)))
#define GAS __attribute__((address_space(1)))
#define DI __device__ __forceinline__

constexpr int NB = 8, DM = 640, NH = 32, DH = 128, SK = 4096, WIN = 1024, FF = 1728;
constexpr int NSW = 7;
constexpr int NL = 37;
#define TD_A 8
#define TD_B 8
#define TD_C 16
#define TD_D 8
#define TD_D2 4
#define TS 6
#define ABL_CTRL 0
#define ABL_SLOT 0
#define ABL_ROLE 0
#define ABL_SENT 0
#define LEAD 3
#define MP0 12
#define MPACE 11
#define DELTA 0
#define HEAVY 1
#define ABL_G1 0
#define ABL_G2 0
#define ABL_G3 0
#define ABL_TAIL 0
#define GB 40
#define GC 40
#define GD1 40
#define GD2 190
#define WARM 1
#define INDIC 1
#define ABL_WO 0
#define ABL_W12 0
#define D1 600
#define D2 100
#define PSLEEP 32
#define DPACE 15
#define PACE_ 3
constexpr int PACE = PACE_;
#define NPRE_ 16
constexpr int NPRE = NPRE_;

constexpr int NMLP = 54;
constexpr unsigned WS_EPOCH = 0;
constexpr unsigned WS_INIT = 4096;
constexpr unsigned WS_MODG = 8192;
constexpr unsigned WS_QP = WS_MODG + NB * 1920 * 8;
constexpr unsigned WS_OX = WS_QP + NH * 8 * 8 * 128 * 4;
constexpr unsigned WS_T1P = WS_OX + NH * 8 * 256;
constexpr unsigned WS_X1B = WS_T1P + NB * NH * DM * 4;
constexpr unsigned WS_T5P = WS_X1B + NB * 320 * 4;
constexpr unsigned WS_END = WS_T5P + NMLP * NB * DM * 4;
constexpr unsigned WS_DIAG = 4u << 20;
constexpr unsigned SENT = 0xFFFFFFFFu;
static_assert(WS_END <= WS_DIAG, "workspace map");

constexpr unsigned L_RT = 0;
constexpr unsigned L_QT = 40960;
constexpr unsigned L_W2 = 61440;
constexpr unsigned L_T1 = 102400;
constexpr unsigned L_CS = 122880;
constexpr unsigned L_MISC = 143360;
constexpr unsigned L_FLAG = L_MISC;
constexpr unsigned L_Q = L_MISC + 64;
constexpr unsigned L_STAT = L_Q + 256;
constexpr unsigned L_VEC = L_STAT + 64;
constexpr unsigned L_X1O = L_VEC + 128;
constexpr unsigned L_SC = L_X1O + 128;
constexpr unsigned L_H2 = L_SC;
constexpr unsigned L_HS = L_SC + 8 * 1296;
constexpr unsigned L_MRG = L_HS + 8 * 176;
constexpr unsigned L_OS = L_MRG + 7 * 132 * 4;
constexpr unsigned L_T4P = L_MRG;
constexpr unsigned L_H1 = L_T4P + 4096;
constexpr unsigned L_END = 163840;
static_assert(L_OS + 8 * 272 <= L_END && L_H1 + 8 * 80 <= L_OS + 8 * 272, "LDS map");
static_assert((L_SC % 16) == 0 && (L_HS % 16) == 0 && (L_MRG % 16) == 0 && (L_OS % 16) == 0 && (L_H1 % 16) == 0, "LDS alignment");

constexpr unsigned SPIN_G = 1u << 16;
constexpr unsigned SPIN_L = 1u << 22;

struct Params {
  const u16 *x, *kc, *vc, *wq, *wo, *w1, *w2, *c, *wmod;
  u16* out;
  unsigned char* ws;
};

DI unsigned rfl(unsigned x) { return __builtin_amdgcn_readfirstlane(x); }
DI float asf(unsigned u) { return __uint_as_float(u); }
DI unsigned asu(float f) { return __float_as_uint(f); }
DI unsigned pk2(float lo, float hi) { bf2 v = {(__bf16)lo, (__bf16)hi}; return __builtin_bit_cast(unsigned, v); }
DI float rbf(float f) { return asf(pk2(f, 0.f) << 16); }
DI float blo(unsigned u) { return asf(u << 16); }
DI float bhi(unsigned u) { return asf(u & 0xffff0000u); }
DI float dot2(unsigned a, unsigned b, float c) {
  return __builtin_amdgcn_fdot2_f32_bf16(__builtin_bit_cast(bf2, a), __builtin_bit_cast(bf2, b), c, false);
}
template <int CTRL> DI float dpp(float x) {
  return __builtin_bit_cast(float, __builtin_amdgcn_mov_dpp(__builtin_bit_cast(int, x), CTRL, 0xf, 0xf, true));
}
DI float row16_sum(float x) { x += dpp<0x128>(x); x += dpp<0x124>(x); x += dpp<0x122>(x); x += dpp<0x121>(x); return x; }
DI float oct_sum(float x) { x += dpp<0xB1>(x); x += dpp<0x4E>(x); x += dpp<0x141>(x); return x; }
DI float x16_sum(float x) {
  auto s = __builtin_amdgcn_permlane16_swap(asu(x), asu(x), false, false);
  return asf(s[0]) + asf(s[1]);
}
DI float x32_sum(float x) {
  auto t = __builtin_amdgcn_permlane32_swap(asu(x), asu(x), false, false);
  return asf(t[0]) + asf(t[1]);
}
DI float xrow_sum(float x) { return x32_sum(x16_sum(x)); }
DI float xrow_max(float x) {
  auto s = __builtin_amdgcn_permlane16_swap(asu(x), asu(x), false, false);
  x = fmaxf(asf(s[0]), asf(s[1]));
  auto t = __builtin_amdgcn_permlane32_swap(asu(x), asu(x), false, false);
  return fmaxf(asf(t[0]), asf(t[1]));
}
DI float wave_sum(float x) { return xrow_sum(row16_sum(x)); }
DI f4 mfma32(v4u a, v4u b, f4 c) {
  return __builtin_amdgcn_mfma_f32_16x16x32_bf16(__builtin_bit_cast(bf8, a), __builtin_bit_cast(bf8, b), c, 0, 0, 0);
}
DI f4 mfma16(v2u a, v2u b, f4 c) {
  return __builtin_amdgcn_mfma_f32_16x16x16bf16_1k(__builtin_bit_cast(s4, a), __builtin_bit_cast(s4, b), c, 0, 0, 0);
}
DI v2u trread(const LAS unsigned char* p) {
  return __builtin_bit_cast(v2u, __builtin_amdgcn_ds_read_tr16_b64_v4i16((LAS s4*)p));
}
DI rsrc_t mkrs(const void* p, unsigned bytes) { return __builtin_amdgcn_make_buffer_rsrc((void*)p, 0, (int)bytes, 0x00020000); }
DI v4u ld_nt(rsrc_t r, unsigned vo, unsigned so) { return __builtin_amdgcn_raw_buffer_load_b128(r, vo, so, 2); }
#define KAUX 2
#define VAUX 2
DI v4u ld_k(rsrc_t r, unsigned vo, unsigned so) { return __builtin_amdgcn_raw_buffer_load_b128(r, vo, so, KAUX); }
DI v4u ld_v(rsrc_t r, unsigned vo, unsigned so) { return __builtin_amdgcn_raw_buffer_load_b128(r, vo, so, VAUX); }
DI v4u ld_sc(rsrc_t r, unsigned vo) { return __builtin_amdgcn_raw_buffer_load_b128(r, vo, 0, 16); }
DI v2u ld_sc2(rsrc_t r, unsigned vo) { return __builtin_amdgcn_raw_buffer_load_b64(r, vo, 0, 16); }
DI void st_sc(v4u v, rsrc_t r, unsigned vo) { __builtin_amdgcn_raw_buffer_store_b128(v, r, vo, 0, 16); }
DI void st_sc2(v2u v, rsrc_t r, unsigned vo) { __builtin_amdgcn_raw_buffer_store_b64(v, r, vo, 0, 16); }
DI unsigned canonu(unsigned u) { return u == 0xFFFFFFFFu ? 0x7FFF7FFFu : u; }
DI void dma16(rsrc_t r, unsigned lds, unsigned vo, unsigned so) {
  unsigned keep;
  asm volatile("s_nop 4\n\ts_mov_b32 %0, m0\n\ts_mov_b32 m0, %1\n\ts_nop 0\n\tbuffer_load_dwordx4 %2, %3, %4 offen nt lds\n\ts_mov_b32 m0, %0"
               : "=&s"(keep) : "s"(lds), "v"(vo), "s"(r), "s"(so) : "memory");
}
template <int N> DI void vmwait() { asm volatile("s_waitcnt vmcnt(%0)" :: "n"(N) : "memory"); }
#define VMWAIT(n) vmwait<(n)>()
DI void bar() { asm volatile("s_waitcnt lgkmcnt(0)\n\ts_barrier" ::: "memory"); }
#define CFENCE() asm volatile("" ::: "memory")
#define SCHED() __builtin_amdgcn_sched_barrier(0)
DI unsigned canon(float f) { unsigned u = asu(f); return u == SENT ? 0x7FFFFFFFu : u; }
DI unsigned tg(float f, unsigned t10) { return ((asu(f) + 0x200u) & ~0x3FFu) | t10; }
DI v4u tg4(f4 v, unsigned t10) { return v4u{tg(v[0], t10), tg(v[1], t10), tg(v[2], t10), tg(v[3], t10)}; }
DI bool tagged4(v4u v, unsigned t10) { return (((v.x ^ t10) | (v.y ^ t10) | (v.z ^ t10) | (v.w ^ t10)) & 0x3FFu) == 0u; }
DI float untg(unsigned u) { return asf(u & ~0x3FFu); }
DI bool valid4(v4u v) { return v.x != SENT && v.y != SENT && v.z != SENT && v.w != SENT; }
DI float rcp(float x) { return __builtin_amdgcn_rcpf(x); }
DI float silu(float v) { return v * rcp(1.f + __builtin_amdgcn_exp2f(v * -1.4426950408889634f)); }

#define STAMP(k) do { } while (0)
#define STAMP2(k) do { } while (0)

__global__ void __launch_bounds__(512, 2) fused_block(Params p) {
  __shared__ __attribute__((aligned(16))) unsigned char smem[L_END];
  LAS unsigned char* L = (LAS unsigned char*)smem;
  const unsigned lds0 = (unsigned)(uintptr_t)L;
  const int tid = threadIdx.x, lane = tid & 63;
  const int w = (int)rfl(tid >> 6);
  const int id = blockIdx.x, xcd = id & 7, sl = id >> 3;
  const int h = xcd * 4 + (sl >> 3), b = sl & 7;
  const int g = lane >> 4, i16 = lane & 15;
  const int tq = i16 >> 2, tp = lane & 3;
  const rsrc_t rsws = mkrs(p.ws, WS_END);
  volatile LAS unsigned* flags = (volatile LAS unsigned*)(L + L_FLAG);
  const int mpr = (sl >> 1) * 8 + xcd;
  const bool has_mod = !ABL_ROLE && sl < 16 && mpr < 60;
  const bool crit = has_mod && mpr < 20;
  const int mcol0 = (2 * mpr + (sl & 1)) * 32;
  const int fpr = ((sl - 16) >> 1) * 8 + xcd;
  const bool is_mlp = !ABL_ROLE && sl >= 16 && sl < 24 && fpr < 27;
  const int mm = 2 * fpr + (sl & 1);

  STAMP(0);
  const unsigned tstart = (unsigned)__builtin_amdgcn_s_memrealtime();
  auto sched = [&](int i, bool light) {
    if (DELTA && light) { while ((unsigned)__builtin_amdgcn_s_memrealtime() - tstart < (unsigned)(i * DELTA / 10)) __builtin_amdgcn_s_sleep(1); }
  };

  if (w < NSW) {
    const rsrc_t rsk = mkrs(p.kc + ((size_t)(b * NH + h) * SK + (SK - WIN)) * DH, WIN * DH * 2);
    const rsrc_t rsv = mkrs(p.vc + ((size_t)(b * NH + h) * SK + (SK - WIN)) * DH, WIN * DH * 2);
    const unsigned vo = (unsigned)(lane * 16 + w * 1024);
    v4u kr[NL], vr[NL], cr[2];
    if (has_mod && w < 5) {
      const GAS v4u* cp = (const GAS v4u*)p.c;
      cr[0] = cp[w * 128 + lane];
      cr[1] = cp[w * 128 + 64 + lane];
    }
    auto silu_c = [&]() {
      if (w < 5) {
#pragma unroll
        for (int u = 0; u < 2; ++u) {
          const int q = w * 128 + u * 64 + lane, j = q / 80, k8 = q - 80 * j;
          v4u o;
          o.x = pk2(silu(blo(cr[u].x)), silu(bhi(cr[u].x)));
          o.y = pk2(silu(blo(cr[u].y)), silu(bhi(cr[u].y)));
          o.z = pk2(silu(blo(cr[u].z)), silu(bhi(cr[u].z)));
          o.w = pk2(silu(blo(cr[u].w)), silu(bhi(cr[u].w)));
          *(LAS v4u*)(L + L_SC + j * 1296 + k8 * 16) = o;
        }
      }
    };
    const rsrc_t rsrt = mkrs(p.wmod, DM * 6 * DM * 2);
    const unsigned rt_vo = (unsigned)((lane >> 2) * 7680 + mcol0 * 2 + (lane & 3) * 16);
    auto dma_rt = [&](int m) { dma16(rsrt, lds0 + L_RT + m * 1024, rt_vo, (unsigned)m * 16u * 7680u); };
    if (!crit) {
#pragma unroll
      for (int i = 0; i < PACE; ++i) kr[i] = ld_k(rsk, vo, i * 7168);
    }
    if (has_mod) { for (int m = w; m < 40; m += NSW) dma_rt(m); }
    SCHED();
    bar();
    if (has_mod) {
      silu_c();
      VMWAIT(0);
      asm volatile("s_waitcnt lgkmcnt(0)" ::: "memory");
      if (lane == 0) __hip_atomic_fetch_add((LAS unsigned*)(L + L_FLAG), 1u, __ATOMIC_RELAXED, __HIP_MEMORY_SCOPE_WORKGROUP);
    }
    {
      const rsrc_t rsq = mkrs(p.wq, DM * NH * DH * 2);
      for (int n = w; n < 20; n += NSW) {
        const int rl = 4 * n + g;
        dma16(rsq, lds0 + L_QT + n * 1024, (unsigned)((80 * b + rl) * 8192 + h * 256 + ((i16 ^ (2 * (rl & 7))) * 16)), 0);
      }
    }
    if (crit) {
#pragma unroll
      for (int i = 0; i < PACE; ++i) kr[i] = ld_k(rsk, vo, i * 7168);
    }
    SCHED();
    const rsrc_t rsw1 = mkrs(p.w1, DM * FF * 2);
    const rsrc_t rsw2 = mkrs(p.w2 + (size_t)(is_mlp ? mm : 0) * 32 * DM, 32 * DM * 2);
    auto slot = [&](int s) {
      const int n = 7 * s + w;
      if (n < 40) {
        dma16(rsw1, lds0 + L_RT + n * 1024, (unsigned)((lane >> 2) * (FF * 2) + mm * 64 + (lane & 3) * 16), (unsigned)n * 16u * (FF * 2));
      } else if (n < 80) {
        const int ci = (n - 40) * 64 + lane, row = ci / 80, pos = ci - 80 * row;
        dma16(rsw2, lds0 + L_W2 + (n - 40) * 1024, (unsigned)(row * 1280 + (pos ^ (2 * (row & 3))) * 16), 0);
      }
    };
    const bool heavy = HEAVY == 1 ? (is_mlp || has_mod) : HEAVY == 2 ? is_mlp : false;
#pragma unroll
    for (int i = PACE; i < NL + NPRE; ++i) {
      sched(i, !is_mlp);
      if (heavy) VMWAIT(PACE); else VMWAIT(PACE - 1);
      if (i < NL) kr[i] = ld_k(rsk, vo, i * 7168); else vr[i - NL] = ld_v(rsv, vo, (i - NL) * 7168);
      SCHED();
      if (i == PACE + 4) {
        asm volatile("s_waitcnt lgkmcnt(0)" ::: "memory");
        if (lane == 0) __hip_atomic_fetch_add((LAS unsigned*)(L + L_FLAG) + 2, 1u, __ATOMIC_RELAXED, __HIP_MEMORY_SCOPE_WORKGROUP);
        STAMP(1);
      }
      if (i >= 7 && (i % 4) == 3 && (i - 7) / 4 < 12) { if (is_mlp) slot((i - 7) / 4); SCHED(); }
    }
    STAMP(2);
    {
      unsigned spins = 0;
      while (flags[1] == 0u) { __builtin_amdgcn_s_sleep(1); if (++spins > SPIN_L) break; }
    }
    CFENCE();
    STAMP(3);
    const v4u q4 = *(const LAS v4u*)(L + L_Q + i16 * 16);
    float s[NL];
#pragma unroll
    for (int i = 0; i < NL; ++i) {
      float d = dot2(kr[i].x, q4.x, 0.f);
      d = dot2(kr[i].y, q4.y, d); d = dot2(kr[i].z, q4.z, d); d = dot2(kr[i].w, q4.w, d);
      s[i] = row16_sum(d);
      if ((i & 3) == 3 && NPRE + (i >> 2) < NL) { SCHED(); sched(NL + NPRE + (i >> 2), !is_mlp); if (heavy) VMWAIT(PACE); else VMWAIT(PACE - 1); vr[NPRE + (i >> 2)] = ld_v(rsv, vo, (NPRE + (i >> 2)) * 7168); SCHED(); }
    }
    constexpr int NV1 = NPRE + (NL >> 2);
    if (w >= 4) s[NL - 1] = -INFINITY;
    float m = s[0];
#pragma unroll
    for (int i = 1; i < NL; ++i) m = fmaxf(m, s[i]);
    m = xrow_max(m);
    constexpr float C2 = 0.08838834764831845f * 1.4426950408889634f;
    const float mc = m * C2;
    unsigned pp[NL];
    float l = 0.f;
#pragma unroll
    for (int i = 0; i < NL; ++i) {
      pp[i] = pk2(0.f, __builtin_amdgcn_exp2f(fmaf(s[i], C2, -mc)));
      l += asf(pp[i]);
    }
    l = xrow_sum(l);
    STAMP(4);
    float acc[8];
#pragma unroll
    for (int k = 0; k < 8; ++k) acc[k] = 0.f;
#pragma unroll
    for (int i = 0; i < NL; ++i) {
      const unsigned p1 = pp[i], p0 = p1 >> 16;
      acc[0] = dot2(vr[i].x, p0, acc[0]); acc[1] = dot2(vr[i].x, p1, acc[1]);
      acc[2] = dot2(vr[i].y, p0, acc[2]); acc[3] = dot2(vr[i].y, p1, acc[3]);
      acc[4] = dot2(vr[i].z, p0, acc[4]); acc[5] = dot2(vr[i].z, p1, acc[5]);
      acc[6] = dot2(vr[i].w, p0, acc[6]); acc[7] = dot2(vr[i].w, p1, acc[7]);
      SCHED();
      if (NV1 + i < NL) { sched(NL + NV1 + i, !is_mlp); if (heavy) VMWAIT(PACE); else VMWAIT(PACE - 1); vr[NV1 + i] = ld_v(rsv, vo, (NV1 + i) * 7168); SCHED(); }
      if (NV1 + i == NL - 1 - LEAD) {
        if (lane == 0) __hip_atomic_fetch_add((LAS unsigned*)(L + L_FLAG) + 3, 1u, __ATOMIC_RELAXED, __HIP_MEMORY_SCOPE_WORKGROUP);
      }
    }
    STAMP(5);
#pragma unroll
    for (int k = 0; k < 8; ++k) acc[k] = xrow_sum(acc[k]);
    if (g == 0) {
      LAS float* mr = (LAS float*)(L + L_MRG) + w * 132;
      *(LAS f4*)(mr + 4 + 8 * i16) = f4{acc[0], acc[1], acc[2], acc[3]};
      *(LAS f4*)(mr + 8 + 8 * i16) = f4{acc[4], acc[5], acc[6], acc[7]};
      if (i16 == 0) { mr[0] = m; mr[1] = l; }
    }
  } else {
    __builtin_amdgcn_s_setprio(3);
    if (lane == 0) { flags[0] = 0u; flags[1] = 0u; flags[2] = 0u; flags[3] = 0u; }
    if (ABL_CTRL) {
      bar();
      while ((unsigned)__builtin_amdgcn_s_memrealtime() - tstart < 800) __builtin_amdgcn_s_sleep(8);
      if (lane == 0) flags[1] = 1u;
    } else {
    const unsigned E = __builtin_amdgcn_raw_buffer_load_b32(rsws, WS_EPOCH, 0, 16);
    unsigned warm = 0;
    {
      const int n = sl * 64 + lane, pi = n / 640, row = n - 640 * pi, pr = xcd + 8 * pi;
      if (WARM && pr < 20) warm = __builtin_amdgcn_raw_buffer_load_b32(mkrs(p.wmod, DM * 6 * DM * 2), (unsigned)(row * 7680 + pr * 128), 0, 2);
    }
    const GAS v4u* xp = (const GAS v4u*)p.x;
    v4u xr[10];
    const int sj = lane >> 3, sub = lane & 7;
#pragma unroll
    for (int u = 0; u < 10; ++u) xr[u] = xp[sj * 80 + sub + 8 * u];
    SCHED();
    bar();
    unsigned xh[5];
#pragma unroll
    for (int u = 0; u < 5; ++u) { const int idx = u * 64 + lane, j = idx / 40, m = idx - 40 * j; xh[u] = ((const GAS unsigned*)p.x)[j * 320 + 40 * b + m]; }
    if (!ABL_SENT) {
      const v4u sv = {SENT, SENT, SENT, SENT};
      if (lane < 16) st_sc(sv, rsws, WS_OX + (unsigned)(h * 8 + b) * 256 + lane * 16);
      if (lane < 5) st_sc2(v2u{SENT, SENT}, rsws, WS_X1B + (unsigned)(b * 320 + 10 * h) * 4 + lane * 8);
    }
    STAMP(1);
    {
      float sm = 0.f;
#pragma unroll
      for (int u = 0; u < 10; ++u)
        sm += ((blo(xr[u].x) + bhi(xr[u].x)) + (blo(xr[u].y) + bhi(xr[u].y))) + ((blo(xr[u].z) + bhi(xr[u].z)) + (blo(xr[u].w) + bhi(xr[u].w)));
      const float mean = oct_sum(sm) * (1.f / DM);
      float sq = 0.f;
#pragma unroll
      for (int u = 0; u < 10; ++u) {
#pragma unroll
        for (int k = 0; k < 4; ++k) { const float a = blo(xr[u][k]) - mean, c2 = bhi(xr[u][k]) - mean; sq = fmaf(a, a, sq); sq = fmaf(c2, c2, sq); }
      }
      const float var = oct_sum(sq) * (1.f / DM);
      if (sub == 0) {
        LAS float* st = (LAS float*)(L + L_STAT) + sj * 2;
        st[0] = rbf(mean);
        st[1] = rcp(rbf(__builtin_amdgcn_sqrtf(rbf(rbf(var) + 1e-5f))));
      }
    }
    VMWAIT(0);
    asm volatile("" :: "v"(warm));
    const unsigned T = rfl(E) + 1u;
    const unsigned t10 = T & 0x3FFu;
    if (lane == 0) flags[4] = T;
    if (lane == 0) st_sc2(v2u{T, ~T}, rsws, WS_INIT + (unsigned)(b * 32 + h) * 8);
    STAMP2(9);
    if (has_mod) {
      unsigned spins = 0;
      while (flags[0] < (unsigned)NSW) { __builtin_amdgcn_s_sleep(1); if (++spins > SPIN_L) break; }
      CFENCE();
      STAMP(2);
      f4 a0 = {0, 0, 0, 0}, a1 = {0, 0, 0, 0};
      const LAS unsigned char* ap = L + L_RT + (8 * g + tq) * 64 + tp * 8;
      const LAS unsigned char* bp = L + L_SC + (lane & 7) * 1296 + g * 16;
#pragma unroll 2
      for (int s = 0; s < 20; ++s) {
        const v4u bb = *(const LAS v4u*)(bp + s * 64);
        const v2u x0 = trread(ap + s * 2048), x1 = trread(ap + s * 2048 + 256);
        const v2u y0 = trread(ap + s * 2048 + 32), y1 = trread(ap + s * 2048 + 256 + 32);
        a0 = mfma32(v4u{x0.x, x0.y, x1.x, x1.y}, bb, a0);
        a1 = mfma32(v4u{y0.x, y0.y, y1.x, y1.y}, bb, a1);
      }
      if (i16 < 8) {
        const unsigned o = WS_MODG + (unsigned)(i16 * 1920 + (mcol0 + 4 * g) / 2) * 8;
        st_sc(v4u{pk2(a0[0], a0[1]), T, pk2(a0[2], a0[3]), T}, rsws, o);
        st_sc(v4u{pk2(a1[0], a1[1]), T, pk2(a1[2], a1[3]), T}, rsws, o + 64);
      }
    }
    STAMP(3);
    {
      v2u sa[5], ca[5], fl[4];
      if (D1) { while ((unsigned)__builtin_amdgcn_s_memrealtime() - tstart < D1) __builtin_amdgcn_s_sleep(2); }
      STAMP2(10);
      if (ABL_G1) { for (int u = 0; u < 5; ++u) { sa[u] = v2u{0u, T}; ca[u] = v2u{0u, T}; } }
      for (unsigned spins = 0; !ABL_G1; ++spins) {
        CFENCE();
        bool ok = true;
#pragma unroll
        for (int u = 0; u < 5; ++u) {
          const int idx = u * 64 + lane, j = idx / 40, m = idx - 40 * j;
          const unsigned o = WS_MODG + (unsigned)(j * 1920 + 40 * b + m) * 8;
          sa[u] = ld_sc2(rsws, o);
          ca[u] = ld_sc2(rsws, o + 320 * 8);
        }
#pragma unroll
        for (int u = 0; u < 4; ++u) fl[u] = ld_sc2(rsws, WS_INIT + (unsigned)(u * 64 + lane) * 8);
#pragma unroll
        for (int u = 0; u < 5; ++u) ok = ok && sa[u].y == T && ca[u].y == T;
#pragma unroll
        for (int u = 0; u < 4; ++u) ok = ok && fl[u].x == T && fl[u].y == ~T;
        if (__all(ok) || spins > SPIN_G) break;
        __builtin_amdgcn_s_sleep(PSLEEP);
      }
      STAMP2(11);
      asm volatile("s_waitcnt lgkmcnt(0)" ::: "memory");
#pragma unroll
      for (int u = 0; u < 5; ++u) {
        const int idx = u * 64 + lane, j = idx / 40, m = idx - 40 * j;
        const float mean = ((LAS float*)(L + L_STAT))[2 * j], den = ((LAS float*)(L + L_STAT))[2 * j + 1];
        const float n0 = rbf(rbf(blo(xh[u]) - mean) * den), n1 = rbf(rbf(bhi(xh[u]) - mean) * den);
        const float h0 = rbf(n0 * rbf(1.f + blo(ca[u].x))) + blo(sa[u].x), h1 = rbf(n1 * rbf(1.f + bhi(ca[u].x))) + bhi(sa[u].x);
        *(LAS unsigned*)(L + L_HS + j * 176 + m * 4) = pk2(h0, h1);
      }
    }
    STAMP(4);
    {
      unsigned spins = 0;
      while (flags[2] < (unsigned)NSW) { __builtin_amdgcn_s_sleep(1); if (++spins > SPIN_L) break; }
      CFENCE();
    }
    STAMP2(12);
    {
      f4 qa[8];
#pragma unroll
      for (int t = 0; t < 8; ++t) qa[t] = f4{0, 0, 0, 0};
#pragma unroll
      for (int s = 0; s < 5; ++s) {
        const v2u bb = *(const LAS v2u*)(L + L_HS + (lane & 7) * 176 + s * 32 + g * 8);
        const int row = 16 * s + 4 * g + tq;
        const LAS unsigned char* rp = L + L_QT + row * 256 + (tp & 1) * 8;
        const int sw = 2 * (row & 7);
#pragma unroll
        for (int t = 0; t < 8; ++t) qa[t] = mfma16(trread(rp + (((2 * t + (tp >> 1)) ^ sw) * 16)), bb, qa[t]);
      }
      if (i16 < 8 && !ABL_G2) {
        const unsigned o = WS_QP + (unsigned)(((h * 8 + b) * 8 + i16) * 128 + 4 * g) * 4;
#pragma unroll
        for (int t = 0; t < 8; ++t) st_sc(tg4(qa[t], t10), rsws, o + t * 64);
      }
    }
    STAMP(5);
    const unsigned tpub = (unsigned)__builtin_amdgcn_s_memrealtime();
    {
      v4u qv[4];
      if (D2) { while ((unsigned)__builtin_amdgcn_s_memrealtime() - tpub < D2) __builtin_amdgcn_s_sleep(2); }
      STAMP2(13);
      if (ABL_G2) { for (int u = 0; u < 4; ++u) qv[u] = v4u{t10, t10, t10, t10}; }
      for (unsigned spins = 0; !ABL_G2; ++spins) {
        CFENCE();
        bool ok = true;
#pragma unroll
        for (int u = 0; u < 4; ++u) qv[u] = ld_sc(rsws, WS_QP + (unsigned)(((h * 8 + 2 * u + (lane >> 5)) * 8 + b) * 128 + 4 * (lane & 31)) * 4);
#pragma unroll
        for (int u = 0; u < 4; ++u) ok = ok && tagged4(qv[u], t10);
        if (__all(ok) || spins > SPIN_G) break;
        __builtin_amdgcn_s_sleep(PSLEEP);
      }
      f4 qs;
#pragma unroll
      for (int k = 0; k < 4; ++k) qs[k] = x32_sum((untg(qv[0][k]) + untg(qv[1][k])) + (untg(qv[2][k]) + untg(qv[3][k])));
      if (lane < 32) *(LAS v2u*)(L + L_Q + lane * 8) = v2u{pk2(qs[0], qs[1]), pk2(qs[2], qs[3])};
      asm volatile("s_waitcnt lgkmcnt(0)" ::: "memory");
      if (lane == 0) flags[1] = 1u;
    }
    STAMP(6);
    if (!ABL_G3) {
      const unsigned go = WS_MODG + (unsigned)(b * 1920 + (lane < 10 ? 640 + 10 * h + lane : 1600 + 10 * h + (lane < 20 ? lane - 10 : 0))) * 8;
      const unsigned xv = ((const GAS unsigned*)p.x)[b * 320 + 10 * h + (lane < 10 ? lane : 0)];
      v2u gv;
      for (unsigned spins = 0;; ++spins) {
        CFENCE();
        gv = ld_sc2(rsws, go);
        if (__all(gv.y == T) || spins > SPIN_G) break;
        __builtin_amdgcn_s_sleep(32);
      }
      if (lane < 20) *(LAS unsigned*)(L + L_VEC + lane * 4) = gv.x;
      if (lane < 10) *(LAS unsigned*)(L + L_VEC + 80 + lane * 4) = xv;
      if (is_mlp) {
        for (int j = 0; j < 8; ++j) {
          v4u cv[5];
          for (unsigned spins = 0;; ++spins) {
            CFENCE();
            bool ok = true;
#pragma unroll
            for (int u = 0; u < 5; ++u) cv[u] = ld_sc(rsws, WS_MODG + (unsigned)(j * 1920 + 960) * 8 + (unsigned)(u * 64 + lane) * 16);
#pragma unroll
            for (int u = 0; u < 5; ++u) ok = ok && cv[u].y == T && cv[u].w == T;
            if (__all(ok) || spins > SPIN_G) break;
            __builtin_amdgcn_s_sleep(32);
          }
#pragma unroll
          for (int u = 0; u < 5; ++u) {
            const int pc = u * 64 + lane;
            const unsigned o = (pc < 160 ? 10240u + (unsigned)(j * 320 + 2 * pc) * 4 : (unsigned)(j * 320 + 2 * (pc - 160)) * 4);
            const v2u sc1 = {pk2(1.f + blo(cv[u].x), 1.f + bhi(cv[u].x)), pk2(1.f + blo(cv[u].z), 1.f + bhi(cv[u].z))};
            *(LAS v2u*)(L + L_CS + o) = pc < 160 ? v2u{cv[u].x, cv[u].z} : sc1;
          }
        }
      }
    }
    STAMP(7);
    {
      unsigned spins = 0;
      while (flags[3] < (unsigned)NSW) { __builtin_amdgcn_s_sleep(1); if (++spins > SPIN_L) break; }
      CFENCE();
    }
    {
      const rsrc_t rswo = mkrs(p.wo + (size_t)h * DH * DM, DH * DM * 2);
      for (int n = 0; n < 20; ++n) {
        const int ci = n * 64 + lane, row = ci / 10, ch = ci - 10 * row;
        dma16(rswo, lds0 + L_T1 + n * 1024, (unsigned)(row * 1280 + b * 160 + ch * 16), 0);
      }
    }
    }
  }

  bar();
  STAMP(8);
  if (ABL_CTRL || ABL_TAIL) {
    STAMP(15);
    return;
  }
  const unsigned t10 = flags[4] & 0x3FFu;
  auto until = [&](unsigned t0, unsigned ticks) { while ((unsigned)__builtin_amdgcn_s_memrealtime() - t0 < ticks) __builtin_amdgcn_s_sleep(1); };
  if (w == 0) {
    if (lane < 32) {
      const LAS float* mr = (const LAS float*)(L + L_MRG);
      float M = mr[0];
#pragma unroll
      for (int u = 1; u < NSW; ++u) M = fmaxf(M, mr[u * 132]);
      constexpr float C2 = 0.08838834764831845f * 1.4426950408889634f;
      float Lsum = 0.f;
      f4 o = {0, 0, 0, 0};
#pragma unroll
      for (int u = 0; u < NSW; ++u) {
        const float f = __builtin_amdgcn_exp2f((mr[u * 132] - M) * C2);
        Lsum = fmaf(f, mr[u * 132 + 1], Lsum);
        o += f * *(const LAS f4*)(mr + u * 132 + 4 + 4 * lane);
      }
      const float inv = rcp(Lsum);
      st_sc2(v2u{canonu(pk2(o[0] * inv, o[1] * inv)), canonu(pk2(o[2] * inv, o[3] * inv))}, rsws, WS_OX + (unsigned)(h * 8 + b) * 256 + lane * 8);
    }
  } else if (w < 3) {
    const int t = tid - 64;
    __builtin_amdgcn_s_sleep(TD_A);
    v4u v;
    for (unsigned spins = 0;; ++spins) {
      CFENCE();
      v = ld_sc(rsws, WS_OX + (unsigned)h * 2048 + t * 16);
      if (__all(valid4(v)) || spins > SPIN_G) break;
      __builtin_amdgcn_s_sleep(TS);
    }
    *(LAS v4u*)(L + L_OS + (t >> 4) * 272 + (t & 15) * 16) = v;
  } else if (w == 7) {
    VMWAIT(0);
  }
  bar();
  const unsigned tB2 = (unsigned)__builtin_amdgcn_s_memrealtime();
  STAMP(9);
  if (w < 4) {
    const LAS unsigned char* bp = L + L_OS + (lane & 7) * 272 + g * 16;
    v4u bb[4];
#pragma unroll
    for (int s = 0; s < 4; ++s) bb[s] = *(const LAS v4u*)(bp + s * 64);
    for (int t = w; t < 5; t += 4) {
      f4 y = {0, 0, 0, 0};
      const LAS unsigned char* ap = L + L_T1 + (8 * g + tq) * 160 + (16 * t + 4 * tp) * 2;
#pragma unroll
      for (int s = 0; s < 4; ++s) {
        const v2u x0 = trread(ap + s * 32 * 160), x1 = trread(ap + s * 32 * 160 + 4 * 160);
        y = mfma32(v4u{x0.x, x0.y, x1.x, x1.y}, bb[s], y);
      }
      const int cc = 4 * t + g;
      if (i16 < 8) st_sc(tg4(y, t10), rsws,
                         WS_T1P + (unsigned)((((i16 * 32 + 4 * b + cc / 5) * 32 + h) * 5 + cc % 5) * 16));
    }
  } else if (w < 7) {
    const int t = tid - 256;
    const bool act = t < 160;
    const unsigned o = WS_T1P + (unsigned)((b * 32 + h) * 160 + (act ? t : 0)) * 16;
    until(tB2, GB);
    v4u v;
    for (unsigned spins = 0;; ++spins) {
      CFENCE();
      v = ld_sc(rsws, o);
      if (__all(tagged4(v, t10)) || spins > SPIN_G) break;
      __builtin_amdgcn_s_sleep(TS);
    }
    if (act) *(LAS f4*)(L + L_T4P + t * 16) = f4{untg(v.x), untg(v.y), untg(v.z), untg(v.w)};
  }
  bar();
  if (w == 4) {
    const int e = lane >> 3, c = lane & 7;
    f4 sm = {0, 0, 0, 0};
    if (c < 5) {
#pragma unroll
      for (int u = 0; u < 4; ++u) sm += *(const LAS f4*)(L + L_T4P + ((4 * e + u) * 5 + c) * 16);
    }
#pragma unroll
    for (int k = 0; k < 4; ++k) { float x = sm[k]; x += dpp<0x128>(x); sm[k] = xrow_sum(x); }
    if (lane < 5) {
      const LAS unsigned* vec = (const LAS unsigned*)(L + L_VEC);
      const unsigned g0 = vec[2 * lane], g1 = vec[2 * lane + 1], x0 = vec[20 + 2 * lane], x1 = vec[20 + 2 * lane + 1];
      const float r0 = rbf(blo(x0) + rbf(blo(g0) * rbf(sm[0]))), r1 = rbf(bhi(x0) + rbf(bhi(g0) * rbf(sm[1])));
      const float r2 = rbf(blo(x1) + rbf(blo(g1) * rbf(sm[2]))), r3 = rbf(bhi(x1) + rbf(bhi(g1) * rbf(sm[3])));
      st_sc2(v2u{canonu(pk2(r0, r1)), canonu(pk2(r2, r3))}, rsws, WS_X1B + (unsigned)(b * 320 + 10 * h + 2 * lane) * 4);
      *(LAS f4*)(L + L_X1O + lane * 16) = f4{r0, r1, r2, r3};
    }
  }
  bar();
  const unsigned tB = (unsigned)__builtin_amdgcn_s_memrealtime();
  STAMP(10);
  if (is_mlp) {
    {
      const unsigned o = WS_X1B + (unsigned)w * 1280 + lane * 4;
      unsigned cs[5], sh[5];
#pragma unroll
      for (int u = 0; u < 5; ++u) {
        cs[u] = *(const LAS unsigned*)(L + L_CS + (w * 320 + u * 64 + lane) * 4);
        sh[u] = *(const LAS unsigned*)(L + L_CS + 10240 + (w * 320 + u * 64 + lane) * 4);
      }
      until(tB, GC);
      unsigned xv[5];
      for (unsigned spins = 0;; ++spins) {
        CFENCE();
        bool ok = true;
#pragma unroll
        for (int u = 0; u < 5; ++u) xv[u] = __builtin_amdgcn_raw_buffer_load_b32(rsws, o + u * 256, 0, 16);
#pragma unroll
        for (int u = 0; u < 5; ++u) ok = ok && xv[u] != SENT;
        if (__all(ok) || spins > SPIN_G) break;
        __builtin_amdgcn_s_sleep(TS);
      }
      const float x00 = blo(rfl(xv[0]));
      float s1 = 0.f, s2 = 0.f;
#pragma unroll
      for (int u = 0; u < 5; ++u) {
        const float d0 = blo(xv[u]) - x00, d1 = bhi(xv[u]) - x00;
        s1 += d0 + d1; s2 = fmaf(d0, d0, fmaf(d1, d1, s2));
      }
      s1 = row16_sum(s1); s2 = row16_sum(s2);
      s1 = xrow_sum(s1) * (1.f / DM); s2 = xrow_sum(s2) * (1.f / DM);
      const float rs = __builtin_amdgcn_rsqf(fmaxf(s2 - s1 * s1, 0.f) + 1e-5f), mr = -(x00 + s1) * rs;
#pragma unroll
      for (int u = 0; u < 5; ++u) {
        const float n0 = fmaf(blo(xv[u]), rs, mr), n1 = fmaf(bhi(xv[u]), rs, mr);
        *(LAS unsigned*)(L + L_H2 + w * 1296 + (u * 64 + lane) * 4) = pk2(fmaf(n0, blo(cs[u]), blo(sh[u])), fmaf(n1, bhi(cs[u]), bhi(sh[u])));
      }
    }
    bar();
    STAMP(11);
    {
      const int t = w & 1, kq = w >> 1;
      f4 a = {0, 0, 0, 0};
      const LAS unsigned char* ap = L + L_RT + (160 * kq + 8 * g + tq) * 64 + t * 32 + tp * 8;
      const LAS unsigned char* bp = L + L_H2 + (lane & 7) * 1296 + (160 * kq + 8 * g) * 2;
#pragma unroll
      for (int s = 0; s < 5; ++s) {
        const v4u bb = *(const LAS v4u*)(bp + s * 64);
        const v2u x0 = trread(ap + s * 2048), x1 = trread(ap + s * 2048 + 256);
        a = mfma32(v4u{x0.x, x0.y, x1.x, x1.y}, bb, a);
      }
      if (i16 < 8) *(LAS f4*)(L + L_T4P + (((kq * 2 + t) * 4 + g) * 8 + i16) * 16) = a;
    }
    bar();
    if (tid < 128) {
      const int j = tid >> 4, c = 2 * (tid & 15), t = c >> 4, gg = (c & 15) >> 2, r = c & 3;
      float z0 = 0.f, z1 = 0.f;
#pragma unroll
      for (int kq = 0; kq < 4; ++kq) {
        const LAS float* z = (const LAS float*)(L + L_T4P + (((kq * 2 + t) * 4 + gg) * 8 + j) * 16) + r;
        z0 += z[0]; z1 += z[1];
      }
      *(LAS unsigned*)(L + L_H1 + j * 80 + c * 2) = pk2(silu(rbf(z0)), silu(rbf(z1)));
    }
    bar();
    STAMP(12);
    {
      const v4u bb = *(const LAS v4u*)(L + L_H1 + (lane & 7) * 80 + g * 16);
      const int r0 = 8 * g + tq;
      const LAS unsigned char* ap = L + L_W2 + r0 * 1280 + (tp & 1) * 8;
      const int f = 2 * (r0 & 3);
#pragma unroll
      for (int tt = 0; tt < 5; ++tt) {
        const int ch = (2 * (5 * w + tt) + (tp >> 1)) ^ f;
        const v2u x0 = trread(ap + ch * 16), x1 = trread(ap + 4 * 1280 + ch * 16);
        const f4 y = mfma32(v4u{x0.x, x0.y, x1.x, x1.y}, bb, f4{0, 0, 0, 0});
        if (i16 < 8) *(LAS v4u*)(L + L_RT + i16 * 2576 + (16 * (5 * w + tt) + 4 * g) * 4) = tg4(y, t10);
      }
    }
    bar();
    {
#pragma unroll
      for (int u = 0; u < 3; ++u) {
        const int pc = u * 512 + tid, j = pc / 160, c = pc - 160 * j;
        if (pc < 1280) st_sc(*(const LAS v4u*)(L + L_RT + j * 2576 + c * 16), rsws, WS_T5P + (unsigned)mm * 20480 + (unsigned)pc * 16);
      }
    }
  }
  const unsigned tD = (unsigned)__builtin_amdgcn_s_memrealtime();
  STAMP(13);
  if (w < 5) {
    const bool act = tid < NMLP * 5;
    const int dm = act ? tid / 5 : 0, dc = act ? tid - 5 * dm : 0;
    const unsigned o = WS_T5P + (unsigned)((dm * 8 + b) * 640 + 20 * h + 4 * dc) * 4;
    if (is_mlp) until(tD, GD1); else until(tB, GD2);
    v4u v;
    for (unsigned spins = 0;; ++spins) {
      CFENCE();
      v = ld_sc(rsws, o);
      if (__all(tagged4(v, t10)) || spins > SPIN_G) break;
      __builtin_amdgcn_s_sleep(TS);
    }
    if (act) *(LAS f4*)(L + L_W2 + tid * 16) = f4{untg(v.x), untg(v.y), untg(v.z), untg(v.w)};
  }
  bar();
  if (w == 0) {
    const int e = lane / 5, c = lane - 5 * e;
    f4 sm = {0, 0, 0, 0};
    if (lane < 45) {
#pragma unroll
      for (int u = 0; u < 6; ++u) sm += *(const LAS f4*)(L + L_W2 + ((6 * e + u) * 5 + c) * 16);
      *(LAS f4*)(L + L_W2 + 8192 + lane * 16) = sm;
    }
    asm volatile("s_waitcnt lgkmcnt(0)" ::: "memory");
    if (lane < 5) {
      sm = f4{0, 0, 0, 0};
#pragma unroll
      for (int u = 0; u < 9; ++u) sm += *(const LAS f4*)(L + L_W2 + 8192 + (u * 5 + lane) * 16);
      const LAS unsigned* vec = (const LAS unsigned*)(L + L_VEC);
      const unsigned g0 = vec[10 + 2 * lane], g1 = vec[10 + 2 * lane + 1];
      const f4 x1 = *(const LAS f4*)(L + L_X1O + lane * 16);
      const float r0 = x1[0] + rbf(blo(g0) * rbf(sm[0])), r1 = x1[1] + rbf(bhi(g0) * rbf(sm[1]));
      const float r2 = x1[2] + rbf(blo(g1) * rbf(sm[2])), r3 = x1[3] + rbf(bhi(g1) * rbf(sm[3]));
      *(GAS v2u*)(p.out + b * DM + 20 * h + 4 * lane) = v2u{pk2(r0, r1), pk2(r2, r3)};
    }
  }
  if (id == 0 && tid == 448) {
    const unsigned E = __builtin_amdgcn_raw_buffer_load_b32(rsws, WS_EPOCH, 0, 16);
    __builtin_amdgcn_raw_buffer_store_b32(E + 1u, rsws, WS_EPOCH, 0, 16);
  }
  STAMP(15);
}

}

extern "C" void kernel_launch(void* const* d_in, const int* in_sizes, int n_in, void* d_out, int out_size, void* d_ws,
                              size_t ws_size, hipStream_t stream) {
  Params p;
  p.x = (const u16*)d_in[0];
  p.kc = (const u16*)d_in[1];
  p.vc = (const u16*)d_in[2];
  p.wq = (const u16*)d_in[3];
  p.wo = (const u16*)d_in[4];
  p.w1 = (const u16*)d_in[6];
  p.w2 = (const u16*)d_in[7];
  p.c = (const u16*)d_in[8];
  p.wmod = (const u16*)d_in[9];
  p.out = (u16*)d_out;
  p.ws = (unsigned char*)d_ws;
  hipLaunchKernelGGL(fused_block, dim3(256), dim3(512), 0, stream, p);
}
```

```cpp
#include <hip/hip_runtime.h>
#include <stdint.h>

#define DIAG 0

namespace {

typedef unsigned short u16;
typedef unsigned v4u __attribute__((ext_vector_type(4)));
typedef unsigned v2u __attribute__((ext_vector_type(2)));
typedef float f4 __attribute__((ext_vector_type(4)));
typedef short s4 __attribute__((ext_vector_type(4)));
typedef short s8 __attribute__((ext_vector_type(8)));
typedef __bf16 bf2 __attribute__((ext_vector_type(2)));
typedef __bf16 bf8 __attribute__((ext_vector_type(8)));
typedef __amdgpu_buffer_rsrc_t rsrc_t;
#define LAS __attribute__((address_space(3)))
#define GAS __attribute__((address_space(1)))
#define DI __device__ __forceinline__

constexpr int NB = 8, DM = 640, NH = 32, DH = 128, SK = 4096, WIN = 1024, FF = 1728;
constexpr int NSW = 7;
constexpr int NL = 37;
#define TD_A 8
#define TD_B 8
#define TD_C 16
#define TD_D 8
#define TD_D2 4
#define TS 6
#define ABL_CTRL 0
#define ABL_SLOT 0
#define ABL_ROLE 0
#define ABL_SENT 0
#define LEAD 3
#define MP0 12
#define MPACE 11
#define DELTA 0
#define HEAVY 1
#define ABL_G1 0
#define ABL_G2 0
#define ABL_G3 0
#define ABL_TAIL 0
#define GB 40
#define GC 40
#define GD1 40
#define GD2 190
#define WARM 1
#define INDIC 1
#define ABL_WO 0
#define ABL_W12 0
#define D1 600
#define D2 100
#define PSLEEP 32
#define DPACE 15
#define PACE_ 3
constexpr int PACE = PACE_;
#define NPRE_ 16
constexpr int NPRE = NPRE_;

constexpr int NMLP = 54;
constexpr unsigned WS_EPOCH = 0;
constexpr unsigned WS_INIT = 4096;
constexpr unsigned WS_MODG = 8192;
constexpr unsigned WS_QP = WS_MODG + NB * 1920 * 8;
constexpr unsigned WS_OX = WS_QP + NH * 8 * 8 * 128 * 4;
constexpr unsigned WS_T1P = WS_OX + NH * 8 * 256;
constexpr unsigned WS_X1B = WS_T1P + NB * NH * DM * 4;
constexpr unsigned WS_T5P = WS_X1B + NB * 320 * 4;
constexpr unsigned WS_END = WS_T5P + NMLP * NB * DM * 4;
constexpr unsigned WS_DIAG = 4u << 20;
constexpr unsigned SENT = 0xFFFFFFFFu;
static_assert(WS_END <= WS_DIAG, "workspace map");

constexpr unsigned L_RT = 0;
constexpr unsigned L_QT = 40960;
constexpr unsigned L_W2 = 61440;
constexpr unsigned L_T1 = 102400;
constexpr unsigned L_CS = 122880;
constexpr unsigned L_MISC = 143360;
constexpr unsigned L_FLAG = L_MISC;
constexpr unsigned L_Q = L_MISC + 64;
constexpr unsigned L_STAT = L_Q + 256;
constexpr unsigned L_VEC = L_STAT + 64;
constexpr unsigned L_X1O = L_VEC + 128;
constexpr unsigned L_SC = L_X1O + 128;
constexpr unsigned L_H2 = L_SC;
constexpr unsigned L_HS = L_SC + 8 * 1296;
constexpr unsigned L_MRG = L_HS + 8 * 176;
constexpr unsigned L_OS = L_MRG + 7 * 132 * 4;
constexpr unsigned L_T4P = L_MRG;
constexpr unsigned L_H1 = L_T4P + 4096;
constexpr unsigned L_END = 163840;
static_assert(L_OS + 8 * 272 <= L_END && L_H1 + 8 * 80 <= L_OS + 8 * 272, "LDS map");
static_assert((L_SC % 16) == 0 && (L_HS % 16) == 0 && (L_MRG % 16) == 0 && (L_OS % 16) == 0 && (L_H1 % 16) == 0, "LDS alignment");

constexpr unsigned SPIN_G = 1u << 16;
constexpr unsigned SPIN_L = 1u << 22;

struct Params {
  const u16 *x, *kc, *vc, *wq, *wo, *w1, *w2, *c, *wmod;
  u16* out;
  unsigned char* ws;
};

DI unsigned rfl(unsigned x) { return __builtin_amdgcn_readfirstlane(x); }
DI float asf(unsigned u) { return __uint_as_float(u); }
DI unsigned asu(float f) { return __float_as_uint(f); }
DI unsigned pk2(float lo, float hi) { bf2 v = {(__bf16)lo, (__bf16)hi}; return __builtin_bit_cast(unsigned, v); }
DI float rbf(float f) { return asf(pk2(f, 0.f) << 16); }
DI float blo(unsigned u) { return asf(u << 16); }
DI float bhi(unsigned u) { return asf(u & 0xffff0000u); }
DI float dot2(unsigned a, unsigned b, float c) {
  return __builtin_amdgcn_fdot2_f32_bf16(__builtin_bit_cast(bf2, a), __builtin_bit_cast(bf2, b), c, false);
}
template <int CTRL> DI float dpp(float x) {
  return __builtin_bit_cast(float, __builtin_amdgcn_mov_dpp(__builtin_bit_cast(int, x), CTRL, 0xf, 0xf, true));
}
DI float row16_sum(float x) { x += dpp<0x128>(x); x += dpp<0x124>(x); x += dpp<0x122>(x); x += dpp<0x121>(x); return x; }
DI float oct_sum(float x) { x += dpp<0xB1>(x); x += dpp<0x4E>(x); x += dpp<0x141>(x); return x; }
DI float x16_sum(float x) {
  auto s = __builtin_amdgcn_permlane16_swap(asu(x), asu(x), false, false);
  return asf(s[0]) + asf(s[1]);
}
DI float x32_sum(float x) {
  auto t = __builtin_amdgcn_permlane32_swap(asu(x), asu(x), false, false);
  return asf(t[0]) + asf(t[1]);
}
DI float xrow_sum(float x) { return x32_sum(x16_sum(x)); }
DI float xrow_max(float x) {
  auto s = __builtin_amdgcn_permlane16_swap(asu(x), asu(x), false, false);
  x = fmaxf(asf(s[0]), asf(s[1]));
  auto t = __builtin_amdgcn_permlane32_swap(asu(x), asu(x), false, false);
  return fmaxf(asf(t[0]), asf(t[1]));
}
DI float wave_sum(float x) { return xrow_sum(row16_sum(x)); }
DI f4 mfma32(v4u a, v4u b, f4 c) {
  return __builtin_amdgcn_mfma_f32_16x16x32_bf16(__builtin_bit_cast(bf8, a), __builtin_bit_cast(bf8, b), c, 0, 0, 0);
}
DI f4 mfma16(v2u a, v2u b, f4 c) {
  return __builtin_amdgcn_mfma_f32_16x16x16bf16_1k(__builtin_bit_cast(s4, a), __builtin_bit_cast(s4, b), c, 0, 0, 0);
}
DI v2u trread(const LAS unsigned char* p) {
  return __builtin_bit_cast(v2u, __builtin_amdgcn_ds_read_tr16_b64_v4i16((LAS s4*)p));
}
DI rsrc_t mkrs(const void* p, unsigned bytes) { return __builtin_amdgcn_make_buffer_rsrc((void*)p, 0, (int)bytes, 0x00020000); }
DI v4u ld_nt(rsrc_t r, unsigned vo, unsigned so) { return __builtin_amdgcn_raw_buffer_load_b128(r, vo, so, 2); }
#define KAUX 2
#define VAUX 2
DI v4u ld_k(rsrc_t r, unsigned vo, unsigned so) { return __builtin_amdgcn_raw_buffer_load_b128(r, vo, so, KAUX); }
DI v4u ld_v(rsrc_t r, unsigned vo, unsigned so) { return __builtin_amdgcn_raw_buffer_load_b128(r, vo, so, VAUX); }
DI v4u ld_sc(rsrc_t r, unsigned vo) { return __builtin_amdgcn_raw_buffer_load_b128(r, vo, 0, 16); }
DI v2u ld_sc2(rsrc_t r, unsigned vo) { return __builtin_amdgcn_raw_buffer_load_b64(r, vo, 0, 16); }
DI void st_sc(v4u v, rsrc_t r, unsigned vo) { __builtin_amdgcn_raw_buffer_store_b128(v, r, vo, 0, 16); }
DI void st_sc2(v2u v, rsrc_t r, unsigned vo) { __builtin_amdgcn_raw_buffer_store_b64(v, r, vo, 0, 16); }
DI unsigned canonu(unsigned u) { return u == 0xFFFFFFFFu ? 0x7FFF7FFFu : u; }
DI void dma16(rsrc_t r, unsigned lds, unsigned vo, unsigned so) {
  unsigned keep;
  asm volatile("s_nop 4\n\ts_mov_b32 %0, m0\n\ts_mov_b32 m0, %1\n\ts_nop 0\n\tbuffer_load_dwordx4 %2, %3, %4 offen nt lds\n\ts_mov_b32 m0, %0"
               : "=&s"(keep) : "s"(lds), "v"(vo), "s"(r), "s"(so) : "memory");
}
template <int N> DI void vmwait() { asm volatile("s_waitcnt vmcnt(%0)" :: "n"(N) : "memory"); }
#define VMWAIT(n) vmwait<(n)>()
DI void bar() { asm volatile("s_waitcnt lgkmcnt(0)\n\ts_barrier" ::: "memory"); }
#define CFENCE() asm volatile("" ::: "memory")
#define SCHED() __builtin_amdgcn_sched_barrier(0)
DI unsigned canon(float f) { unsigned u = asu(f); return u == SENT ? 0x7FFFFFFFu : u; }
DI unsigned tg(float f, unsigned t10) { return ((asu(f) + 0x200u) & ~0x3FFu) | t10; }
DI v4u tg4(f4 v, unsigned t10) { return v4u{tg(v[0], t10), tg(v[1], t10), tg(v[2], t10), tg(v[3], t10)}; }
DI bool tagged4(v4u v, unsigned t10) { return (((v.x ^ t10) | (v.y ^ t10) | (v.z ^ t10) | (v.w ^ t10)) & 0x3FFu) == 0u; }
DI float untg(unsigned u) { return asf(u & ~0x3FFu); }
DI bool valid4(v4u v) { return v.x != SENT && v.y != SENT && v.z != SENT && v.w != SENT; }
DI float rcp(float x) { return __builtin_amdgcn_rcpf(x); }
DI float silu(float v) { return v * rcp(1.f + __builtin_amdgcn_exp2f(v * -1.4426950408889634f)); }

#define STAMP(k) do { } while (0)
#define STAMP2(k) do { } while (0)

__global__ void __launch_bounds__(512, 2) fused_block(Params p_) {
  unsigned long long tearly;
  const __attribute__((address_space(4))) Params* pp = (const __attribute__((address_space(4))) Params*)__builtin_amdgcn_kernarg_segment_ptr();
  asm volatile("s_memrealtime %0\n\ts_waitcnt lgkmcnt(0)" : "=s"(tearly), "+s"(pp) :: "memory");
  Params p;
  p.x = pp->x; p.kc = pp->kc; p.vc = pp->vc; p.wq = pp->wq; p.wo = pp->wo; p.w1 = pp->w1; p.w2 = pp->w2; p.c = pp->c; p.wmod = pp->wmod; p.out = pp->out; p.ws = pp->ws;
  __shared__ __attribute__((aligned(16))) unsigned char smem[L_END];
  LAS unsigned char* L = (LAS unsigned char*)smem;
  const unsigned lds0 = (unsigned)(uintptr_t)L;
  const int tid = threadIdx.x, lane = tid & 63;
  const int w = (int)rfl(tid >> 6);
  const int id = blockIdx.x, xcd = id & 7, sl = id >> 3;
  const int h = xcd * 4 + (sl >> 3), b = sl & 7;
  const int g = lane >> 4, i16 = lane & 15;
  const int tq = i16 >> 2, tp = lane & 3;
  const rsrc_t rsws = mkrs(p.ws, WS_END);
  volatile LAS unsigned* flags = (volatile LAS unsigned*)(L + L_FLAG);
  const int mpr = (sl >> 1) * 8 + xcd;
  const bool has_mod = !ABL_ROLE && sl < 16 && mpr < 60;
  const bool crit = has_mod && mpr < 20;
  const int mcol0 = (2 * mpr + (sl & 1)) * 32;
  const int fpr = ((sl - 16) >> 1) * 8 + xcd;
  const bool is_mlp = !ABL_ROLE && sl >= 16 && sl < 24 && fpr < 27;
  const int mm = 2 * fpr + (sl & 1);

  STAMP(0);
  const unsigned tstart = (unsigned)__builtin_amdgcn_s_memrealtime();
  auto sched = [&](int i, bool light) {
    if (DELTA && light) { while ((unsigned)__builtin_amdgcn_s_memrealtime() - tstart < (unsigned)(i * DELTA / 10)) __builtin_amdgcn_s_sleep(1); }
  };

  if (w < NSW) {
    const rsrc_t rsk = mkrs(p.kc + ((size_t)(b * NH + h) * SK + (SK - WIN)) * DH, WIN * DH * 2);
    const rsrc_t rsv = mkrs(p.vc + ((size_t)(b * NH + h) * SK + (SK - WIN)) * DH, WIN * DH * 2);
    const unsigned vo = (unsigned)(lane * 16 + w * 1024);
    v4u kr[NL], vr[NL], cr[2];
    if (has_mod && w < 5) {
      const GAS v4u* cp = (const GAS v4u*)p.c;
      cr[0] = cp[w * 128 + lane];
      cr[1] = cp[w * 128 + 64 + lane];
    }
    auto silu_c = [&]() {
      if (w < 5) {
#pragma unroll
        for (int u = 0; u < 2; ++u) {
          const int q = w * 128 + u * 64 + lane, j = q / 80, k8 = q - 80 * j;
          v4u o;
          o.x = pk2(silu(blo(cr[u].x)), silu(bhi(cr[u].x)));
          o.y = pk2(silu(blo(cr[u].y)), silu(bhi(cr[u].y)));
          o.z = pk2(silu(blo(cr[u].z)), silu(bhi(cr[u].z)));
          o.w = pk2(silu(blo(cr[u].w)), silu(bhi(cr[u].w)));
          *(LAS v4u*)(L + L_SC + j * 1296 + k8 * 16) = o;
        }
      }
    };
    const rsrc_t rsrt = mkrs(p.wmod, DM * 6 * DM * 2);
    const unsigned rt_vo = (unsigned)((lane >> 2) * 7680 + mcol0 * 2 + (lane & 3) * 16);
    auto dma_rt = [&](int m) { dma16(rsrt, lds0 + L_RT + m * 1024, rt_vo, (unsigned)m * 16u * 7680u); };
    if (!crit) {
#pragma unroll
      for (int i = 0; i < PACE; ++i) kr[i] = ld_k(rsk, vo, i * 7168);
    }
    if (has_mod) { for (int m = w; m < 40; m += NSW) dma_rt(m); }
    SCHED();
    bar();
    if (has_mod) {
      silu_c();
      VMWAIT(0);
      asm volatile("s_waitcnt lgkmcnt(0)" ::: "memory");
      if (lane == 0) __hip_atomic_fetch_add((LAS unsigned*)(L + L_FLAG), 1u, __ATOMIC_RELAXED, __HIP_MEMORY_SCOPE_WORKGROUP);
    }
    {
      const rsrc_t rsq = mkrs(p.wq, DM * NH * DH * 2);
      for (int n = w; n < 20; n += NSW) {
        const int rl = 4 * n + g;
        dma16(rsq, lds0 + L_QT + n * 1024, (unsigned)((80 * b + rl) * 8192 + h * 256 + ((i16 ^ (2 * (rl & 7))) * 16)), 0);
      }
    }
    if (crit) {
#pragma unroll
      for (int i = 0; i < PACE; ++i) kr[i] = ld_k(rsk, vo, i * 7168);
    }
    SCHED();
    const rsrc_t rsw1 = mkrs(p.w1, DM * FF * 2);
    const rsrc_t rsw2 = mkrs(p.w2 + (size_t)(is_mlp ? mm : 0) * 32 * DM, 32 * DM * 2);
    auto slot = [&](int s) {
      const int n = 7 * s + w;
      if (n < 40) {
        dma16(rsw1, lds0 + L_RT + n * 1024, (unsigned)((lane >> 2) * (FF * 2) + mm * 64 + (lane & 3) * 16), (unsigned)n * 16u * (FF * 2));
      } else if (n < 80) {
        const int ci = (n - 40) * 64 + lane, row = ci / 80, pos = ci - 80 * row;
        dma16(rsw2, lds0 + L_W2 + (n - 40) * 1024, (unsigned)(row * 1280 + (pos ^ (2 * (row & 3))) * 16), 0);
      }
    };
    const bool heavy = HEAVY == 1 ? (is_mlp || has_mod) : HEAVY == 2 ? is_mlp : false;
#pragma unroll
    for (int i = PACE; i < NL + NPRE; ++i) {
      sched(i, !is_mlp);
      if (heavy) VMWAIT(PACE); else VMWAIT(PACE - 1);
      if (i < NL) kr[i] = ld_k(rsk, vo, i * 7168); else vr[i - NL] = ld_v(rsv, vo, (i - NL) * 7168);
      SCHED();
      if (i == PACE + 4) {
        asm volatile("s_waitcnt lgkmcnt(0)" ::: "memory");
        if (lane == 0) __hip_atomic_fetch_add((LAS unsigned*)(L + L_FLAG) + 2, 1u, __ATOMIC_RELAXED, __HIP_MEMORY_SCOPE_WORKGROUP);
        STAMP(1);
      }
      if (i >= 7 && (i % 4) == 3 && (i - 7) / 4 < 12) { if (is_mlp) slot((i - 7) / 4); SCHED(); }
    }
    STAMP(2);
    {
      unsigned spins = 0;
      while (flags[1] == 0u) { __builtin_amdgcn_s_sleep(1); if (++spins > SPIN_L) break; }
    }
    CFENCE();
    STAMP(3);
    const v4u q4 = *(const LAS v4u*)(L + L_Q + i16 * 16);
    float s[NL];
#pragma unroll
    for (int i = 0; i < NL; ++i) {
      float d = dot2(kr[i].x, q4.x, 0.f);
      d = dot2(kr[i].y, q4.y, d); d = dot2(kr[i].z, q4.z, d); d = dot2(kr[i].w, q4.w, d);
      s[i] = row16_sum(d);
      if ((i & 3) == 3 && NPRE + (i >> 2) < NL) { SCHED(); sched(NL + NPRE + (i >> 2), !is_mlp); if (heavy) VMWAIT(PACE); else VMWAIT(PACE - 1); vr[NPRE + (i >> 2)] = ld_v(rsv, vo, (NPRE + (i >> 2)) * 7168); SCHED(); }
    }
    constexpr int NV1 = NPRE + (NL >> 2);
    if (w >= 4) s[NL - 1] = -INFINITY;
    float m = s[0];
#pragma unroll
    for (int i = 1; i < NL; ++i) m = fmaxf(m, s[i]);
    m = xrow_max(m);
    constexpr float C2 = 0.08838834764831845f * 1.4426950408889634f;
    const float mc = m * C2;
    unsigned pp[NL];
    float l = 0.f;
#pragma unroll
    for (int i = 0; i < NL; ++i) {
      pp[i] = pk2(0.f, __builtin_amdgcn_exp2f(fmaf(s[i], C2, -mc)));
      l += asf(pp[i]);
    }
    l = xrow_sum(l);
    STAMP(4);
    float acc[8];
#pragma unroll
    for (int k = 0; k < 8; ++k) acc[k] = 0.f;
#pragma unroll
    for (int i = 0; i < NL; ++i) {
      const unsigned p1 = pp[i], p0 = p1 >> 16;
      acc[0] = dot2(vr[i].x, p0, acc[0]); acc[1] = dot2(vr[i].x, p1, acc[1]);
      acc[2] = dot2(vr[i].y, p0, acc[2]); acc[3] = dot2(vr[i].y, p1, acc[3]);
      acc[4] = dot2(vr[i].z, p0, acc[4]); acc[5] = dot2(vr[i].z, p1, acc[5]);
      acc[6] = dot2(vr[i].w, p0, acc[6]); acc[7] = dot2(vr[i].w, p1, acc[7]);
      SCHED();
      if (NV1 + i < NL) { sched(NL + NV1 + i, !is_mlp); if (heavy) VMWAIT(PACE); else VMWAIT(PACE - 1); vr[NV1 + i] = ld_v(rsv, vo, (NV1 + i) * 7168); SCHED(); }
      if (NV1 + i == NL - 1 - LEAD) {
        if (lane == 0) __hip_atomic_fetch_add((LAS unsigned*)(L + L_FLAG) + 3, 1u, __ATOMIC_RELAXED, __HIP_MEMORY_SCOPE_WORKGROUP);
      }
    }
    STAMP(5);
#pragma unroll
    for (int k = 0; k < 8; ++k) acc[k] = xrow_sum(acc[k]);
    if (g == 0) {
      LAS float* mr = (LAS float*)(L + L_MRG) + w * 132;
      *(LAS f4*)(mr + 4 + 8 * i16) = f4{acc[0], acc[1], acc[2], acc[3]};
      *(LAS f4*)(mr + 8 + 8 * i16) = f4{acc[4], acc[5], acc[6], acc[7]};
      if (i16 == 0) { mr[0] = m; mr[1] = l; }
    }
  } else {
    __builtin_amdgcn_s_setprio(3);
    if (lane == 0) { flags[0] = 0u; flags[1] = 0u; flags[2] = 0u; flags[3] = 0u; }
    if (ABL_CTRL) {
      bar();
      while ((unsigned)__builtin_amdgcn_s_memrealtime() - tstart < 800) __builtin_amdgcn_s_sleep(8);
      if (lane == 0) flags[1] = 1u;
    } else {
    const unsigned E = __builtin_amdgcn_raw_buffer_load_b32(rsws, WS_EPOCH, 0, 16);
    unsigned warm = 0;
    {
      const int n = sl * 64 + lane, pi = n / 640, row = n - 640 * pi, pr = xcd + 8 * pi;
      if (WARM && pr < 20) warm = __builtin_amdgcn_raw_buffer_load_b32(mkrs(p.wmod, DM * 6 * DM * 2), (unsigned)(row * 7680 + pr * 128), 0, 2);
    }
    const GAS v4u* xp = (const GAS v4u*)p.x;
    v4u xr[10];
    const int sj = lane >> 3, sub = lane & 7;
#pragma unroll
    for (int u = 0; u < 10; ++u) xr[u] = xp[sj * 80 + sub + 8 * u];
    SCHED();
    bar();
    unsigned xh[5];
#pragma unroll
    for (int u = 0; u < 5; ++u) { const int idx = u * 64 + lane, j = idx / 40, m = idx - 40 * j; xh[u] = ((const GAS unsigned*)p.x)[j * 320 + 40 * b + m]; }
    if (!ABL_SENT) {
      const v4u sv = {SENT, SENT, SENT, SENT};
      if (lane < 16) st_sc(sv, rsws, WS_OX + (unsigned)(h * 8 + b) * 256 + lane * 16);
      if (lane < 5) st_sc2(v2u{SENT, SENT}, rsws, WS_X1B + (unsigned)(b * 320 + 10 * h) * 4 + lane * 8);
    }
    STAMP(1);
    {
      float sm = 0.f;
#pragma unroll
      for (int u = 0; u < 10; ++u)
        sm += ((blo(xr[u].x) + bhi(xr[u].x)) + (blo(xr[u].y) + bhi(xr[u].y))) + ((blo(xr[u].z) + bhi(xr[u].z)) + (blo(xr[u].w) + bhi(xr[u].w)));
      const float mean = oct_sum(sm) * (1.f / DM);
      float sq = 0.f;
#pragma unroll
      for (int u = 0; u < 10; ++u) {
#pragma unroll
        for (int k = 0; k < 4; ++k) { const float a = blo(xr[u][k]) - mean, c2 = bhi(xr[u][k]) - mean; sq = fmaf(a, a, sq); sq = fmaf(c2, c2, sq); }
      }
      const float var = oct_sum(sq) * (1.f / DM);
      if (sub == 0) {
        LAS float* st = (LAS float*)(L + L_STAT) + sj * 2;
        st[0] = rbf(mean);
        st[1] = rcp(rbf(__builtin_amdgcn_sqrtf(rbf(rbf(var) + 1e-5f))));
      }
    }
    VMWAIT(0);
    asm volatile("" :: "v"(warm));
    const unsigned T = rfl(E) + 1u;
    const unsigned t10 = T & 0x3FFu;
    if (lane == 0) flags[4] = T;
    if (lane == 0) st_sc2(v2u{T, ~T}, rsws, WS_INIT + (unsigned)(b * 32 + h) * 8);
    STAMP2(9);
    if (has_mod) {
      unsigned spins = 0;
      while (flags[0] < (unsigned)NSW) { __builtin_amdgcn_s_sleep(1); if (++spins > SPIN_L) break; }
      CFENCE();
      STAMP(2);
      f4 a0 = {0, 0, 0, 0}, a1 = {0, 0, 0, 0};
      const LAS unsigned char* ap = L + L_RT + (8 * g + tq) * 64 + tp * 8;
      const LAS unsigned char* bp = L + L_SC + (lane & 7) * 1296 + g * 16;
#pragma unroll 2
      for (int s = 0; s < 20; ++s) {
        const v4u bb = *(const LAS v4u*)(bp + s * 64);
        const v2u x0 = trread(ap + s * 2048), x1 = trread(ap + s * 2048 + 256);
        const v2u y0 = trread(ap + s * 2048 + 32), y1 = trread(ap + s * 2048 + 256 + 32);
        a0 = mfma32(v4u{x0.x, x0.y, x1.x, x1.y}, bb, a0);
        a1 = mfma32(v4u{y0.x, y0.y, y1.x, y1.y}, bb, a1);
      }
      if (i16 < 8) {
        const unsigned o = WS_MODG + (unsigned)(i16 * 1920 + (mcol0 + 4 * g) / 2) * 8;
        st_sc(v4u{pk2(a0[0], a0[1]), T, pk2(a0[2], a0[3]), T}, rsws, o);
        st_sc(v4u{pk2(a1[0], a1[1]), T, pk2(a1[2], a1[3]), T}, rsws, o + 64);
      }
    }
    STAMP(3);
    {
      v2u sa[5], ca[5], fl[4];
      if (D1) { while ((unsigned)__builtin_amdgcn_s_memrealtime() - tstart < D1) __builtin_amdgcn_s_sleep(2); }
      STAMP2(10);
      if (ABL_G1) { for (int u = 0; u < 5; ++u) { sa[u] = v2u{0u, T}; ca[u] = v2u{0u, T}; } }
      for (unsigned spins = 0; !ABL_G1; ++spins) {
        CFENCE();
        bool ok = true;
#pragma unroll
        for (int u = 0; u < 5; ++u) {
          const int idx = u * 64 + lane, j = idx / 40, m = idx - 40 * j;
          const unsigned o = WS_MODG + (unsigned)(j * 1920 + 40 * b + m) * 8;
          sa[u] = ld_sc2(rsws, o);
          ca[u] = ld_sc2(rsws, o + 320 * 8);
        }
#pragma unroll
        for (int u = 0; u < 4; ++u) fl[u] = ld_sc2(rsws, WS_INIT + (unsigned)(u * 64 + lane) * 8);
#pragma unroll
        for (int u = 0; u < 5; ++u) ok = ok && sa[u].y == T && ca[u].y == T;
#pragma unroll
        for (int u = 0; u < 4; ++u) ok = ok && fl[u].x == T && fl[u].y == ~T;
        if (__all(ok) || spins > SPIN_G) break;
        __builtin_amdgcn_s_sleep(PSLEEP);
      }
      STAMP2(11);
      asm volatile("s_waitcnt lgkmcnt(0)" ::: "memory");
#pragma unroll
      for (int u = 0; u < 5; ++u) {
        const int idx = u * 64 + lane, j = idx / 40, m = idx - 40 * j;
        const float mean = ((LAS float*)(L + L_STAT))[2 * j], den = ((LAS float*)(L + L_STAT))[2 * j + 1];
        const float n0 = rbf(rbf(blo(xh[u]) - mean) * den), n1 = rbf(rbf(bhi(xh[u]) - mean) * den);
        const float h0 = rbf(n0 * rbf(1.f + blo(ca[u].x))) + blo(sa[u].x), h1 = rbf(n1 * rbf(1.f + bhi(ca[u].x))) + bhi(sa[u].x);
        *(LAS unsigned*)(L + L_HS + j * 176 + m * 4) = pk2(h0, h1);
      }
    }
    STAMP(4);
    {
      unsigned spins = 0;
      while (flags[2] < (unsigned)NSW) { __builtin_amdgcn_s_sleep(1); if (++spins > SPIN_L) break; }
      CFENCE();
    }
    STAMP2(12);
    {
      f4 qa[8];
#pragma unroll
      for (int t = 0; t < 8; ++t) qa[t] = f4{0, 0, 0, 0};
#pragma unroll
      for (int s = 0; s < 5; ++s) {
        const v2u bb = *(const LAS v2u*)(L + L_HS + (lane & 7) * 176 + s * 32 + g * 8);
        const int row = 16 * s + 4 * g + tq;
        const LAS unsigned char* rp = L + L_QT + row * 256 + (tp & 1) * 8;
        const int sw = 2 * (row & 7);
#pragma unroll
        for (int t = 0; t < 8; ++t) qa[t] = mfma16(trread(rp + (((2 * t + (tp >> 1)) ^ sw) * 16)), bb, qa[t]);
      }
      if (i16 < 8 && !ABL_G2) {
        const unsigned o = WS_QP + (unsigned)(((h * 8 + b) * 8 + i16) * 128 + 4 * g) * 4;
#pragma unroll
        for (int t = 0; t < 8; ++t) st_sc(tg4(qa[t], t10), rsws, o + t * 64);
      }
    }
    STAMP(5);
    const unsigned tpub = (unsigned)__builtin_amdgcn_s_memrealtime();
    {
      v4u qv[4];
      if (D2) { while ((unsigned)__builtin_amdgcn_s_memrealtime() - tpub < D2) __builtin_amdgcn_s_sleep(2); }
      STAMP2(13);
      if (ABL_G2) { for (int u = 0; u < 4; ++u) qv[u] = v4u{t10, t10, t10, t10}; }
      for (unsigned spins = 0; !ABL_G2; ++spins) {
        CFENCE();
        bool ok = true;
#pragma unroll
        for (int u = 0; u < 4; ++u) qv[u] = ld_sc(rsws, WS_QP + (unsigned)(((h * 8 + 2 * u + (lane >> 5)) * 8 + b) * 128 + 4 * (lane & 31)) * 4);
#pragma unroll
        for (int u = 0; u < 4; ++u) ok = ok && tagged4(qv[u], t10);
        if (__all(ok) || spins > SPIN_G) break;
        __builtin_amdgcn_s_sleep(PSLEEP);
      }
      f4 qs;
#pragma unroll
      for (int k = 0; k < 4; ++k) qs[k] = x32_sum((untg(qv[0][k]) + untg(qv[1][k])) + (untg(qv[2][k]) + untg(qv[3][k])));
      if (lane < 32) *(LAS v2u*)(L + L_Q + lane * 8) = v2u{pk2(qs[0], qs[1]), pk2(qs[2], qs[3])};
      asm volatile("s_waitcnt lgkmcnt(0)" ::: "memory");
      if (lane == 0) flags[1] = 1u;
    }
    STAMP(6);
    if (!ABL_G3) {
      const unsigned go = WS_MODG + (unsigned)(b * 1920 + (lane < 10 ? 640 + 10 * h + lane : 1600 + 10 * h + (lane < 20 ? lane - 10 : 0))) * 8;
      const unsigned xv = ((const GAS unsigned*)p.x)[b * 320 + 10 * h + (lane < 10 ? lane : 0)];
      v2u gv;
      for (unsigned spins = 0;; ++spins) {
        CFENCE();
        gv = ld_sc2(rsws, go);
        if (__all(gv.y == T) || spins > SPIN_G) break;
        __builtin_amdgcn_s_sleep(32);
      }
      if (lane < 20) *(LAS unsigned*)(L + L_VEC + lane * 4) = gv.x;
      if (lane < 10) *(LAS unsigned*)(L + L_VEC + 80 + lane * 4) = xv;
      if (is_mlp) {
        for (int j = 0; j < 8; ++j) {
          v4u cv[5];
          for (unsigned spins = 0;; ++spins) {
            CFENCE();
            bool ok = true;
#pragma unroll
            for (int u = 0; u < 5; ++u) cv[u] = ld_sc(rsws, WS_MODG + (unsigned)(j * 1920 + 960) * 8 + (unsigned)(u * 64 + lane) * 16);
#pragma unroll
            for (int u = 0; u < 5; ++u) ok = ok && cv[u].y == T && cv[u].w == T;
            if (__all(ok) || spins > SPIN_G) break;
            __builtin_amdgcn_s_sleep(32);
          }
#pragma unroll
          for (int u = 0; u < 5; ++u) {
            const int pc = u * 64 + lane;
            const unsigned o = (pc < 160 ? 10240u + (unsigned)(j * 320 + 2 * pc) * 4 : (unsigned)(j * 320 + 2 * (pc - 160)) * 4);
            const v2u sc1 = {pk2(1.f + blo(cv[u].x), 1.f + bhi(cv[u].x)), pk2(1.f + blo(cv[u].z), 1.f + bhi(cv[u].z))};
            *(LAS v2u*)(L + L_CS + o) = pc < 160 ? v2u{cv[u].x, cv[u].z} : sc1;
          }
        }
      }
    }
    STAMP(7);
    {
      unsigned spins = 0;
      while (flags[3] < (unsigned)NSW) { __builtin_amdgcn_s_sleep(1); if (++spins > SPIN_L) break; }
      CFENCE();
    }
    {
      const rsrc_t rswo = mkrs(p.wo + (size_t)h * DH * DM, DH * DM * 2);
      for (int n = 0; n < 20; ++n) {
        const int ci = n * 64 + lane, row = ci / 10, ch = ci - 10 * row;
        dma16(rswo, lds0 + L_T1 + n * 1024, (unsigned)(row * 1280 + b * 160 + ch * 16), 0);
      }
    }
    }
  }

  bar();
  STAMP(8);
  if (ABL_CTRL || ABL_TAIL) {
    STAMP(15);
    return;
  }
  const unsigned t10 = flags[4] & 0x3FFu;
  auto until = [&](unsigned t0, unsigned ticks) { while ((unsigned)__builtin_amdgcn_s_memrealtime() - t0 < ticks) __builtin_amdgcn_s_sleep(1); };
  if (w == 0) {
    if (lane < 32) {
      const LAS float* mr = (const LAS float*)(L + L_MRG);
      float M = mr[0];
#pragma unroll
      for (int u = 1; u < NSW; ++u) M = fmaxf(M, mr[u * 132]);
      constexpr float C2 = 0.08838834764831845f * 1.4426950408889634f;
      float Lsum = 0.f;
      f4 o = {0, 0, 0, 0};
#pragma unroll
      for (int u = 0; u < NSW; ++u) {
        const float f = __builtin_amdgcn_exp2f((mr[u * 132] - M) * C2);
        Lsum = fmaf(f, mr[u * 132 + 1], Lsum);
        o += f * *(const LAS f4*)(mr + u * 132 + 4 + 4 * lane);
      }
      const float inv = rcp(Lsum);
      st_sc2(v2u{canonu(pk2(o[0] * inv, o[1] * inv)), canonu(pk2(o[2] * inv, o[3] * inv))}, rsws, WS_OX + (unsigned)(h * 8 + b) * 256 + lane * 8);
    }
  } else if (w < 3) {
    const int t = tid - 64;
    __builtin_amdgcn_s_sleep(TD_A);
    v4u v;
    for (unsigned spins = 0;; ++spins) {
      CFENCE();
      v = ld_sc(rsws, WS_OX + (unsigned)h * 2048 + t * 16);
      if (__all(valid4(v)) || spins > SPIN_G) break;
      __builtin_amdgcn_s_sleep(TS);
    }
    *(LAS v4u*)(L + L_OS + (t >> 4) * 272 + (t & 15) * 16) = v;
  } else if (w == 7) {
    VMWAIT(0);
  }
  bar();
  const unsigned tB2 = (unsigned)__builtin_amdgcn_s_memrealtime();
  STAMP(9);
  if (w < 4) {
    const LAS unsigned char* bp = L + L_OS + (lane & 7) * 272 + g * 16;
    v4u bb[4];
#pragma unroll
    for (int s = 0; s < 4; ++s) bb[s] = *(const LAS v4u*)(bp + s * 64);
    for (int t = w; t < 5; t += 4) {
      f4 y = {0, 0, 0, 0};
      const LAS unsigned char* ap = L + L_T1 + (8 * g + tq) * 160 + (16 * t + 4 * tp) * 2;
#pragma unroll
      for (int s = 0; s < 4; ++s) {
        const v2u x0 = trread(ap + s * 32 * 160), x1 = trread(ap + s * 32 * 160 + 4 * 160);
        y = mfma32(v4u{x0.x, x0.y, x1.x, x1.y}, bb[s], y);
      }
      const int cc = 4 * t + g;
      if (i16 < 8) st_sc(tg4(y, t10), rsws,
                         WS_T1P + (unsigned)((((i16 * 32 + 4 * b + cc / 5) * 32 + h) * 5 + cc % 5) * 16));
    }
  } else if (w < 7) {
    const int t = tid - 256;
    const bool act = t < 160;
    const unsigned o = WS_T1P + (unsigned)((b * 32 + h) * 160 + (act ? t : 0)) * 16;
    until(tB2, GB);
    v4u v;
    for (unsigned spins = 0;; ++spins) {
      CFENCE();
      v = ld_sc(rsws, o);
      if (__all(tagged4(v, t10)) || spins > SPIN_G) break;
      __builtin_amdgcn_s_sleep(TS);
    }
    if (act) *(LAS f4*)(L + L_T4P + t * 16) = f4{untg(v.x), untg(v.y), untg(v.z), untg(v.w)};
  }
  bar();
  if (w == 4) {
    const int e = lane >> 3, c = lane & 7;
    f4 sm = {0, 0, 0, 0};
    if (c < 5) {
#pragma unroll
      for (int u = 0; u < 4; ++u) sm += *(const LAS f4*)(L + L_T4P + ((4 * e + u) * 5 + c) * 16);
    }
#pragma unroll
    for (int k = 0; k < 4; ++k) { float x = sm[k]; x += dpp<0x128>(x); sm[k] = xrow_sum(x); }
    if (lane < 5) {
      const LAS unsigned* vec = (const LAS unsigned*)(L + L_VEC);
      const unsigned g0 = vec[2 * lane], g1 = vec[2 * lane + 1], x0 = vec[20 + 2 * lane], x1 = vec[20 + 2 * lane + 1];
      const float r0 = rbf(blo(x0) + rbf(blo(g0) * rbf(sm[0]))), r1 = rbf(bhi(x0) + rbf(bhi(g0) * rbf(sm[1])));
      const float r2 = rbf(blo(x1) + rbf(blo(g1) * rbf(sm[2]))), r3 = rbf(bhi(x1) + rbf(bhi(g1) * rbf(sm[3])));
      st_sc2(v2u{canonu(pk2(r0, r1)), canonu(pk2(r2, r3))}, rsws, WS_X1B + (unsigned)(b * 320 + 10 * h + 2 * lane) * 4);
      *(LAS f4*)(L + L_X1O + lane * 16) = f4{r0, r1, r2, r3};
    }
  }
  bar();
  const unsigned tB = (unsigned)__builtin_amdgcn_s_memrealtime();
  STAMP(10);
  if (is_mlp) {
    {
      const unsigned o = WS_X1B + (unsigned)w * 1280 + lane * 4;
      unsigned cs[5], sh[5];
#pragma unroll
      for (int u = 0; u < 5; ++u) {
        cs[u] = *(const LAS unsigned*)(L + L_CS + (w * 320 + u * 64 + lane) * 4);
        sh[u] = *(const LAS unsigned*)(L + L_CS + 10240 + (w * 320 + u * 64 + lane) * 4);
      }
      until(tB, GC);
      unsigned xv[5];
      for (unsigned spins = 0;; ++spins) {
        CFENCE();
        bool ok = true;
#pragma unroll
        for (int u = 0; u < 5; ++u) xv[u] = __builtin_amdgcn_raw_buffer_load_b32(rsws, o + u * 256, 0, 16);
#pragma unroll
        for (int u = 0; u < 5; ++u) ok = ok && xv[u] != SENT;
        if (__all(ok) || spins > SPIN_G) break;
        __builtin_amdgcn_s_sleep(TS);
      }
      const float x00 = blo(rfl(xv[0]));
      float s1 = 0.f, s2 = 0.f;
#pragma unroll
      for (int u = 0; u < 5; ++u) {
        const float d0 = blo(xv[u]) - x00, d1 = bhi(xv[u]) - x00;
        s1 += d0 + d1; s2 = fmaf(d0, d0, fmaf(d1, d1, s2));
      }
      s1 = row16_sum(s1); s2 = row16_sum(s2);
      s1 = xrow_sum(s1) * (1.f / DM); s2 = xrow_sum(s2) * (1.f / DM);
      const float rs = __builtin_amdgcn_rsqf(fmaxf(s2 - s1 * s1, 0.f) + 1e-5f), mr = -(x00 + s1) * rs;
#pragma unroll
      for (int u = 0; u < 5; ++u) {
        const float n0 = fmaf(blo(xv[u]), rs, mr), n1 = fmaf(bhi(xv[u]), rs, mr);
        *(LAS unsigned*)(L + L_H2 + w * 1296 + (u * 64 + lane) * 4) = pk2(fmaf(n0, blo(cs[u]), blo(sh[u])), fmaf(n1, bhi(cs[u]), bhi(sh[u])));
      }
    }
    bar();
    STAMP(11);
    {
      const int t = w & 1, kq = w >> 1;
      f4 a = {0, 0, 0, 0};
      const LAS unsigned char* ap = L + L_RT + (160 * kq + 8 * g + tq) * 64 + t * 32 + tp * 8;
      const LAS unsigned char* bp = L + L_H2 + (lane & 7) * 1296 + (160 * kq + 8 * g) * 2;
#pragma unroll
      for (int s = 0; s < 5; ++s) {
        const v4u bb = *(const LAS v4u*)(bp + s * 64);
        const v2u x0 = trread(ap + s * 2048), x1 = trread(ap + s * 2048 + 256);
        a = mfma32(v4u{x0.x, x0.y, x1.x, x1.y}, bb, a);
      }
      if (i16 < 8) *(LAS f4*)(L + L_T4P + (((kq * 2 + t) * 4 + g) * 8 + i16) * 16) = a;
    }
    bar();
    if (tid < 128) {
      const int j = tid >> 4, c = 2 * (tid & 15), t = c >> 4, gg = (c & 15) >> 2, r = c & 3;
      float z0 = 0.f, z1 = 0.f;
#pragma unroll
      for (int kq = 0; kq < 4; ++kq) {
        const LAS float* z = (const LAS float*)(L + L_T4P + (((kq * 2 + t) * 4 + gg) * 8 + j) * 16) + r;
        z0 += z[0]; z1 += z[1];
      }
      *(LAS unsigned*)(L + L_H1 + j * 80 + c * 2) = pk2(silu(rbf(z0)), silu(rbf(z1)));
    }
    bar();
    STAMP(12);
    {
      const v4u bb = *(const LAS v4u*)(L + L_H1 + (lane & 7) * 80 + g * 16);
      const int r0 = 8 * g + tq;
      const LAS unsigned char* ap = L + L_W2 + r0 * 1280 + (tp & 1) * 8;
      const int f = 2 * (r0 & 3);
#pragma unroll
      for (int tt = 0; tt < 5; ++tt) {
        const int ch = (2 * (5 * w + tt) + (tp >> 1)) ^ f;
        const v2u x0 = trread(ap + ch * 16), x1 = trread(ap + 4 * 1280 + ch * 16);
        const f4 y = mfma32(v4u{x0.x, x0.y, x1.x, x1.y}, bb, f4{0, 0, 0, 0});
        if (i16 < 8) *(LAS v4u*)(L + L_RT + i16 * 2576 + (16 * (5 * w + tt) + 4 * g) * 4) = tg4(y, t10);
      }
    }
    bar();
    {
#pragma unroll
      for (int u = 0; u < 3; ++u) {
        const int pc = u * 512 + tid, j = pc / 160, c = pc - 160 * j;
        if (pc < 1280) st_sc(*(const LAS v4u*)(L + L_RT + j * 2576 + c * 16), rsws, WS_T5P + (unsigned)mm * 20480 + (unsigned)pc * 16);
      }
    }
  }
  const unsigned tD = (unsigned)__builtin_amdgcn_s_memrealtime();
  STAMP(13);
  if (w < 5) {
    const bool act = tid < NMLP * 5;
    const int dm = act ? tid / 5 : 0, dc = act ? tid - 5 * dm : 0;
    const unsigned o = WS_T5P + (unsigned)((dm * 8 + b) * 640 + 20 * h + 4 * dc) * 4;
    if (is_mlp) until(tD, GD1); else until(tB, GD2);
    v4u v;
    for (unsigned spins = 0;; ++spins) {
      CFENCE();
      v = ld_sc(rsws, o);
      if (__all(tagged4(v, t10)) || spins > SPIN_G) break;
      __builtin_amdgcn_s_sleep(TS);
    }
    if (act) *(LAS f4*)(L + L_W2 + tid * 16) = f4{untg(v.x), untg(v.y), untg(v.z), untg(v.w)};
  }
  bar();
  if (w == 0) {
    const int e = lane / 5, c = lane - 5 * e;
    f4 sm = {0, 0, 0, 0};
    if (lane < 45) {
#pragma unroll
      for (int u = 0; u < 6; ++u) sm += *(const LAS f4*)(L + L_W2 + ((6 * e + u) * 5 + c) * 16);
      *(LAS f4*)(L + L_W2 + 8192 + lane * 16) = sm;
    }
    asm volatile("s_waitcnt lgkmcnt(0)" ::: "memory");
    if (lane < 5) {
      sm = f4{0, 0, 0, 0};
#pragma unroll
      for (int u = 0; u < 9; ++u) sm += *(const LAS f4*)(L + L_W2 + 8192 + (u * 5 + lane) * 16);
      const LAS unsigned* vec = (const LAS unsigned*)(L + L_VEC);
      const unsigned g0 = vec[10 + 2 * lane], g1 = vec[10 + 2 * lane + 1];
      const f4 x1 = *(const LAS f4*)(L + L_X1O + lane * 16);
      const float r0 = x1[0] + rbf(blo(g0) * rbf(sm[0])), r1 = x1[1] + rbf(bhi(g0) * rbf(sm[1]));
      const float r2 = x1[2] + rbf(blo(g1) * rbf(sm[2])), r3 = x1[3] + rbf(bhi(g1) * rbf(sm[3]));
      *(GAS v2u*)(p.out + b * DM + 20 * h + 4 * lane) = v2u{pk2(r0, r1), pk2(r2, r3)};
    }
  }
  if (id == 0 && tid == 448) {
    const unsigned E = __builtin_amdgcn_raw_buffer_load_b32(rsws, WS_EPOCH, 0, 16);
    __builtin_amdgcn_raw_buffer_store_b32(E + 1u, rsws, WS_EPOCH, 0, 16);
  }
  if (w == 7) { while ((unsigned)__builtin_amdgcn_s_memrealtime() - (unsigned)tearly < 5000u) __builtin_amdgcn_s_sleep(2); }
  STAMP(15);
}

}

extern "C" void kernel_launch(void* const* d_in, const int* in_sizes, int n_in, void* d_out, int out_size, void* d_ws,
                              size_t ws_size, hipStream_t stream) {
  Params p;
  p.x = (const u16*)d_in[0];
  p.kc = (const u16*)d_in[1];
  p.vc = (const u16*)d_in[2];
  p.wq = (const u16*)d_in[3];
  p.wo = (const u16*)d_in[4];
  p.w1 = (const u16*)d_in[6];
  p.w2 = (const u16*)d_in[7];
  p.c = (const u16*)d_in[8];
  p.wmod = (const u16*)d_in[9];
  p.out = (u16*)d_out;
  p.ws = (unsigned char*)d_ws;
  hipLaunchKernelGGL(fused_block, dim3(256), dim3(512), 0, stream, p);
}
```

```cpp
#include <hip/hip_runtime.h>
#include <stdint.h>

#define DIAG 0

namespace {

typedef unsigned short u16;
typedef unsigned v4u __attribute__((ext_vector_type(4)));
typedef unsigned v2u __attribute__((ext_vector_type(2)));
typedef float f4 __attribute__((ext_vector_type(4)));
typedef short s4 __attribute__((ext_vector_type(4)));
typedef short s8 __attribute__((ext_vector_type(8)));
typedef __bf16 bf2 __attribute__((ext_vector_type(2)));
typedef __bf16 bf8 __attribute__((ext_vector_type(8)));
typedef __amdgpu_buffer_rsrc_t rsrc_t;
#define LAS __attribute__((address_space(3)))
#define GAS __attribute__((address_space(1)))
#define DI __device__ __forceinline__

constexpr int NB = 8, DM = 640, NH = 32, DH = 128, SK = 4096, WIN = 1024, FF = 1728;
constexpr int NSW = 7;
constexpr int NL = 37;
#define TD_A 8
#define TD_B 8
#define TD_C 16
#define TD_D 8
#define TD_D2 4
#define TS 6
#define ABL_CTRL 0
#define ABL_SLOT 0
#define ABL_ROLE 0
#define ABL_SENT 0
#define LEAD 3
#define MP0 12
#define MPACE 11
#define DELTA 0
#define HEAVY 1
#define ABL_G1 0
#define ABL_G2 0
#define ABL_G3 0
#define ABL_TAIL 0
#define GB 40
#define GC 40
#define GD1 40
#define GD2 190
#define WARM 1
#define INDIC 1
#define ABL_WO 0
#define ABL_W12 0
#define D1 600
#define D2 100
#define PSLEEP 32
#define DPACE 15
#define PACE_ 3
constexpr int PACE = PACE_;
#define NPRE_ 16
constexpr int NPRE = NPRE_;

constexpr int NMLP = 54;
constexpr unsigned WS_EPOCH = 0;
constexpr unsigned WS_INIT = 4096;
constexpr unsigned WS_MODG = 8192;
constexpr unsigned WS_QP = WS_MODG + NB * 1920 * 8;
constexpr unsigned WS_OX = WS_QP + NH * 8 * 8 * 128 * 4;
constexpr unsigned WS_T1P = WS_OX + NH * 8 * 256;
constexpr unsigned WS_X1B = WS_T1P + NB * NH * DM * 4;
constexpr unsigned WS_T5P = WS_X1B + NB * 320 * 4;
constexpr unsigned WS_END = WS_T5P + NMLP * NB * DM * 4;
constexpr unsigned WS_DIAG = 4u << 20;
constexpr unsigned SENT = 0xFFFFFFFFu;
static_assert(WS_END <= WS_DIAG, "workspace map");

constexpr unsigned L_RT = 0;
constexpr unsigned L_QT = 40960;
constexpr unsigned L_W2 = 61440;
constexpr unsigned L_T1 = 102400;
constexpr unsigned L_CS = 122880;
constexpr unsigned L_MISC = 143360;
constexpr unsigned L_FLAG = L_MISC;
constexpr unsigned L_Q = L_MISC + 64;
constexpr unsigned L_STAT = L_Q + 256;
constexpr unsigned L_VEC = L_STAT + 64;
constexpr unsigned L_X1O = L_VEC + 128;
constexpr unsigned L_SC = L_X1O + 128;
constexpr unsigned L_H2 = L_SC;
constexpr unsigned L_HS = L_SC + 8 * 1296;
constexpr unsigned L_MRG = L_HS + 8 * 176;
constexpr unsigned L_OS = L_MRG + 7 * 132 * 4;
constexpr unsigned L_T4P = L_MRG;
constexpr unsigned L_H1 = L_T4P + 4096;
constexpr unsigned L_END = 163840;
static_assert(L_OS + 8 * 272 <= L_END && L_H1 + 8 * 80 <= L_OS + 8 * 272, "LDS map");
static_assert((L_SC % 16) == 0 && (L_HS % 16) == 0 && (L_MRG % 16) == 0 && (L_OS % 16) == 0 && (L_H1 % 16) == 0, "LDS alignment");

constexpr unsigned SPIN_G = 1u << 16;
constexpr unsigned SPIN_L = 1u << 22;

struct Params {
  const u16 *x, *kc, *vc, *wq, *wo, *w1, *w2, *c, *wmod;
  u16* out;
  unsigned char* ws;
};

DI unsigned rfl(unsigned x) { return __builtin_amdgcn_readfirstlane(x); }
DI float asf(unsigned u) { return __uint_as_float(u); }
DI unsigned asu(float f) { return __float_as_uint(f); }
DI unsigned pk2(float lo, float hi) { bf2 v = {(__bf16)lo, (__bf16)hi}; return __builtin_bit_cast(unsigned, v); }
DI float rbf(float f) { return asf(pk2(f, 0.f) << 16); }
DI float blo(unsigned u) { return asf(u << 16); }
DI float bhi(unsigned u) { return asf(u & 0xffff0000u); }
DI float dot2(unsigned a, unsigned b, float c) {
  return __builtin_amdgcn_fdot2_f32_bf16(__builtin_bit_cast(bf2, a), __builtin_bit_cast(bf2, b), c, false);
}
template <int CTRL> DI float dpp(float x) {
  return __builtin_bit_cast(float, __builtin_amdgcn_mov_dpp(__builtin_bit_cast(int, x), CTRL, 0xf, 0xf, true));
}
DI float row16_sum(float x) { x += dpp<0x128>(x); x += dpp<0x124>(x); x += dpp<0x122>(x); x += dpp<0x121>(x); return x; }
DI float oct_sum(float x) { x += dpp<0xB1>(x); x += dpp<0x4E>(x); x += dpp<0x141>(x); return x; }
DI float x16_sum(float x) {
  auto s = __builtin_amdgcn_permlane16_swap(asu(x), asu(x), false, false);
  return asf(s[0]) + asf(s[1]);
}
DI float x32_sum(float x) {
  auto t = __builtin_amdgcn_permlane32_swap(asu(x), asu(x), false, false);
  return asf(t[0]) + asf(t[1]);
}
DI float xrow_sum(float x) { return x32_sum(x16_sum(x)); }
DI float xrow_max(float x) {
  auto s = __builtin_amdgcn_permlane16_swap(asu(x), asu(x), false, false);
  x = fmaxf(asf(s[0]), asf(s[1]));
  auto t = __builtin_amdgcn_permlane32_swap(asu(x), asu(x), false, false);
  return fmaxf(asf(t[0]), asf(t[1]));
}
DI float wave_sum(float x) { return xrow_sum(row16_sum(x)); }
DI f4 mfma32(v4u a, v4u b, f4 c) {
  return __builtin_amdgcn_mfma_f32_16x16x32_bf16(__builtin_bit_cast(bf8, a), __builtin_bit_cast(bf8, b), c, 0, 0, 0);
}
DI f4 mfma16(v2u a, v2u b, f4 c) {
  return __builtin_amdgcn_mfma_f32_16x16x16bf16_1k(__builtin_bit_cast(s4, a), __builtin_bit_cast(s4, b), c, 0, 0, 0);
}
DI v2u trread(const LAS unsigned char* p) {
  return __builtin_bit_cast(v2u, __builtin_amdgcn_ds_read_tr16_b64_v4i16((LAS s4*)p));
}
DI rsrc_t mkrs(const void* p, unsigned bytes) { return __builtin_amdgcn_make_buffer_rsrc((void*)p, 0, (int)bytes, 0x00020000); }
DI v4u ld_nt(rsrc_t r, unsigned vo, unsigned so) { return __builtin_amdgcn_raw_buffer_load_b128(r, vo, so, 2); }
#define KAUX 2
#define VAUX 2
DI v4u ld_k(rsrc_t r, unsigned vo, unsigned so) { return __builtin_amdgcn_raw_buffer_load_b128(r, vo, so, KAUX); }
DI v4u ld_v(rsrc_t r, unsigned vo, unsigned so) { return __builtin_amdgcn_raw_buffer_load_b128(r, vo, so, VAUX); }
DI v4u ld_sc(rsrc_t r, unsigned vo) { return __builtin_amdgcn_raw_buffer_load_b128(r, vo, 0, 16); }
DI v2u ld_sc2(rsrc_t r, unsigned vo) { return __builtin_amdgcn_raw_buffer_load_b64(r, vo, 0, 16); }
DI void st_sc(v4u v, rsrc_t r, unsigned vo) { __builtin_amdgcn_raw_buffer_store_b128(v, r, vo, 0, 16); }
DI void st_sc2(v2u v, rsrc_t r, unsigned vo) { __builtin_amdgcn_raw_buffer_store_b64(v, r, vo, 0, 16); }
DI unsigned canonu(unsigned u) { return u == 0xFFFFFFFFu ? 0x7FFF7FFFu : u; }
DI void dma16(rsrc_t r, unsigned lds, unsigned vo, unsigned so) {
  unsigned keep;
  asm volatile("s_nop 4\n\ts_mov_b32 %0, m0\n\ts_mov_b32 m0, %1\n\ts_nop 0\n\tbuffer_load_dwordx4 %2, %3, %4 offen nt lds\n\ts_mov_b32 m0, %0"
               : "=&s"(keep) : "s"(lds), "v"(vo), "s"(r), "s"(so) : "memory");
}
template <int N> DI void vmwait() { asm volatile("s_waitcnt vmcnt(%0)" :: "n"(N) : "memory"); }
#define VMWAIT(n) vmwait<(n)>()
DI void bar() { asm volatile("s_waitcnt lgkmcnt(0)\n\ts_barrier" ::: "memory"); }
#define CFENCE() asm volatile("" ::: "memory")
#define SCHED() __builtin_amdgcn_sched_barrier(0)
DI unsigned canon(float f) { unsigned u = asu(f); return u == SENT ? 0x7FFFFFFFu : u; }
DI unsigned tg(float f, unsigned t10) { return ((asu(f) + 0x200u) & ~0x3FFu) | t10; }
DI v4u tg4(f4 v, unsigned t10) { return v4u{tg(v[0], t10), tg(v[1], t10), tg(v[2], t10), tg(v[3], t10)}; }
DI bool tagged4(v4u v, unsigned t10) { return (((v.x ^ t10) | (v.y ^ t10) | (v.z ^ t10) | (v.w ^ t10)) & 0x3FFu) == 0u; }
DI float untg(unsigned u) { return asf(u & ~0x3FFu); }
DI bool valid4(v4u v) { return v.x != SENT && v.y != SENT && v.z != SENT && v.w != SENT; }
DI float rcp(float x) { return __builtin_amdgcn_rcpf(x); }
DI float silu(float v) { return v * rcp(1.f + __builtin_amdgcn_exp2f(v * -1.4426950408889634f)); }

#define STAMP(k) do { } while (0)
#define STAMP2(k) do { } while (0)

__global__ void __launch_bounds__(512, 2) fused_block(Params p) {
  __shared__ __attribute__((aligned(16))) unsigned char smem[L_END];
  LAS unsigned char* L = (LAS unsigned char*)smem;
  const unsigned lds0 = (unsigned)(uintptr_t)L;
  const int tid = threadIdx.x, lane = tid & 63;
  const int w = (int)rfl(tid >> 6);
  const int id = blockIdx.x, xcd = id & 7, sl = id >> 3;
  const int h = xcd * 4 + (sl >> 3), b = sl & 7;
  const int g = lane >> 4, i16 = lane & 15;
  const int tq = i16 >> 2, tp = lane & 3;
  const rsrc_t rsws = mkrs(p.ws, WS_END);
  volatile LAS unsigned* flags = (volatile LAS unsigned*)(L + L_FLAG);
  const int mpr = (sl >> 1) * 8 + xcd;
  const bool has_mod = !ABL_ROLE && sl < 16 && mpr < 60;
  const bool crit = has_mod && mpr < 20;
  const int mcol0 = (2 * mpr + (sl & 1)) * 32;
  const int fpr = ((sl - 16) >> 1) * 8 + xcd;
  const bool is_mlp = !ABL_ROLE && sl >= 16 && sl < 24 && fpr < 27;
  const int mm = 2 * fpr + (sl & 1);

  STAMP(0);
  const unsigned tstart = (unsigned)__builtin_amdgcn_s_memrealtime();
  auto sched = [&](int i, bool light) {
    if (DELTA && light) { while ((unsigned)__builtin_amdgcn_s_memrealtime() - tstart < (unsigned)(i * DELTA / 10)) __builtin_amdgcn_s_sleep(1); }
  };

  if (w < NSW) {
    const rsrc_t rsk = mkrs(p.kc + ((size_t)(b * NH + h) * SK + (SK - WIN)) * DH, WIN * DH * 2);
    const rsrc_t rsv = mkrs(p.vc + ((size_t)(b * NH + h) * SK + (SK - WIN)) * DH, WIN * DH * 2);
    const unsigned vo = (unsigned)(lane * 16 + w * 1024);
    v4u kr[NL], vr[NL], cr[2];
    if (has_mod && w < 5) {
      const GAS v4u* cp = (const GAS v4u*)p.c;
      cr[0] = cp[w * 128 + lane];
      cr[1] = cp[w * 128 + 64 + lane];
    }
    auto silu_c = [&]() {
      if (w < 5) {
#pragma unroll
        for (int u = 0; u < 2; ++u) {
          const int q = w * 128 + u * 64 + lane, j = q / 80, k8 = q - 80 * j;
          v4u o;
          o.x = pk2(silu(blo(cr[u].x)), silu(bhi(cr[u].x)));
          o.y = pk2(silu(blo(cr[u].y)), silu(bhi(cr[u].y)));
          o.z = pk2(silu(blo(cr[u].z)), silu(bhi(cr[u].z)));
          o.w = pk2(silu(blo(cr[u].w)), silu(bhi(cr[u].w)));
          *(LAS v4u*)(L + L_SC + j * 1296 + k8 * 16) = o;
        }
      }
    };
    const rsrc_t rsrt = mkrs(p.wmod, DM * 6 * DM * 2);
    const unsigned rt_vo = (unsigned)((lane >> 2) * 7680 + mcol0 * 2 + (lane & 3) * 16);
    auto dma_rt = [&](int m) { dma16(rsrt, lds0 + L_RT + m * 1024, rt_vo, (unsigned)m * 16u * 7680u); };
    if (!crit) {
#pragma unroll
      for (int i = 0; i < PACE; ++i) kr[i] = ld_k(rsk, vo, i * 7168);
    }
    if (has_mod) { for (int m = w; m < 40; m += NSW) dma_rt(m); }
    SCHED();
    bar();
    if (has_mod) {
      silu_c();
      VMWAIT(0);
      asm volatile("s_waitcnt lgkmcnt(0)" ::: "memory");
      if (lane == 0) __hip_atomic_fetch_add((LAS unsigned*)(L + L_FLAG), 1u, __ATOMIC_RELAXED, __HIP_MEMORY_SCOPE_WORKGROUP);
    }
    {
      const rsrc_t rsq = mkrs(p.wq, DM * NH * DH * 2);
      for (int n = w; n < 20; n += NSW) {
        const int rl = 4 * n + g;
        dma16(rsq, lds0 + L_QT + n * 1024, (unsigned)((80 * b + rl) * 8192 + h * 256 + ((i16 ^ (2 * (rl & 7))) * 16)), 0);
      }
    }
    if (crit) {
#pragma unroll
      for (int i = 0; i < PACE; ++i) kr[i] = ld_k(rsk, vo, i * 7168);
    }
    SCHED();
    const rsrc_t rsw1 = mkrs(p.w1, DM * FF * 2);
    const rsrc_t rsw2 = mkrs(p.w2 + (size_t)(is_mlp ? mm : 0) * 32 * DM, 32 * DM * 2);
    auto slot = [&](int s) {
      const int n = 7 * s + w;
      if (n < 40) {
        dma16(rsw1, lds0 + L_RT + n * 1024, (unsigned)((lane >> 2) * (FF * 2) + mm * 64 + (lane & 3) * 16), (unsigned)n * 16u * (FF * 2));
      } else if (n < 80) {
        const int ci = (n - 40) * 64 + lane, row = ci / 80, pos = ci - 80 * row;
        dma16(rsw2, lds0 + L_W2 + (n - 40) * 1024, (unsigned)(row * 1280 + (pos ^ (2 * (row & 3))) * 16), 0);
      }
    };
    const bool heavy = HEAVY == 1 ? (is_mlp || has_mod) : HEAVY == 2 ? is_mlp : false;
#pragma unroll
    for (int i = PACE; i < NL + NPRE; ++i) {
      sched(i, !is_mlp);
      if (heavy) VMWAIT(PACE); else VMWAIT(PACE - 1);
      if (i < NL) kr[i] = ld_k(rsk, vo, i * 7168); else vr[i - NL] = ld_v(rsv, vo, (i - NL) * 7168);
      SCHED();
      if (i == PACE + 4) {
        asm volatile("s_waitcnt lgkmcnt(0)" ::: "memory");
        if (lane == 0) __hip_atomic_fetch_add((LAS unsigned*)(L + L_FLAG) + 2, 1u, __ATOMIC_RELAXED, __HIP_MEMORY_SCOPE_WORKGROUP);
        STAMP(1);
      }
      if (i >= 7 && (i % 4) == 3 && (i - 7) / 4 < 12) { if (is_mlp) slot((i - 7) / 4); SCHED(); }
    }
    STAMP(2);
    {
      unsigned spins = 0;
      while (flags[1] == 0u) { __builtin_amdgcn_s_sleep(1); if (++spins > SPIN_L) break; }
    }
    CFENCE();
    STAMP(3);
    const v4u q4 = *(const LAS v4u*)(L + L_Q + i16 * 16);
    float s[NL];
#pragma unroll
    for (int i = 0; i < NL; ++i) {
      float d = dot2(kr[i].x, q4.x, 0.f);
      d = dot2(kr[i].y, q4.y, d); d = dot2(kr[i].z, q4.z, d); d = dot2(kr[i].w, q4.w, d);
      s[i] = row16_sum(d);
      if ((i & 3) == 3 && NPRE + (i >> 2) < NL) { SCHED(); sched(NL + NPRE + (i >> 2), !is_mlp); if (heavy) VMWAIT(PACE); else VMWAIT(PACE - 1); vr[NPRE + (i >> 2)] = ld_v(rsv, vo, (NPRE + (i >> 2)) * 7168); SCHED(); }
    }
    constexpr int NV1 = NPRE + (NL >> 2);
    if (w >= 4) s[NL - 1] = -INFINITY;
    float m = s[0];
#pragma unroll
    for (int i = 1; i < NL; ++i) m = fmaxf(m, s[i]);
    m = xrow_max(m);
    constexpr float C2 = 0.08838834764831845f * 1.4426950408889634f;
    const float mc = m * C2;
    unsigned pp[NL];
    float l = 0.f;
#pragma unroll
    for (int i = 0; i < NL; ++i) {
      pp[i] = pk2(0.f, __builtin_amdgcn_exp2f(fmaf(s[i], C2, -mc)));
      l += asf(pp[i]);
    }
    l = xrow_sum(l);
    STAMP(4);
    float acc[8];
#pragma unroll
    for (int k = 0; k < 8; ++k) acc[k] = 0.f;
#pragma unroll
    for (int i = 0; i < NL; ++i) {
      const unsigned p1 = pp[i], p0 = p1 >> 16;
      acc[0] = dot2(vr[i].x, p0, acc[0]); acc[1] = dot2(vr[i].x, p1, acc[1]);
      acc[2] = dot2(vr[i].y, p0, acc[2]); acc[3] = dot2(vr[i].y, p1, acc[3]);
      acc[4] = dot2(vr[i].z, p0, acc[4]); acc[5] = dot2(vr[i].z, p1, acc[5]);
      acc[6] = dot2(vr[i].w, p0, acc[6]); acc[7] = dot2(vr[i].w, p1, acc[7]);
      SCHED();
      if (NV1 + i < NL) { sched(NL + NV1 + i, !is_mlp); if (heavy) VMWAIT(PACE); else VMWAIT(PACE - 1); vr[NV1 + i] = ld_v(rsv, vo, (NV1 + i) * 7168); SCHED(); }
      if (NV1 + i == NL - 1 - LEAD) {
        if (lane == 0) __hip_atomic_fetch_add((LAS unsigned*)(L + L_FLAG) + 3, 1u, __ATOMIC_RELAXED, __HIP_MEMORY_SCOPE_WORKGROUP);
      }
    }
    STAMP(5);
#pragma unroll
    for (int k = 0; k < 8; ++k) acc[k] = xrow_sum(acc[k]);
    if (g == 0) {
      LAS float* mr = (LAS float*)(L + L_MRG) + w * 132;
      *(LAS f4*)(mr + 4 + 8 * i16) = f4{acc[0], acc[1], acc[2], acc[3]};
      *(LAS f4*)(mr + 8 + 8 * i16) = f4{acc[4], acc[5], acc[6], acc[7]};
      if (i16 == 0) { mr[0] = m; mr[1] = l; }
    }
  } else {
    __builtin_amdgcn_s_setprio(3);
    if (lane == 0) { flags[0] = 0u; flags[1] = 0u; flags[2] = 0u; flags[3] = 0u; }
    if (ABL_CTRL) {
      bar();
      while ((unsigned)__builtin_amdgcn_s_memrealtime() - tstart < 800) __builtin_amdgcn_s_sleep(8);
      if (lane == 0) flags[1] = 1u;
    } else {
    const unsigned E = __builtin_amdgcn_raw_buffer_load_b32(rsws, WS_EPOCH, 0, 16);
    unsigned warm = 0;
    {
      const int n = sl * 64 + lane, pi = n / 640, row = n - 640 * pi, pr = xcd + 8 * pi;
      if (WARM && pr < 20) warm = __builtin_amdgcn_raw_buffer_load_b32(mkrs(p.wmod, DM * 6 * DM * 2), (unsigned)(row * 7680 + pr * 128), 0, 2);
    }
    const GAS v4u* xp = (const GAS v4u*)p.x;
    v4u xr[10];
    const int sj = lane >> 3, sub = lane & 7;
#pragma unroll
    for (int u = 0; u < 10; ++u) xr[u] = xp[sj * 80 + sub + 8 * u];
    SCHED();
    bar();
    unsigned xh[5];
#pragma unroll
    for (int u = 0; u < 5; ++u) { const int idx = u * 64 + lane, j = idx / 40, m = idx - 40 * j; xh[u] = ((const GAS unsigned*)p.x)[j * 320 + 40 * b + m]; }
    if (!ABL_SENT) {
      const v4u sv = {SENT, SENT, SENT, SENT};
      if (lane < 16) st_sc(sv, rsws, WS_OX + (unsigned)(h * 8 + b) * 256 + lane * 16);
      if (lane < 5) st_sc2(v2u{SENT, SENT}, rsws, WS_X1B + (unsigned)(b * 320 + 10 * h) * 4 + lane * 8);
    }
    STAMP(1);
    {
      float sm = 0.f;
#pragma unroll
      for (int u = 0; u < 10; ++u)
        sm += ((blo(xr[u].x) + bhi(xr[u].x)) + (blo(xr[u].y) + bhi(xr[u].y))) + ((blo(xr[u].z) + bhi(xr[u].z)) + (blo(xr[u].w) + bhi(xr[u].w)));
      const float mean = oct_sum(sm) * (1.f / DM);
      float sq = 0.f;
#pragma unroll
      for (int u = 0; u < 10; ++u) {
#pragma unroll
        for (int k = 0; k < 4; ++k) { const float a = blo(xr[u][k]) - mean, c2 = bhi(xr[u][k]) - mean; sq = fmaf(a, a, sq); sq = fmaf(c2, c2, sq); }
      }
      const float var = oct_sum(sq) * (1.f / DM);
      if (sub == 0) {
        LAS float* st = (LAS float*)(L + L_STAT) + sj * 2;
        st[0] = rbf(mean);
        st[1] = rcp(rbf(__builtin_amdgcn_sqrtf(rbf(rbf(var) + 1e-5f))));
      }
    }
    VMWAIT(0);
    asm volatile("" :: "v"(warm));
    const unsigned T = rfl(E) + 1u;
    const unsigned t10 = T & 0x3FFu;
    if (lane == 0) flags[4] = T;
    if (lane == 0) st_sc2(v2u{T, ~T}, rsws, WS_INIT + (unsigned)(b * 32 + h) * 8);
    STAMP2(9);
    if (has_mod) {
      unsigned spins = 0;
      while (flags[0] < (unsigned)NSW) { __builtin_amdgcn_s_sleep(1); if (++spins > SPIN_L) break; }
      CFENCE();
      STAMP(2);
      f4 a0 = {0, 0, 0, 0}, a1 = {0, 0, 0, 0};
      const LAS unsigned char* ap = L + L_RT + (8 * g + tq) * 64 + tp * 8;
      const LAS unsigned char* bp = L + L_SC + (lane & 7) * 1296 + g * 16;
#pragma unroll 2
      for (int s = 0; s < 20; ++s) {
        const v4u bb = *(const LAS v4u*)(bp + s * 64);
        const v2u x0 = trread(ap + s * 2048), x1 = trread(ap + s * 2048 + 256);
        const v2u y0 = trread(ap + s * 2048 + 32), y1 = trread(ap + s * 2048 + 256 + 32);
        a0 = mfma32(v4u{x0.x, x0.y, x1.x, x1.y}, bb, a0);
        a1 = mfma32(v4u{y0.x, y0.y, y1.x, y1.y}, bb, a1);
      }
      if (i16 < 8) {
        const unsigned o = WS_MODG + (unsigned)(i16 * 1920 + (mcol0 + 4 * g) / 2) * 8;
        st_sc(v4u{pk2(a0[0], a0[1]), T, pk2(a0[2], a0[3]), T}, rsws, o);
        st_sc(v4u{pk2(a1[0], a1[1]), T, pk2(a1[2], a1[3]), T}, rsws, o + 64);
      }
    }
    STAMP(3);
    {
      v2u sa[5], ca[5], fl[4];
      if (D1) { while ((unsigned)__builtin_amdgcn_s_memrealtime() - tstart < D1) __builtin_amdgcn_s_sleep(2); }
      STAMP2(10);
      if (ABL_G1) { for (int u = 0; u < 5; ++u) { sa[u] = v2u{0u, T}; ca[u] = v2u{0u, T}; } }
      for (unsigned spins = 0; !ABL_G1; ++spins) {
        CFENCE();
        bool ok = true;
#pragma unroll
        for (int u = 0; u < 5; ++u) {
          const int idx = u * 64 + lane, j = idx / 40, m = idx - 40 * j;
          const unsigned o = WS_MODG + (unsigned)(j * 1920 + 40 * b + m) * 8;
          sa[u] = ld_sc2(rsws, o);
          ca[u] = ld_sc2(rsws, o + 320 * 8);
        }
#pragma unroll
        for (int u = 0; u < 4; ++u) fl[u] = ld_sc2(rsws, WS_INIT + (unsigned)(u * 64 + lane) * 8);
#pragma unroll
        for (int u = 0; u < 5; ++u) ok = ok && sa[u].y == T && ca[u].y == T;
#pragma unroll
        for (int u = 0; u < 4; ++u) ok = ok && fl[u].x == T && fl[u].y == ~T;
        if (__all(ok) || spins > SPIN_G) break;
        __builtin_amdgcn_s_sleep(PSLEEP);
      }
      STAMP2(11);
      asm volatile("s_waitcnt lgkmcnt(0)" ::: "memory");
#pragma unroll
      for (int u = 0; u < 5; ++u) {
        const int idx = u * 64 + lane, j = idx / 40, m = idx - 40 * j;
        const float mean = ((LAS float*)(L + L_STAT))[2 * j], den = ((LAS float*)(L + L_STAT))[2 * j + 1];
        const float n0 = rbf(rbf(blo(xh[u]) - mean) * den), n1 = rbf(rbf(bhi(xh[u]) - mean) * den);
        const float h0 = rbf(n0 * rbf(1.f + blo(ca[u].x))) + blo(sa[u].x), h1 = rbf(n1 * rbf(1.f + bhi(ca[u].x))) + bhi(sa[u].x);
        *(LAS unsigned*)(L + L_HS + j * 176 + m * 4) = pk2(h0, h1);
      }
    }
    STAMP(4);
    {
      unsigned spins = 0;
      while (flags[2] < (unsigned)NSW) { __builtin_amdgcn_s_sleep(1); if (++spins > SPIN_L) break; }
      CFENCE();
    }
    STAMP2(12);
    {
      f4 qa[8];
#pragma unroll
      for (int t = 0; t < 8; ++t) qa[t] = f4{0, 0, 0, 0};
#pragma unroll
      for (int s = 0; s < 5; ++s) {
        const v2u bb = *(const LAS v2u*)(L + L_HS + (lane & 7) * 176 + s * 32 + g * 8);
        const int row = 16 * s + 4 * g + tq;
        const LAS unsigned char* rp = L + L_QT + row * 256 + (tp & 1) * 8;
        const int sw = 2 * (row & 7);
#pragma unroll
        for (int t = 0; t < 8; ++t) qa[t] = mfma16(trread(rp + (((2 * t + (tp >> 1)) ^ sw) * 16)), bb, qa[t]);
      }
      if (i16 < 8 && !ABL_G2) {
        const unsigned o = WS_QP + (unsigned)(((h * 8 + b) * 8 + i16) * 128 + 4 * g) * 4;
#pragma unroll
        for (int t = 0; t < 8; ++t) st_sc(tg4(qa[t], t10), rsws, o + t * 64);
      }
    }
    STAMP(5);
    const unsigned tpub = (unsigned)__builtin_amdgcn_s_memrealtime();
    {
      v4u qv[4];
      if (D2) { while ((unsigned)__builtin_amdgcn_s_memrealtime() - tpub < D2) __builtin_amdgcn_s_sleep(2); }
      STAMP2(13);
      if (ABL_G2) { for (int u = 0; u < 4; ++u) qv[u] = v4u{t10, t10, t10, t10}; }
      for (unsigned spins = 0; !ABL_G2; ++spins) {
        CFENCE();
        bool ok = true;
#pragma unroll
        for (int u = 0; u < 4; ++u) qv[u] = ld_sc(rsws, WS_QP + (unsigned)(((h * 8 + 2 * u + (lane >> 5)) * 8 + b) * 128 + 4 * (lane & 31)) * 4);
#pragma unroll
        for (int u = 0; u < 4; ++u) ok = ok && tagged4(qv[u], t10);
        if (__all(ok) || spins > SPIN_G) break;
        __builtin_amdgcn_s_sleep(PSLEEP);
      }
      f4 qs;
#pragma unroll
      for (int k = 0; k < 4; ++k) qs[k] = x32_sum((untg(qv[0][k]) + untg(qv[1][k])) + (untg(qv[2][k]) + untg(qv[3][k])));
      if (lane < 32) *(LAS v2u*)(L + L_Q + lane * 8) = v2u{pk2(qs[0], qs[1]), pk2(qs[2], qs[3])};
      asm volatile("s_waitcnt lgkmcnt(0)" ::: "memory");
      if (lane == 0) flags[1] = 1u;
    }
    STAMP(6);
    if (!ABL_G3) {
      const unsigned go = WS_MODG + (unsigned)(b * 1920 + (lane < 10 ? 640 + 10 * h + lane : 1600 + 10 * h + (lane < 20 ? lane - 10 : 0))) * 8;
      const unsigned xv = ((const GAS unsigned*)p.x)[b * 320 + 10 * h + (lane < 10 ? lane : 0)];
      v2u gv;
      for (unsigned spins = 0;; ++spins) {
        CFENCE();
        gv = ld_sc2(rsws, go);
        if (__all(gv.y == T) || spins > SPIN_G) break;
        __builtin_amdgcn_s_sleep(32);
      }
      if (lane < 20) *(LAS unsigned*)(L + L_VEC + lane * 4) = gv.x;
      if (lane < 10) *(LAS unsigned*)(L + L_VEC + 80 + lane * 4) = xv;
      if (is_mlp) {
        for (int j = 0; j < 8; ++j) {
          v4u cv[5];
          for (unsigned spins = 0;; ++spins) {
            CFENCE();
            bool ok = true;
#pragma unroll
            for (int u = 0; u < 5; ++u) cv[u] = ld_sc(rsws, WS_MODG + (unsigned)(j * 1920 + 960) * 8 + (unsigned)(u * 64 + lane) * 16);
#pragma unroll
            for (int u = 0; u < 5; ++u) ok = ok && cv[u].y == T && cv[u].w == T;
            if (__all(ok) || spins > SPIN_G) break;
            __builtin_amdgcn_s_sleep(32);
          }
#pragma unroll
          for (int u = 0; u < 5; ++u) {
            const int pc = u * 64 + lane;
            const unsigned o = (pc < 160 ? 10240u + (unsigned)(j * 320 + 2 * pc) * 4 : (unsigned)(j * 320 + 2 * (pc - 160)) * 4);
            const v2u sc1 = {pk2(1.f + blo(cv[u].x), 1.f + bhi(cv[u].x)), pk2(1.f + blo(cv[u].z), 1.f + bhi(cv[u].z))};
            *(LAS v2u*)(L + L_CS + o) = pc < 160 ? v2u{cv[u].x, cv[u].z} : sc1;
          }
        }
      }
    }
    STAMP(7);
    {
      unsigned spins = 0;
      while (flags[3] < (unsigned)NSW) { __builtin_amdgcn_s_sleep(1); if (++spins > SPIN_L) break; }
      CFENCE();
    }
    {
      const rsrc_t rswo = mkrs(p.wo + (size_t)h * DH * DM, DH * DM * 2);
      for (int n = 0; n < 20; ++n) {
        const int ci = n * 64 + lane, row = ci / 10, ch = ci - 10 * row;
        dma16(rswo, lds0 + L_T1 + n * 1024, (unsigned)(row * 1280 + b * 160 + ch * 16), 0);
      }
    }
    }
  }

  bar();
  STAMP(8);
  if (ABL_CTRL || ABL_TAIL) {
    STAMP(15);
    return;
  }
  const unsigned t10 = flags[4] & 0x3FFu;
  auto until = [&](unsigned t0, unsigned ticks) { while ((unsigned)__builtin_amdgcn_s_memrealtime() - t0 < ticks) __builtin_amdgcn_s_sleep(1); };
  if (w == 0) {
    if (lane < 32) {
      const LAS float* mr = (const LAS float*)(L + L_MRG);
      float M = mr[0];
#pragma unroll
      for (int u = 1; u < NSW; ++u) M = fmaxf(M, mr[u * 132]);
      constexpr float C2 = 0.08838834764831845f * 1.4426950408889634f;
      float Lsum = 0.f;
      f4 o = {0, 0, 0, 0};
#pragma unroll
      for (int u = 0; u < NSW; ++u) {
        const float f = __builtin_amdgcn_exp2f((mr[u * 132] - M) * C2);
        Lsum = fmaf(f, mr[u * 132 + 1], Lsum);
        o += f * *(const LAS f4*)(mr + u * 132 + 4 + 4 * lane);
      }
      const float inv = rcp(Lsum);
      st_sc2(v2u{canonu(pk2(o[0] * inv, o[1] * inv)), canonu(pk2(o[2] * inv, o[3] * inv))}, rsws, WS_OX + (unsigned)(h * 8 + b) * 256 + lane * 8);
    }
  } else if (w < 3) {
    const int t = tid - 64;
    __builtin_amdgcn_s_sleep(TD_A);
    v4u v;
    for (unsigned spins = 0;; ++spins) {
      CFENCE();
      v = ld_sc(rsws, WS_OX + (unsigned)h * 2048 + t * 16);
      if (__all(valid4(v)) || spins > SPIN_G) break;
      __builtin_amdgcn_s_sleep(TS);
    }
    *(LAS v4u*)(L + L_OS + (t >> 4) * 272 + (t & 15) * 16) = v;
  } else if (w == 7) {
    VMWAIT(0);
  }
  bar();
  const unsigned tB2 = (unsigned)__builtin_amdgcn_s_memrealtime();
  STAMP(9);
  if (w < 4) {
    const LAS unsigned char* bp = L + L_OS + (lane & 7) * 272 + g * 16;
    v4u bb[4];
#pragma unroll
    for (int s = 0; s < 4; ++s) bb[s] = *(const LAS v4u*)(bp + s * 64);
    for (int t = w; t < 5; t += 4) {
      f4 y = {0, 0, 0, 0};
      const LAS unsigned char* ap = L + L_T1 + (8 * g + tq) * 160 + (16 * t + 4 * tp) * 2;
#pragma unroll
      for (int s = 0; s < 4; ++s) {
        const v2u x0 = trread(ap + s * 32 * 160), x1 = trread(ap + s * 32 * 160 + 4 * 160);
        y = mfma32(v4u{x0.x, x0.y, x1.x, x1.y}, bb[s], y);
      }
      const int cc = 4 * t + g;
      if (i16 < 8) st_sc(tg4(y, t10), rsws,
                         WS_T1P + (unsigned)((((i16 * 32 + 4 * b + cc / 5) * 32 + h) * 5 + cc % 5) * 16));
    }
  } else if (w < 7) {
    const int t = tid - 256;
    const bool act = t < 160;
    const unsigned o = WS_T1P + (unsigned)((b * 32 + h) * 160 + (act ? t : 0)) * 16;
    until(tB2, GB);
    v4u v;
    for (unsigned spins = 0;; ++spins) {
      CFENCE();
      v = ld_sc(rsws, o);
      if (__all(tagged4(v, t10)) || spins > SPIN_G) break;
      __builtin_amdgcn_s_sleep(TS);
    }
    if (act) *(LAS f4*)(L + L_T4P + t * 16) = f4{untg(v.x), untg(v.y), untg(v.z), untg(v.w)};
  }
  bar();
  if (w == 4) {
    const int e = lane >> 3, c = lane & 7;
    f4 sm = {0, 0, 0, 0};
    if (c < 5) {
#pragma unroll
      for (int u = 0; u < 4; ++u) sm += *(const LAS f4*)(L + L_T4P + ((4 * e + u) * 5 + c) * 16);
    }
#pragma unroll
    for (int k = 0; k < 4; ++k) { float x = sm[k]; x += dpp<0x128>(x); sm[k] = xrow_sum(x); }
    if (lane < 5) {
      const LAS unsigned* vec = (const LAS unsigned*)(L + L_VEC);
      const unsigned g0 = vec[2 * lane], g1 = vec[2 * lane + 1], x0 = vec[20 + 2 * lane], x1 = vec[20 + 2 * lane + 1];
      const float r0 = rbf(blo(x0) + rbf(blo(g0) * rbf(sm[0]))), r1 = rbf(bhi(x0) + rbf(bhi(g0) * rbf(sm[1])));
      const float r2 = rbf(blo(x1) + rbf(blo(g1) * rbf(sm[2]))), r3 = rbf(bhi(x1) + rbf(bhi(g1) * rbf(sm[3])));
      st_sc2(v2u{canonu(pk2(r0, r1)), canonu(pk2(r2, r3))}, rsws, WS_X1B + (unsigned)(b * 320 + 10 * h + 2 * lane) * 4);
      *(LAS f4*)(L + L_X1O + lane * 16) = f4{r0, r1, r2, r3};
    }
  }
  bar();
  const unsigned tB = (unsigned)__builtin_amdgcn_s_memrealtime();
  STAMP(10);
  if (is_mlp) {
    {
      const unsigned o = WS_X1B + (unsigned)w * 1280 + lane * 4;
      unsigned cs[5], sh[5];
#pragma unroll
      for (int u = 0; u < 5; ++u) {
        cs[u] = *(const LAS unsigned*)(L + L_CS + (w * 320 + u * 64 + lane) * 4);
        sh[u] = *(const LAS unsigned*)(L + L_CS + 10240 + (w * 320 + u * 64 + lane) * 4);
      }
      until(tB, GC);
      unsigned xv[5];
      for (unsigned spins = 0;; ++spins) {
        CFENCE();
        bool ok = true;
#pragma unroll
        for (int u = 0; u < 5; ++u) xv[u] = __builtin_amdgcn_raw_buffer_load_b32(rsws, o + u * 256, 0, 16);
#pragma unroll
        for (int u = 0; u < 5; ++u) ok = ok && xv[u] != SENT;
        if (__all(ok) || spins > SPIN_G) break;
        __builtin_amdgcn_s_sleep(TS);
      }
      const float x00 = blo(rfl(xv[0]));
      float s1 = 0.f, s2 = 0.f;
#pragma unroll
      for (int u = 0; u < 5; ++u) {
        const float d0 = blo(xv[u]) - x00, d1 = bhi(xv[u]) - x00;
        s1 += d0 + d1; s2 = fmaf(d0, d0, fmaf(d1, d1, s2));
      }
      s1 = row16_sum(s1); s2 = row16_sum(s2);
      s1 = xrow_sum(s1) * (1.f / DM); s2 = xrow_sum(s2) * (1.f / DM);
      const float rs = __builtin_amdgcn_rsqf(fmaxf(s2 - s1 * s1, 0.f) + 1e-5f), mr = -(x00 + s1) * rs;
#pragma unroll
      for (int u = 0; u < 5; ++u) {
        const float n0 = fmaf(blo(xv[u]), rs, mr), n1 = fmaf(bhi(xv[u]), rs, mr);
        *(LAS unsigned*)(L + L_H2 + w * 1296 + (u * 64 + lane) * 4) = pk2(fmaf(n0, blo(cs[u]), blo(sh[u])), fmaf(n1, bhi(cs[u]), bhi(sh[u])));
      }
    }
    bar();
    STAMP(11);
    {
      const int t = w & 1, kq = w >> 1;
      f4 a = {0, 0, 0, 0};
      const LAS unsigned char* ap = L + L_RT + (160 * kq + 8 * g + tq) * 64 + t * 32 + tp * 8;
      const LAS unsigned char* bp = L + L_H2 + (lane & 7) * 1296 + (160 * kq + 8 * g) * 2;
#pragma unroll
      for (int s = 0; s < 5; ++s) {
        const v4u bb = *(const LAS v4u*)(bp + s * 64);
        const v2u x0 = trread(ap + s * 2048), x1 = trread(ap + s * 2048 + 256);
        a = mfma32(v4u{x0.x, x0.y, x1.x, x1.y}, bb, a);
      }
      if (i16 < 8) *(LAS f4*)(L + L_T4P + (((kq * 2 + t) * 4 + g) * 8 + i16) * 16) = a;
    }
    bar();
    if (tid < 128) {
      const int j = tid >> 4, c = 2 * (tid & 15), t = c >> 4, gg = (c & 15) >> 2, r = c & 3;
      float z0 = 0.f, z1 = 0.f;
#pragma unroll
      for (int kq = 0; kq < 4; ++kq) {
        const LAS float* z = (const LAS float*)(L + L_T4P + (((kq * 2 + t) * 4 + gg) * 8 + j) * 16) + r;
        z0 += z[0]; z1 += z[1];
      }
      *(LAS unsigned*)(L + L_H1 + j * 80 + c * 2) = pk2(silu(rbf(z0)), silu(rbf(z1)));
    }
    bar();
    STAMP(12);
    {
      const v4u bb = *(const LAS v4u*)(L + L_H1 + (lane & 7) * 80 + g * 16);
      const int r0 = 8 * g + tq;
      const LAS unsigned char* ap = L + L_W2 + r0 * 1280 + (tp & 1) * 8;
      const int f = 2 * (r0 & 3);
#pragma unroll
      for (int tt = 0; tt < 5; ++tt) {
        const int ch = (2 * (5 * w + tt) + (tp >> 1)) ^ f;
        const v2u x0 = trread(ap + ch * 16), x1 = trread(ap + 4 * 1280 + ch * 16);
        const f4 y = mfma32(v4u{x0.x, x0.y, x1.x, x1.y}, bb, f4{0, 0, 0, 0});
        if (i16 < 8) *(LAS v4u*)(L + L_RT + i16 * 2576 + (16 * (5 * w + tt) + 4 * g) * 4) = tg4(y, t10);
      }
    }
    bar();
    {
#pragma unroll
      for (int u = 0; u < 3; ++u) {
        const int pc = u * 512 + tid, j = pc / 160, c = pc - 160 * j;
        if (pc < 1280) st_sc(*(const LAS v4u*)(L + L_RT + j * 2576 + c * 16), rsws, WS_T5P + (unsigned)mm * 20480 + (unsigned)pc * 16);
      }
    }
  }
  const unsigned tD = (unsigned)__builtin_amdgcn_s_memrealtime();
  STAMP(13);
  if (w < 5) {
    const bool act = tid < NMLP * 5;
    const int dm = act ? tid / 5 : 0, dc = act ? tid - 5 * dm : 0;
    const unsigned o = WS_T5P + (unsigned)((dm * 8 + b) * 640 + 20 * h + 4 * dc) * 4;
    if (is_mlp) until(tD, GD1); else until(tB, GD2);
    v4u v;
    for (unsigned spins = 0;; ++spins) {
      CFENCE();
      v = ld_sc(rsws, o);
      if (__all(tagged4(v, t10)) || spins > SPIN_G) break;
      __builtin_amdgcn_s_sleep(TS);
    }
    if (act) *(LAS f4*)(L + L_W2 + tid * 16) = f4{untg(v.x), untg(v.y), untg(v.z), untg(v.w)};
  }
  bar();
  if (w == 0) {
    const int e = lane / 5, c = lane - 5 * e;
    f4 sm = {0, 0, 0, 0};
    if (lane < 45) {
#pragma unroll
      for (int u = 0; u < 6; ++u) sm += *(const LAS f4*)(L + L_W2 + ((6 * e + u) * 5 + c) * 16);
      *(LAS f4*)(L + L_W2 + 8192 + lane * 16) = sm;
    }
    asm volatile("s_waitcnt lgkmcnt(0)" ::: "memory");
    if (lane < 5) {
      sm = f4{0, 0, 0, 0};
#pragma unroll
      for (int u = 0; u < 9; ++u) sm += *(const LAS f4*)(L + L_W2 + 8192 + (u * 5 + lane) * 16);
      const LAS unsigned* vec = (const LAS unsigned*)(L + L_VEC);
      const unsigned g0 = vec[10 + 2 * lane], g1 = vec[10 + 2 * lane + 1];
      const f4 x1 = *(const LAS f4*)(L + L_X1O + lane * 16);
      const float r0 = x1[0] + rbf(blo(g0) * rbf(sm[0])), r1 = x1[1] + rbf(bhi(g0) * rbf(sm[1]));
      const float r2 = x1[2] + rbf(blo(g1) * rbf(sm[2])), r3 = x1[3] + rbf(bhi(g1) * rbf(sm[3]));
      *(GAS v2u*)(p.out + b * DM + 20 * h + 4 * lane) = v2u{pk2(r0, r1), pk2(r2, r3)};
    }
  }
  if (id == 0 && tid == 448) {
    const unsigned E = __builtin_amdgcn_raw_buffer_load_b32(rsws, WS_EPOCH, 0, 16);
    __builtin_amdgcn_raw_buffer_store_b32(E + 1u, rsws, WS_EPOCH, 0, 16);
  }
  if (w == 7) { while ((unsigned)__builtin_amdgcn_s_memrealtime() - tstart < 5000u) __builtin_amdgcn_s_sleep(2); }
  STAMP(15);
}

__global__ void __launch_bounds__(64) k_nop() { }
}

extern "C" void kernel_launch(void* const* d_in, const int* in_sizes, int n_in, void* d_out, int out_size, void* d_ws,
                              size_t ws_size, hipStream_t stream) {
  Params p;
  p.x = (const u16*)d_in[0];
  p.kc = (const u16*)d_in[1];
  p.vc = (const u16*)d_in[2];
  p.wq = (const u16*)d_in[3];
  p.wo = (const u16*)d_in[4];
  p.w1 = (const u16*)d_in[6];
  p.w2 = (const u16*)d_in[7];
  p.c = (const u16*)d_in[8];
  p.wmod = (const u16*)d_in[9];
  p.out = (u16*)d_out;
  p.ws = (unsigned char*)d_ws;
  hipLaunchKernelGGL(fused_block, dim3(256), dim3(512), 0, stream, p);
  hipLaunchKernelGGL(k_nop, dim3(1), dim3(64), 0, stream);
}
```

```cpp
#include <hip/hip_runtime.h>
#include <stdint.h>

#define DIAG 0

namespace {

typedef unsigned short u16;
typedef unsigned v4u __attribute__((ext_vector_type(4)));
typedef unsigned v2u __attribute__((ext_vector_type(2)));
typedef float f4 __attribute__((ext_vector_type(4)));
typedef short s4 __attribute__((ext_vector_type(4)));
typedef short s8 __attribute__((ext_vector_type(8)));
typedef __bf16 bf2 __attribute__((ext_vector_type(2)));
typedef __bf16 bf8 __attribute__((ext_vector_type(8)));
typedef __amdgpu_buffer_rsrc_t rsrc_t;
#define LAS __attribute__((address_space(3)))
#define GAS __attribute__((address_space(1)))
#define DI __device__ __forceinline__

constexpr int NB = 8, DM = 640, NH = 32, DH = 128, SK = 4096, WIN = 1024, FF = 1728;
constexpr int NSW = 7;
constexpr int NL = 37;
#define TD_A 8
#define TD_B 8
#define TD_C 16
#define TD_D 8
#define TD_D2 4
#define TS 4
#define ABL_CTRL 0
#define ABL_SLOT 0
#define ABL_ROLE 0
#define ABL_SENT 0
#define LEAD 3
#define MP0 12
#define MPACE 11
#define DELTA 0
#define HEAVY 0
#define ABL_G1 0
#define ABL_G2 0
#define ABL_G3 0
#define ABL_TAIL 0
#define GB 45
#define GC 30
#define GD1 45
#define GD2 170
#define SLOT0 18
#define SLOTP 3
#define WOLATE 0
#define WARM 1
#define INDIC 1
#define ABL_WO 0
#define ABL_W12 0
#define D1 600
#define D2 100
#define PSLEEP 32
#define DPACE 15
#define PACE_ 3
constexpr int PACE = PACE_;
#define NPRE_ 16
constexpr int NPRE = NPRE_;

constexpr int NMLP = 54;
constexpr unsigned WS_EPOCH = 0;
constexpr unsigned WS_INIT = 4096;
constexpr unsigned WS_MODG = 8192;
constexpr unsigned WS_QP = WS_MODG + NB * 1920 * 8;
constexpr unsigned WS_OX = WS_QP + NH * 8 * 8 * 128 * 4;
constexpr unsigned WS_T1P = WS_OX + NH * 8 * 256;
constexpr unsigned WS_X1B = WS_T1P + NB * NH * DM * 4;
constexpr unsigned WS_T5P = WS_X1B + NB * 320 * 4;
constexpr unsigned WS_END = WS_T5P + NMLP * NB * DM * 4;
constexpr unsigned WS_DIAG = 4u << 20;
constexpr unsigned SENT = 0xFFFFFFFFu;
static_assert(WS_END <= WS_DIAG, "workspace map");

constexpr unsigned L_RT = 0;
constexpr unsigned L_QT = 40960;
constexpr unsigned L_W2 = 61440;
constexpr unsigned L_T1 = 102400;
constexpr unsigned L_CS = 122880;
constexpr unsigned L_MISC = 143360;
constexpr unsigned L_FLAG = L_MISC;
constexpr unsigned L_Q = L_MISC + 64;
constexpr unsigned L_STAT = L_Q + 256;
constexpr unsigned L_VEC = L_STAT + 64;
constexpr unsigned L_X1O = L_VEC + 128;
constexpr unsigned L_SC = L_X1O + 128;
constexpr unsigned L_H2 = L_SC;
constexpr unsigned L_HS = L_SC + 8 * 1296;
constexpr unsigned L_MRG = L_HS + 8 * 176;
constexpr unsigned L_OS = L_MRG + 7 * 132 * 4;
constexpr unsigned L_T4P = L_MRG;
constexpr unsigned L_H1 = L_T4P + 4096;
constexpr unsigned L_END = 163840;
static_assert(L_OS + 8 * 272 <= L_END && L_H1 + 8 * 80 <= L_OS + 8 * 272, "LDS map");
static_assert((L_SC % 16) == 0 && (L_HS % 16) == 0 && (L_MRG % 16) == 0 && (L_OS % 16) == 0 && (L_H1 % 16) == 0, "LDS alignment");

constexpr unsigned SPIN_G = 1u << 16;
constexpr unsigned SPIN_L = 1u << 22;

struct Params {
  const u16 *x, *kc, *vc, *wq, *wo, *w1, *w2, *c, *wmod;
  u16* out;
  unsigned char* ws;
};

DI unsigned rfl(unsigned x) { return __builtin_amdgcn_readfirstlane(x); }
DI float asf(unsigned u) { return __uint_as_float(u); }
DI unsigned asu(float f) { return __float_as_uint(f); }
DI unsigned pk2(float lo, float hi) { bf2 v = {(__bf16)lo, (__bf16)hi}; return __builtin_bit_cast(unsigned, v); }
DI float rbf(float f) { return asf(pk2(f, 0.f) << 16); }
DI float blo(unsigned u) { return asf(u << 16); }
DI float bhi(unsigned u) { return asf(u & 0xffff0000u); }
DI float dot2(unsigned a, unsigned b, float c) {
  return __builtin_amdgcn_fdot2_f32_bf16(__builtin_bit_cast(bf2, a), __builtin_bit_cast(bf2, b), c, false);
}
template <int CTRL> DI float dpp(float x) {
  return __builtin_bit_cast(float, __builtin_amdgcn_mov_dpp(__builtin_bit_cast(int, x), CTRL, 0xf, 0xf, true));
}
DI float row16_sum(float x) { x += dpp<0x128>(x); x += dpp<0x124>(x); x += dpp<0x122>(x); x += dpp<0x121>(x); return x; }
DI float oct_sum(float x) { x += dpp<0xB1>(x); x += dpp<0x4E>(x); x += dpp<0x141>(x); return x; }
DI float x16_sum(float x) {
  auto s = __builtin_amdgcn_permlane16_swap(asu(x), asu(x), false, false);
  return asf(s[0]) + asf(s[1]);
}
DI float x32_sum(float x) {
  auto t = __builtin_amdgcn_permlane32_swap(asu(x), asu(x), false, false);
  return asf(t[0]) + asf(t[1]);
}
DI float xrow_sum(float x) { return x32_sum(x16_sum(x)); }
DI float xrow_max(float x) {
  auto s = __builtin_amdgcn_permlane16_swap(asu(x), asu(x), false, false);
  x = fmaxf(asf(s[0]), asf(s[1]));
  auto t = __builtin_amdgcn_permlane32_swap(asu(x), asu(x), false, false);
  return fmaxf(asf(t[0]), asf(t[1]));
}
DI float wave_sum(float x) { return xrow_sum(row16_sum(x)); }
DI f4 mfma32(v4u a, v4u b, f4 c) {
  return __builtin_amdgcn_mfma_f32_16x16x32_bf16(__builtin_bit_cast(bf8, a), __builtin_bit_cast(bf8, b), c, 0, 0, 0);
}
DI f4 mfma16(v2u a, v2u b, f4 c) {
  return __builtin_amdgcn_mfma_f32_16x16x16bf16_1k(__builtin_bit_cast(s4, a), __builtin_bit_cast(s4, b), c, 0, 0, 0);
}
DI v2u trread(const LAS unsigned char* p) {
  return __builtin_bit_cast(v2u, __builtin_amdgcn_ds_read_tr16_b64_v4i16((LAS s4*)p));
}
DI rsrc_t mkrs(const void* p, unsigned bytes) { return __builtin_amdgcn_make_buffer_rsrc((void*)p, 0, (int)bytes, 0x00020000); }
DI v4u ld_nt(rsrc_t r, unsigned vo, unsigned so) { return __builtin_amdgcn_raw_buffer_load_b128(r, vo, so, 2); }
#define KAUX 2
#define VAUX 2
DI v4u ld_k(rsrc_t r, unsigned vo, unsigned so) { return __builtin_amdgcn_raw_buffer_load_b128(r, vo, so, KAUX); }
DI v4u ld_v(rsrc_t r, unsigned vo, unsigned so) { return __builtin_amdgcn_raw_buffer_load_b128(r, vo, so, VAUX); }
DI v4u ld_sc(rsrc_t r, unsigned vo) { return __builtin_amdgcn_raw_buffer_load_b128(r, vo, 0, 16); }
DI v2u ld_sc2(rsrc_t r, unsigned vo) { return __builtin_amdgcn_raw_buffer_load_b64(r, vo, 0, 16); }
DI void st_sc(v4u v, rsrc_t r, unsigned vo) { __builtin_amdgcn_raw_buffer_store_b128(v, r, vo, 0, 16); }
DI void st_sc2(v2u v, rsrc_t r, unsigned vo) { __builtin_amdgcn_raw_buffer_store_b64(v, r, vo, 0, 16); }
DI unsigned canonu(unsigned u) { return u == 0xFFFFFFFFu ? 0x7FFF7FFFu : u; }
DI void dma16(rsrc_t r, unsigned lds, unsigned vo, unsigned so) {
  unsigned keep;
  asm volatile("s_nop 4\n\ts_mov_b32 %0, m0\n\ts_mov_b32 m0, %1\n\ts_nop 0\n\tbuffer_load_dwordx4 %2, %3, %4 offen nt lds\n\ts_mov_b32 m0, %0"
               : "=&s"(keep) : "s"(lds), "v"(vo), "s"(r), "s"(so) : "memory");
}
template <int N> DI void vmwait() { asm volatile("s_waitcnt vmcnt(%0)" :: "n"(N) : "memory"); }
#define VMWAIT(n) vmwait<(n)>()
DI void bar() { asm volatile("s_waitcnt lgkmcnt(0)\n\ts_barrier" ::: "memory"); }
#define CFENCE() asm volatile("" ::: "memory")
#define SCHED() __builtin_amdgcn_sched_barrier(0)
DI unsigned canon(float f) { unsigned u = asu(f); return u == SENT ? 0x7FFFFFFFu : u; }
DI unsigned tg(float f, unsigned t10) { return (asu(f) & ~0x3FFu) | t10; }
DI v4u tg4(f4 v, unsigned t10) { return v4u{tg(v[0], t10), tg(v[1], t10), tg(v[2], t10), tg(v[3], t10)}; }
DI bool tagged4(v4u v, unsigned t10) { return (((v.x ^ t10) | (v.y ^ t10) | (v.z ^ t10) | (v.w ^ t10)) & 0x3FFu) == 0u; }
DI float untg(unsigned u) { return asf(u & ~0x3FFu); }
DI bool valid4(v4u v) { return v.x != SENT && v.y != SENT && v.z != SENT && v.w != SENT; }
DI float rcp(float x) { return __builtin_amdgcn_rcpf(x); }
DI float silu(float v) { return v * rcp(1.f + __builtin_amdgcn_exp2f(v * -1.4426950408889634f)); }

#define STAMP(k) do { } while (0)
#define STAMP2(k) do { } while (0)
#define STAMP3(k) do { } while (0)
#define COUNT3(k, v) do { } while (0)

__global__ void __launch_bounds__(512, 2) fused_block(Params p) {
  __shared__ __attribute__((aligned(16))) unsigned char smem[L_END];
  LAS unsigned char* L = (LAS unsigned char*)smem;
  const unsigned lds0 = (unsigned)(uintptr_t)L;
  const int tid = threadIdx.x, lane = tid & 63;
  const int w = (int)rfl(tid >> 6);
  const int id = blockIdx.x, xcd = id & 7, sl = id >> 3;
  const int h = xcd * 4 + (sl >> 3), b = sl & 7;
  const int g = lane >> 4, i16 = lane & 15;
  const int tq = i16 >> 2, tp = lane & 3;
  const rsrc_t rsws = mkrs(p.ws, WS_END);
  volatile LAS unsigned* flags = (volatile LAS unsigned*)(L + L_FLAG);
  const int mpr = (sl >> 1) * 8 + xcd;
  const bool has_mod = !ABL_ROLE && sl < 16 && mpr < 60;
  const bool crit = has_mod && mpr < 20;
  const int mcol0 = (2 * mpr + (sl & 1)) * 32;
  const int fpr = ((sl - 16) >> 1) * 8 + xcd;
  const bool is_mlp = !ABL_ROLE && sl >= 16 && sl < 24 && fpr < 27;
  const int mm = 2 * fpr + (sl & 1);

  const rsrc_t rswo = mkrs(p.wo + (size_t)h * DH * DM, DH * DM * 2);
  unsigned wov[5];
  STAMP(0);
  const unsigned tstart = (unsigned)__builtin_amdgcn_s_memrealtime();
  auto sched = [&](int i, bool light) {
    if (DELTA && light) { while ((unsigned)__builtin_amdgcn_s_memrealtime() - tstart < (unsigned)(i * DELTA / 10)) __builtin_amdgcn_s_sleep(1); }
  };

  if (w < NSW) {
    const rsrc_t rsk = mkrs(p.kc + ((size_t)(b * NH + h) * SK + (SK - WIN)) * DH, WIN * DH * 2);
    const rsrc_t rsv = mkrs(p.vc + ((size_t)(b * NH + h) * SK + (SK - WIN)) * DH, WIN * DH * 2);
    const unsigned vo = (unsigned)(lane * 16 + w * 1024);
    v4u kr[NL], vr[NL], cr[2];
    if (has_mod && w < 5) {
      const GAS v4u* cp = (const GAS v4u*)p.c;
      cr[0] = cp[w * 128 + lane];
      cr[1] = cp[w * 128 + 64 + lane];
    }
    auto silu_c = [&]() {
      if (w < 5) {
#pragma unroll
        for (int u = 0; u < 2; ++u) {
          const int q = w * 128 + u * 64 + lane, j = q / 80, k8 = q - 80 * j;
          v4u o;
          o.x = pk2(silu(blo(cr[u].x)), silu(bhi(cr[u].x)));
          o.y = pk2(silu(blo(cr[u].y)), silu(bhi(cr[u].y)));
          o.z = pk2(silu(blo(cr[u].z)), silu(bhi(cr[u].z)));
          o.w = pk2(silu(blo(cr[u].w)), silu(bhi(cr[u].w)));
          *(LAS v4u*)(L + L_SC + j * 1296 + k8 * 16) = o;
        }
      }
    };
    const rsrc_t rsrt = mkrs(p.wmod, DM * 6 * DM * 2);
    const unsigned rt_vo = (unsigned)((lane >> 2) * 7680 + mcol0 * 2 + (lane & 3) * 16);
    auto dma_rt = [&](int m) { dma16(rsrt, lds0 + L_RT + m * 1024, rt_vo, (unsigned)m * 16u * 7680u); };
    if (!crit) {
#pragma unroll
      for (int i = 0; i < PACE; ++i) kr[i] = ld_k(rsk, vo, i * 7168);
    }
    if (has_mod) { for (int m = w; m < 40; m += NSW) dma_rt(m); }
    SCHED();
    bar();
    if (has_mod) {
      silu_c();
      VMWAIT(0);
      asm volatile("s_waitcnt lgkmcnt(0)" ::: "memory");
      if (lane == 0) __hip_atomic_fetch_add((LAS unsigned*)(L + L_FLAG), 1u, __ATOMIC_RELAXED, __HIP_MEMORY_SCOPE_WORKGROUP);
    }
    {
      const rsrc_t rsq = mkrs(p.wq, DM * NH * DH * 2);
      for (int n = w; n < 20; n += NSW) {
        const int rl = 4 * n + g;
        dma16(rsq, lds0 + L_QT + n * 1024, (unsigned)((80 * b + rl) * 8192 + h * 256 + ((i16 ^ (2 * (rl & 7))) * 16)), 0);
      }
    }
    if (crit) {
#pragma unroll
      for (int i = 0; i < PACE; ++i) kr[i] = ld_k(rsk, vo, i * 7168);
    }
    SCHED();
    const rsrc_t rsw1 = mkrs(p.w1, DM * FF * 2);
    const rsrc_t rsw2 = mkrs(p.w2 + (size_t)(is_mlp ? mm : 0) * 32 * DM, 32 * DM * 2);
    auto slot = [&](int s) {
      const int n = 7 * s + w;
      if (n < 40) {
        dma16(rsw1, lds0 + L_RT + n * 1024, (unsigned)((lane >> 2) * (FF * 2) + mm * 64 + (lane & 3) * 16), (unsigned)n * 16u * (FF * 2));
      } else if (n < 80) {
        const int ci = (n - 40) * 64 + lane, row = ci / 80, pos = ci - 80 * row;
        dma16(rsw2, lds0 + L_W2 + (n - 40) * 1024, (unsigned)(row * 1280 + (pos ^ (2 * (row & 3))) * 16), 0);
      }
    };
    static_assert(SLOT0 + 11 * SLOTP < NL + NPRE - 1, "all slots inside phase A");
    const bool heavy = HEAVY == 1 ? (is_mlp || has_mod) : HEAVY == 2 ? is_mlp : HEAVY == 3 ? has_mod : false;
#pragma unroll
    for (int i = PACE; i < NL + NPRE; ++i) {
      sched(i, !is_mlp);
      if (heavy) VMWAIT(PACE); else VMWAIT(PACE - 1);
      if (i < NL) kr[i] = ld_k(rsk, vo, i * 7168); else vr[i - NL] = ld_v(rsv, vo, (i - NL) * 7168);
      SCHED();
      if (i == PACE + 4) {
        asm volatile("s_waitcnt lgkmcnt(0)" ::: "memory");
        if (lane == 0) __hip_atomic_fetch_add((LAS unsigned*)(L + L_FLAG) + 2, 1u, __ATOMIC_RELAXED, __HIP_MEMORY_SCOPE_WORKGROUP);
        STAMP(1);
      }
      if (i >= SLOT0 && ((i - SLOT0) % SLOTP) == 0 && (i - SLOT0) / SLOTP < 12) { if (is_mlp) slot((i - SLOT0) / SLOTP); SCHED(); }
    }
    STAMP(2);
    {
      unsigned spins = 0;
      while (flags[1] == 0u) { __builtin_amdgcn_s_sleep(1); if (++spins > SPIN_L) break; }
    }
    CFENCE();
    STAMP(3);
    const v4u q4 = *(const LAS v4u*)(L + L_Q + i16 * 16);
    float s[NL];
#pragma unroll
    for (int i = 0; i < NL; ++i) {
      float d = dot2(kr[i].x, q4.x, 0.f);
      d = dot2(kr[i].y, q4.y, d); d = dot2(kr[i].z, q4.z, d); d = dot2(kr[i].w, q4.w, d);
      s[i] = row16_sum(d);
      if ((i & 3) == 3 && NPRE + (i >> 2) < NL) { SCHED(); sched(NL + NPRE + (i >> 2), !is_mlp); if (heavy) VMWAIT(PACE); else VMWAIT(PACE - 1); vr[NPRE + (i >> 2)] = ld_v(rsv, vo, (NPRE + (i >> 2)) * 7168); SCHED(); }
    }
    constexpr int NV1 = NPRE + (NL >> 2);
    if (w >= 4) s[NL - 1] = -INFINITY;
    float m = s[0];
#pragma unroll
    for (int i = 1; i < NL; ++i) m = fmaxf(m, s[i]);
    m = xrow_max(m);
    constexpr float C2 = 0.08838834764831845f * 1.4426950408889634f;
    const float mc = m * C2;
    unsigned pp[NL];
    float l = 0.f;
#pragma unroll
    for (int i = 0; i < NL; ++i) {
      pp[i] = pk2(0.f, __builtin_amdgcn_exp2f(fmaf(s[i], C2, -mc)));
      l += asf(pp[i]);
    }
    l = xrow_sum(l);
    STAMP(4);
    float acc[8];
#pragma unroll
    for (int k = 0; k < 8; ++k) acc[k] = 0.f;
#pragma unroll
    for (int i = 0; i < NL; ++i) {
      const unsigned p1 = pp[i], p0 = p1 >> 16;
      acc[0] = dot2(vr[i].x, p0, acc[0]); acc[1] = dot2(vr[i].x, p1, acc[1]);
      acc[2] = dot2(vr[i].y, p0, acc[2]); acc[3] = dot2(vr[i].y, p1, acc[3]);
      acc[4] = dot2(vr[i].z, p0, acc[4]); acc[5] = dot2(vr[i].z, p1, acc[5]);
      acc[6] = dot2(vr[i].w, p0, acc[6]); acc[7] = dot2(vr[i].w, p1, acc[7]);
      SCHED();
      if (NV1 + i < NL) { sched(NL + NV1 + i, !is_mlp); if (heavy) VMWAIT(PACE); else VMWAIT(PACE - 1); vr[NV1 + i] = ld_v(rsv, vo, (NV1 + i) * 7168); SCHED(); }
      if (NV1 + i == NL - 1 - LEAD) {
        if (lane == 0) __hip_atomic_fetch_add((LAS unsigned*)(L + L_FLAG) + 3, 1u, __ATOMIC_RELAXED, __HIP_MEMORY_SCOPE_WORKGROUP);
      }
    }
    STAMP(5);
#pragma unroll
    for (int k = 0; k < 8; ++k) acc[k] = xrow_sum(acc[k]);
    if (g == 0) {
      LAS float* mr = (LAS float*)(L + L_MRG) + w * 132;
      *(LAS f4*)(mr + 4 + 8 * i16) = f4{acc[0], acc[1], acc[2], acc[3]};
      *(LAS f4*)(mr + 8 + 8 * i16) = f4{acc[4], acc[5], acc[6], acc[7]};
      if (i16 == 0) { mr[0] = m; mr[1] = l; }
    }
  } else {
    __builtin_amdgcn_s_setprio(3);
    if (lane == 0) { flags[0] = 0u; flags[1] = 0u; flags[2] = 0u; flags[3] = 0u; flags[5] = 0u; flags[6] = 0u; }
    if (ABL_CTRL) {
      bar();
      while ((unsigned)__builtin_amdgcn_s_memrealtime() - tstart < 800) __builtin_amdgcn_s_sleep(8);
      if (lane == 0) flags[1] = 1u;
    } else {
    const unsigned E = __builtin_amdgcn_raw_buffer_load_b32(rsws, WS_EPOCH, 0, 16);
    unsigned warm = 0;
    {
      const int n = sl * 64 + lane, pi = n / 640, row = n - 640 * pi, pr = xcd + 8 * pi;
      if (WARM && pr < 20) warm = __builtin_amdgcn_raw_buffer_load_b32(mkrs(p.wmod, DM * 6 * DM * 2), (unsigned)(row * 7680 + pr * 128), 0, 2);
    }
    const GAS v4u* xp = (const GAS v4u*)p.x;
    v4u xr[10];
    const int sj = lane >> 3, sub = lane & 7;
#pragma unroll
    for (int u = 0; u < 10; ++u) xr[u] = xp[sj * 80 + sub + 8 * u];
    SCHED();
    bar();
    unsigned xh[5];
#pragma unroll
    for (int u = 0; u < 5; ++u) { const int idx = u * 64 + lane, j = idx / 40, m = idx - 40 * j; xh[u] = ((const GAS unsigned*)p.x)[j * 320 + 40 * b + m]; }
    if (!ABL_SENT) {
      const v4u sv = {SENT, SENT, SENT, SENT};
      if (lane < 16) st_sc(sv, rsws, WS_OX + (unsigned)(h * 8 + b) * 256 + lane * 16);
      if (lane < 5) st_sc2(v2u{SENT, SENT}, rsws, WS_X1B + (unsigned)(b * 320 + 10 * h) * 4 + lane * 8);
    }
    STAMP(1);
    {
      float sm = 0.f;
#pragma unroll
      for (int u = 0; u < 10; ++u)
        sm += ((blo(xr[u].x) + bhi(xr[u].x)) + (blo(xr[u].y) + bhi(xr[u].y))) + ((blo(xr[u].z) + bhi(xr[u].z)) + (blo(xr[u].w) + bhi(xr[u].w)));
      const float mean = oct_sum(sm) * (1.f / DM);
      float sq = 0.f;
#pragma unroll
      for (int u = 0; u < 10; ++u) {
#pragma unroll
        for (int k = 0; k < 4; ++k) { const float a = blo(xr[u][k]) - mean, c2 = bhi(xr[u][k]) - mean; sq = fmaf(a, a, sq); sq = fmaf(c2, c2, sq); }
      }
      const float var = oct_sum(sq) * (1.f / DM);
      if (sub == 0) {
        LAS float* st = (LAS float*)(L + L_STAT) + sj * 2;
        st[0] = rbf(mean);
        st[1] = rcp(rbf(__builtin_amdgcn_sqrtf(rbf(rbf(var) + 1e-5f))));
      }
    }
    VMWAIT(0);
    asm volatile("" :: "v"(warm));
    const unsigned T = rfl(E) + 1u;
    const unsigned t10 = T & 0x3FFu;
    if (lane == 0) flags[4] = T;
    if (lane == 0) st_sc2(v2u{T, ~T}, rsws, WS_INIT + (unsigned)(h * 8 + b) * 8);
    STAMP2(9);
    if (has_mod) {
      unsigned spins = 0;
      while (flags[0] < (unsigned)NSW) { __builtin_amdgcn_s_sleep(1); if (++spins > SPIN_L) break; }
      CFENCE();
      STAMP(2);
      f4 a0 = {0, 0, 0, 0}, a1 = {0, 0, 0, 0};
      const LAS unsigned char* ap = L + L_RT + (8 * g + tq) * 64 + tp * 8;
      const LAS unsigned char* bp = L + L_SC + (lane & 7) * 1296 + g * 16;
#pragma unroll 2
      for (int s = 0; s < 20; ++s) {
        const v4u bb = *(const LAS v4u*)(bp + s * 64);
        const v2u x0 = trread(ap + s * 2048), x1 = trread(ap + s * 2048 + 256);
        const v2u y0 = trread(ap + s * 2048 + 32), y1 = trread(ap + s * 2048 + 256 + 32);
        a0 = mfma32(v4u{x0.x, x0.y, x1.x, x1.y}, bb, a0);
        a1 = mfma32(v4u{y0.x, y0.y, y1.x, y1.y}, bb, a1);
      }
      if (i16 < 8) {
        const unsigned o = WS_MODG + (unsigned)(i16 * 1920 + (mcol0 + 4 * g) / 2) * 8;
        st_sc(v4u{pk2(a0[0], a0[1]), T, pk2(a0[2], a0[3]), T}, rsws, o);
        st_sc(v4u{pk2(a1[0], a1[1]), T, pk2(a1[2], a1[3]), T}, rsws, o + 64);
      }
    }
    STAMP(3);
    {
      v2u sa[5], ca[5], fl;
      if (D1) { while ((unsigned)__builtin_amdgcn_s_memrealtime() - tstart < D1) __builtin_amdgcn_s_sleep(2); }
      STAMP2(10);
      if (ABL_G1) { for (int u = 0; u < 5; ++u) { sa[u] = v2u{0u, T}; ca[u] = v2u{0u, T}; } }
      for (unsigned spins = 0; !ABL_G1; ++spins) {
        CFENCE();
        bool ok = true;
#pragma unroll
        for (int u = 0; u < 5; ++u) {
          const int idx = u * 64 + lane, j = idx / 40, m = idx - 40 * j;
          const unsigned o = WS_MODG + (unsigned)(j * 1920 + 40 * b + m) * 8;
          sa[u] = ld_sc2(rsws, o);
          ca[u] = ld_sc2(rsws, o + 320 * 8);
        }
        fl = ld_sc2(rsws, WS_INIT + (unsigned)(h * 8 + (lane & 7)) * 8);
#pragma unroll
        for (int u = 0; u < 5; ++u) ok = ok && sa[u].y == T && ca[u].y == T;
        ok = ok && fl.x == T && fl.y == ~T;
        if (__all(ok) || spins > SPIN_G) break;
        __builtin_amdgcn_s_sleep(PSLEEP);
      }
      STAMP2(11);
      asm volatile("s_waitcnt lgkmcnt(0)" ::: "memory");
#pragma unroll
      for (int u = 0; u < 5; ++u) {
        const int idx = u * 64 + lane, j = idx / 40, m = idx - 40 * j;
        const float mean = ((LAS float*)(L + L_STAT))[2 * j], den = ((LAS float*)(L + L_STAT))[2 * j + 1];
        const float n0 = rbf(rbf(blo(xh[u]) - mean) * den), n1 = rbf(rbf(bhi(xh[u]) - mean) * den);
        const float h0 = rbf(n0 * rbf(1.f + blo(ca[u].x))) + blo(sa[u].x), h1 = rbf(n1 * rbf(1.f + bhi(ca[u].x))) + bhi(sa[u].x);
        *(LAS unsigned*)(L + L_HS + j * 176 + m * 4) = pk2(h0, h1);
      }
    }
    STAMP(4);
    {
      unsigned spins = 0;
      while (flags[2] < (unsigned)NSW) { __builtin_amdgcn_s_sleep(1); if (++spins > SPIN_L) break; }
      CFENCE();
    }
    STAMP2(12);
    {
      f4 qa[8];
#pragma unroll
      for (int t = 0; t < 8; ++t) qa[t] = f4{0, 0, 0, 0};
#pragma unroll
      for (int s = 0; s < 5; ++s) {
        const v2u bb = *(const LAS v2u*)(L + L_HS + (lane & 7) * 176 + s * 32 + g * 8);
        const int row = 16 * s + 4 * g + tq;
        const LAS unsigned char* rp = L + L_QT + row * 256 + (tp & 1) * 8;
        const int sw = 2 * (row & 7);
#pragma unroll
        for (int t = 0; t < 8; ++t) qa[t] = mfma16(trread(rp + (((2 * t + (tp >> 1)) ^ sw) * 16)), bb, qa[t]);
      }
      if (i16 < 8 && !ABL_G2) {
        const unsigned o = WS_QP + (unsigned)(((h * 8 + b) * 8 + i16) * 128 + 4 * g) * 4;
#pragma unroll
        for (int t = 0; t < 8; ++t) st_sc(tg4(qa[t], t10), rsws, o + t * 64);
      }
    }
    STAMP(5);
    const unsigned tpub = (unsigned)__builtin_amdgcn_s_memrealtime();
    {
      v4u qv[4];
      if (D2) { while ((unsigned)__builtin_amdgcn_s_memrealtime() - tpub < D2) __builtin_amdgcn_s_sleep(2); }
      STAMP2(13);
      if (ABL_G2) { for (int u = 0; u < 4; ++u) qv[u] = v4u{t10, t10, t10, t10}; }
      for (unsigned spins = 0; !ABL_G2; ++spins) {
        CFENCE();
        bool ok = true;
#pragma unroll
        for (int u = 0; u < 4; ++u) qv[u] = ld_sc(rsws, WS_QP + (unsigned)(((h * 8 + 2 * u + (lane >> 5)) * 8 + b) * 128 + 4 * (lane & 31)) * 4);
#pragma unroll
        for (int u = 0; u < 4; ++u) ok = ok && tagged4(qv[u], t10);
        if (__all(ok) || spins > SPIN_G) break;
        __builtin_amdgcn_s_sleep(PSLEEP);
      }
      f4 qs;
#pragma unroll
      for (int k = 0; k < 4; ++k) qs[k] = x32_sum((untg(qv[0][k]) + untg(qv[1][k])) + (untg(qv[2][k]) + untg(qv[3][k])));
      if (lane < 32) *(LAS v2u*)(L + L_Q + lane * 8) = v2u{pk2(qs[0], qs[1]), pk2(qs[2], qs[3])};
      asm volatile("s_waitcnt lgkmcnt(0)" ::: "memory");
      if (lane == 0) flags[1] = 1u;
    }
    STAMP(6);
    if (!ABL_G3) {
      const unsigned go = WS_MODG + (unsigned)(b * 1920 + (lane < 10 ? 640 + 10 * h + lane : 1600 + 10 * h + (lane < 20 ? lane - 10 : 0))) * 8;
      const unsigned xv = ((const GAS unsigned*)p.x)[b * 320 + 10 * h + (lane < 10 ? lane : 0)];
      v2u gv;
      for (unsigned spins = 0;; ++spins) {
        CFENCE();
        gv = ld_sc2(rsws, go);
        if (__all(gv.y == T) || spins > SPIN_G) break;
        __builtin_amdgcn_s_sleep(32);
      }
      if (lane < 20) *(LAS unsigned*)(L + L_VEC + lane * 4) = gv.x;
      if (lane < 10) *(LAS unsigned*)(L + L_VEC + 80 + lane * 4) = xv;
      if (is_mlp) {
        for (unsigned spins = 0;; ++spins) {
          CFENCE();
          bool ok = true;
#pragma unroll
          for (int u = 0; u < 4; ++u) { const v2u f = ld_sc2(rsws, WS_INIT + (unsigned)(u * 64 + lane) * 8); ok = ok && f.x == T && f.y == ~T; }
          if (__all(ok) || spins > SPIN_G) break;
          __builtin_amdgcn_s_sleep(32);
        }
        for (int j = 0; j < 8; ++j) {
          v4u cv[5];
          for (unsigned spins = 0;; ++spins) {
            CFENCE();
            bool ok = true;
#pragma unroll
            for (int u = 0; u < 5; ++u) cv[u] = ld_sc(rsws, WS_MODG + (unsigned)(j * 1920 + 960) * 8 + (unsigned)(u * 64 + lane) * 16);
#pragma unroll
            for (int u = 0; u < 5; ++u) ok = ok && cv[u].y == T && cv[u].w == T;
            if (__all(ok) || spins > SPIN_G) break;
            __builtin_amdgcn_s_sleep(32);
          }
#pragma unroll
          for (int u = 0; u < 5; ++u) {
            const int pc = u * 64 + lane;
            const unsigned o = (pc < 160 ? 10240u + (unsigned)(j * 320 + 2 * pc) * 4 : (unsigned)(j * 320 + 2 * (pc - 160)) * 4);
            const v2u sc1 = {pk2(1.f + blo(cv[u].x), 1.f + bhi(cv[u].x)), pk2(1.f + blo(cv[u].z), 1.f + bhi(cv[u].z))};
            *(LAS v2u*)(L + L_CS + o) = pc < 160 ? v2u{cv[u].x, cv[u].z} : sc1;
          }
        }
      }
    }
    STAMP(7);
#pragma unroll
    for (int c = 0; c < 5; ++c) { const int ci = c * 64 + lane, row = ci / 10, ch = ci - 10 * row; wov[c] = (unsigned)(row * 1280 + b * 160 + ch * 16); asm volatile("" : "+v"(wov[c])); }
    if (!WOLATE) {
      unsigned spins = 0;
      while (flags[3] < (unsigned)NSW) { __builtin_amdgcn_s_sleep(1); if (++spins > SPIN_L) break; }
      CFENCE();
#pragma unroll
      for (int n = 0; n < 20; ++n) dma16(rswo, lds0 + L_T1 + n * 1024, wov[n % 5], (unsigned)(n / 5) * 32u * 1280u);
    }
    }
  }

  bar();
  STAMP(8);
  STAMP3(1);
  if (ABL_CTRL || ABL_TAIL) {
    STAMP(15);
    return;
  }
  const unsigned t10 = flags[4] & 0x3FFu;
  auto until = [&](unsigned t0, unsigned ticks) { while ((unsigned)__builtin_amdgcn_s_memrealtime() - t0 < ticks) __builtin_amdgcn_s_sleep(1); };
  auto lds_wait = [&](int k, unsigned v) { unsigned spins = 0; while (flags[k] < v) { if (++spins > SPIN_L) break; } CFENCE(); };
  const int t1t = w == 0 ? 0 : w - 2;
  v4u af[4];
  if (w == 0) {
    if (lane < 32) {
      const LAS float* mr = (const LAS float*)(L + L_MRG);
      float mw[NSW], lw[NSW];
      f4 aw[NSW];
#pragma unroll
      for (int u = 0; u < NSW; ++u) { mw[u] = mr[u * 132]; lw[u] = mr[u * 132 + 1]; aw[u] = *(const LAS f4*)(mr + u * 132 + 4 + 4 * lane); }
      const float M = fmaxf(fmaxf(fmaxf(mw[0], mw[1]), fmaxf(mw[2], mw[3])), fmaxf(fmaxf(mw[4], mw[5]), mw[6]));
      constexpr float C2 = 0.08838834764831845f * 1.4426950408889634f;
      float Lsum = 0.f;
      f4 o = {0, 0, 0, 0};
#pragma unroll
      for (int u = 0; u < NSW; ++u) {
        const float f = __builtin_amdgcn_exp2f((mw[u] - M) * C2);
        Lsum = fmaf(f, lw[u], Lsum);
        o += f * aw[u];
      }
      const float inv = rcp(Lsum);
      st_sc2(v2u{canonu(pk2(o[0] * inv, o[1] * inv)), canonu(pk2(o[2] * inv, o[3] * inv))}, rsws, WS_OX + (unsigned)(h * 8 + b) * 256 + lane * 8);
    }
    STAMP3(2);
  }
  if (w == 1 || w == 2) {
    const int t = tid - 64;
    __builtin_amdgcn_s_sleep(TD_A);
    STAMP3(2);
    v4u v;
    for (unsigned spins = 0;; ++spins) {
      CFENCE();
      v = ld_sc(rsws, WS_OX + (unsigned)h * 2048 + t * 16);
      if (__all(valid4(v)) || spins > SPIN_G) { COUNT3(4, spins + 1); break; }
      __builtin_amdgcn_s_sleep(TS);
    }
    STAMP3(3);
    *(LAS v4u*)(L + L_OS + (t >> 4) * 272 + (t & 15) * 16) = v;
  } else if (w == 7) {
    if (WOLATE) {
#pragma unroll
      for (int n = 0; n < 20; ++n) dma16(rswo, lds0 + L_T1 + n * 1024, wov[n % 5], (unsigned)(n / 5) * 32u * 1280u);
    }
    VMWAIT(0);
    if (lane == 0) flags[5] = 1u;
  } else {
    lds_wait(5, 1u);
    const LAS unsigned char* ap = L + L_T1 + (8 * g + tq) * 160 + (16 * t1t + 4 * tp) * 2;
#pragma unroll
    for (int s = 0; s < 4; ++s) {
      const v2u x0 = trread(ap + s * 32 * 160), x1 = trread(ap + s * 32 * 160 + 4 * 160);
      af[s] = v4u{x0.x, x0.y, x1.x, x1.y};
    }
  }
  bar();
  const unsigned tB2 = (unsigned)__builtin_amdgcn_s_memrealtime();
  STAMP(9);
  STAMP3(5);
  if (w == 0 || (w >= 3 && w < 7)) {
    const LAS unsigned char* bp = L + L_OS + (lane & 7) * 272 + g * 16;
    v4u bb[4];
#pragma unroll
    for (int s = 0; s < 4; ++s) bb[s] = *(const LAS v4u*)(bp + s * 64);
    f4 y0 = mfma32(af[0], bb[0], f4{0, 0, 0, 0}), y1 = mfma32(af[1], bb[1], f4{0, 0, 0, 0});
    y0 = mfma32(af[2], bb[2], y0); y1 = mfma32(af[3], bb[3], y1);
    const int cc = 4 * t1t + g;
    if (i16 < 8) st_sc(tg4(y0 + y1, t10), rsws, WS_T1P + (unsigned)((((i16 * 32 + 4 * b + cc / 5) * 32 + h) * 5 + cc % 5) * 16));
    if (w == 0 || w == 3) STAMP3(6);
  }
  v4u a4[5], a5[5];
  if (is_mlp) {
    const LAS unsigned char* ap = L + L_RT + (160 * (w >> 1) + 8 * g + tq) * 64 + (w & 1) * 32 + tp * 8;
#pragma unroll
    for (int s = 0; s < 5; ++s) {
      const v2u x0 = trread(ap + s * 2048), x1 = trread(ap + s * 2048 + 256);
      a4[s] = v4u{x0.x, x0.y, x1.x, x1.y};
    }
    const int r0 = 8 * g + tq;
    const LAS unsigned char* wp = L + L_W2 + r0 * 1280 + (tp & 1) * 8;
    const int f = 2 * (r0 & 3);
#pragma unroll
    for (int tt = 0; tt < 5; ++tt) {
      const int ch = (2 * (5 * w + tt) + (tp >> 1)) ^ f;
      const v2u x0 = trread(wp + ch * 16), x1 = trread(wp + 4 * 1280 + ch * 16);
      a5[tt] = v4u{x0.x, x0.y, x1.x, x1.y};
    }
  }
  if (w >= 4 && w < 7) {
    const int c = 2 * (w - 4) + (lane >> 5), pr = lane & 31;
    const bool act = c < 5;
    const unsigned o = WS_T1P + (unsigned)(((b * 32 + h) * 32 + pr) * 5 + (act ? c : 0)) * 16;
    const LAS unsigned* vec = (const LAS unsigned*)(L + L_VEC);
    const int cv = act ? c : 0;
    const unsigned g0 = vec[2 * cv], g1 = vec[2 * cv + 1], x0 = vec[20 + 2 * cv], x1 = vec[20 + 2 * cv + 1];
    until(tB2, GB);
    STAMP3(6);
    v4u v;
    for (unsigned spins = 0;; ++spins) {
      CFENCE();
      v = ld_sc(rsws, o);
      if (__all(tagged4(v, t10)) || spins > SPIN_G) { COUNT3(8, spins + 1); STAMP3(7); break; }
      __builtin_amdgcn_s_sleep(TS);
    }
    f4 sm;
#pragma unroll
    for (int k = 0; k < 4; ++k) sm[k] = x16_sum(row16_sum(untg(v[k])));
    const float r0 = rbf(blo(x0) + rbf(blo(g0) * rbf(sm[0]))), r1 = rbf(bhi(x0) + rbf(bhi(g0) * rbf(sm[1])));
    const float r2 = rbf(blo(x1) + rbf(blo(g1) * rbf(sm[2]))), r3 = rbf(bhi(x1) + rbf(bhi(g1) * rbf(sm[3])));
    if (pr == 0 && act) {
      st_sc2(v2u{canonu(pk2(r0, r1)), canonu(pk2(r2, r3))}, rsws, WS_X1B + (unsigned)(b * 320 + 10 * h + 2 * c) * 4);
      *(LAS f4*)(L + L_X1O + c * 16) = f4{r0, r1, r2, r3};
    }
    asm volatile("s_waitcnt lgkmcnt(0)" ::: "memory");
    if (lane == 0) __hip_atomic_fetch_add((LAS unsigned*)(L + L_FLAG) + 6, 1u, __ATOMIC_RELAXED, __HIP_MEMORY_SCOPE_WORKGROUP);
  }
  lds_wait(6, 3u);
  const unsigned tB = (unsigned)__builtin_amdgcn_s_memrealtime();
  STAMP(10);
  STAMP3(10);
  if (is_mlp) {
    {
      const unsigned o = WS_X1B + (unsigned)w * 1280 + lane * 4;
      unsigned cs[5], sh[5];
#pragma unroll
      for (int u = 0; u < 5; ++u) {
        cs[u] = *(const LAS unsigned*)(L + L_CS + (w * 320 + u * 64 + lane) * 4);
        sh[u] = *(const LAS unsigned*)(L + L_CS + 10240 + (w * 320 + u * 64 + lane) * 4);
      }
      until(tB, GC);
      STAMP3(2);
      unsigned xv[5];
      for (unsigned spins = 0;; ++spins) {
        CFENCE();
        bool ok = true;
#pragma unroll
        for (int u = 0; u < 5; ++u) xv[u] = __builtin_amdgcn_raw_buffer_load_b32(rsws, o + u * 256, 0, 16);
#pragma unroll
        for (int u = 0; u < 5; ++u) ok = ok && xv[u] != SENT;
        if (__all(ok) || spins > SPIN_G) { COUNT3(4, spins + 1); STAMP3(3); break; }
        __builtin_amdgcn_s_sleep(TS);
      }
      const float x00 = blo(rfl(xv[0]));
      float s1 = 0.f, s2 = 0.f;
#pragma unroll
      for (int u = 0; u < 5; ++u) {
        const float d0 = blo(xv[u]) - x00, d1 = bhi(xv[u]) - x00;
        s1 += d0 + d1; s2 = fmaf(d0, d0, fmaf(d1, d1, s2));
      }
      s1 = row16_sum(s1); s2 = row16_sum(s2);
      s1 = xrow_sum(s1) * (1.f / DM); s2 = xrow_sum(s2) * (1.f / DM);
      const float rs = __builtin_amdgcn_rsqf(fmaxf(s2 - s1 * s1, 0.f) + 1e-5f), mr = -(x00 + s1) * rs;
#pragma unroll
      for (int u = 0; u < 5; ++u) {
        const float n0 = fmaf(blo(xv[u]), rs, mr), n1 = fmaf(bhi(xv[u]), rs, mr);
        *(LAS unsigned*)(L + L_H2 + w * 1296 + (u * 64 + lane) * 4) = pk2(fmaf(n0, blo(cs[u]), blo(sh[u])), fmaf(n1, bhi(cs[u]), bhi(sh[u])));
      }
    }
    STAMP3(11);
    bar();
    STAMP(11);
    {
      const LAS unsigned char* bp = L + L_H2 + (lane & 7) * 1296 + (160 * (w >> 1) + 8 * g) * 2;
      v4u bb[5];
#pragma unroll
      for (int s = 0; s < 5; ++s) bb[s] = *(const LAS v4u*)(bp + s * 64);
      f4 a = mfma32(a4[0], bb[0], f4{0, 0, 0, 0}), a2 = mfma32(a4[1], bb[1], f4{0, 0, 0, 0});
      a = mfma32(a4[2], bb[2], a); a2 = mfma32(a4[3], bb[3], a2); a = mfma32(a4[4], bb[4], a);
      *(LAS f4*)(L + L_T4P + ((w * 4 + g) * 8 + (lane & 7)) * 16) = a + a2;
    }
    bar();
    if (tid < 128) {
      const int j = tid >> 4, c = 2 * (tid & 15), t = c >> 4, gg = (c & 15) >> 2, r = c & 3;
      const LAS float* z = (const LAS float*)(L + L_T4P + ((t * 4 + gg) * 8 + j) * 16) + r;
      const float z0 = (z[0] + z[256]) + (z[512] + z[768]), z1 = (z[1] + z[257]) + (z[513] + z[769]);
      *(LAS unsigned*)(L + L_H1 + j * 80 + c * 2) = pk2(silu(rbf(z0)), silu(rbf(z1)));
    }
    bar();
    STAMP(12);
    {
      const v4u bb = *(const LAS v4u*)(L + L_H1 + (lane & 7) * 80 + g * 16);
      f4 y[5];
#pragma unroll
      for (int tt = 0; tt < 5; ++tt) y[tt] = mfma32(a5[tt], bb, f4{0, 0, 0, 0});
#pragma unroll
      for (int tt = 0; tt < 5; ++tt) *(LAS v4u*)(L + L_RT + (lane & 7) * 2576 + (16 * (5 * w + tt) + 4 * g) * 4) = tg4(y[tt], t10);
    }
    STAMP3(12);
    bar();
    {
      v4u pv[3];
#pragma unroll
      for (int u = 0; u < 3; ++u) {
        const int pc = min(u * 512 + tid, 1279), j = pc / 160, c = pc - 160 * j;
        pv[u] = *(const LAS v4u*)(L + L_RT + j * 2576 + c * 16);
      }
      const unsigned o = WS_T5P + (unsigned)mm * 20480 + (unsigned)tid * 16;
      st_sc(pv[0], rsws, o); st_sc(pv[1], rsws, o + 8192);
      if (tid < 256) st_sc(pv[2], rsws, o + 16384);
    }
  }
  const unsigned tD = (unsigned)__builtin_amdgcn_s_memrealtime();
  STAMP(13);
  STAMP3(13);
  if (w < 5) {
    const bool act = lane < NMLP;
    const unsigned o = WS_T5P + (unsigned)(((act ? lane : 0) * 8 + b) * 640 + 20 * h + 4 * w) * 4;
    const LAS unsigned* vec = (const LAS unsigned*)(L + L_VEC);
    const unsigned g0 = vec[10 + 2 * w], g1 = vec[10 + 2 * w + 1];
    const f4 x1 = *(const LAS f4*)(L + L_X1O + w * 16);
    if (is_mlp) until(tD, GD1); else until(tB, GD2);
    if (w == 3) STAMP3(6);
    v4u v;
    for (unsigned spins = 0;; ++spins) {
      CFENCE();
      v = ld_sc(rsws, o);
      if (__all(tagged4(v, t10)) || spins > SPIN_G) { if (w == 3) { COUNT3(8, spins + 1); STAMP3(7); } break; }
      __builtin_amdgcn_s_sleep(TS);
    }
    f4 sm;
#pragma unroll
    for (int k = 0; k < 4; ++k) sm[k] = wave_sum(act ? untg(v[k]) : 0.f);
    if (lane == 0) {
      const float r0 = x1[0] + rbf(blo(g0) * rbf(sm[0])), r1 = x1[1] + rbf(bhi(g0) * rbf(sm[1]));
      const float r2 = x1[2] + rbf(blo(g1) * rbf(sm[2])), r3 = x1[3] + rbf(bhi(g1) * rbf(sm[3]));
      *(GAS v2u*)(p.out + b * DM + 20 * h + 4 * w) = v2u{pk2(r0, r1), pk2(r2, r3)};
    }
  }
  if (id == 0 && tid == 448) {
    const unsigned E = __builtin_amdgcn_raw_buffer_load_b32(rsws, WS_EPOCH, 0, 16);
    __builtin_amdgcn_raw_buffer_store_b32(E + 1u, rsws, WS_EPOCH, 0, 16);
  }
  STAMP(15);
}

}

extern "C" void kernel_launch(void* const* d_in, const int* in_sizes, int n_in, void* d_out, int out_size, void* d_ws,
                              size_t ws_size, hipStream_t stream) {
  Params p;
  p.x = (const u16*)d_in[0];
  p.kc = (const u16*)d_in[1];
  p.vc = (const u16*)d_in[2];
  p.wq = (const u16*)d_in[3];
  p.wo = (const u16*)d_in[4];
  p.w1 = (const u16*)d_in[6];
  p.w2 = (const u16*)d_in[7];
  p.c = (const u16*)d_in[8];
  p.wmod = (const u16*)d_in[9];
  p.out = (u16*)d_out;
  p.ws = (unsigned char*)d_ws;
  hipLaunchKernelGGL(fused_block, dim3(256), dim3(512), 0, stream, p);
}
```

```cpp
#include <hip/hip_runtime.h>
#include <stdint.h>

#define DIAG 0

namespace {

typedef unsigned short u16;
typedef unsigned v4u __attribute__((ext_vector_type(4)));
typedef unsigned v2u __attribute__((ext_vector_type(2)));
typedef float f4 __attribute__((ext_vector_type(4)));
typedef short s4 __attribute__((ext_vector_type(4)));
typedef short s8 __attribute__((ext_vector_type(8)));
typedef __bf16 bf2 __attribute__((ext_vector_type(2)));
typedef __bf16 bf8 __attribute__((ext_vector_type(8)));
typedef __amdgpu_buffer_rsrc_t rsrc_t;
#define LAS __attribute__((address_space(3)))
#define GAS __attribute__((address_space(1)))
#define DI __device__ __forceinline__

constexpr int NB = 8, DM = 640, NH = 32, DH = 128, SK = 4096, WIN = 1024, FF = 1728;
constexpr int NSW = 7;
constexpr int NL = 37;
#define TD_A 6
#define TD_B 8
#define TD_C 16
#define TD_D 8
#define TD_D2 4
#define TS 4
#define ABL_CTRL 0
#define ABL_SLOT 0
#define ABL_ROLE 0
#define ABL_SENT 0
#define LEAD 3
#define MP0 12
#define MPACE 11
#define DELTA 0
#define HEAVY 3
#define ABL_G1 0
#define ABL_G2 0
#define ABL_G3 0
#define ABL_TAIL 0
#define GB 45
#define GC 30
#define GD1 40
#define GD2 220
#define SLOT0 20
#define SLOTP 3
#define WOLATE 0
#define MLPLATE 2
#define HN 45
#define MPRE 0
#define INIT PACE_
#define CPACE 12
#define WARM 1
#define INDIC 1
#define ABL_WO 0
#define ABL_W12 0
#define D1 600
#define D2 100
#define PSLEEP 32
#define DPACE 15
#define PACE_ 3
constexpr int PACE = PACE_;
#define NPRE_ 16
constexpr int NPRE = NPRE_;

constexpr int NMLP = 54;
constexpr unsigned WS_EPOCH = 0;
constexpr unsigned WS_INIT = 4096;
constexpr unsigned WS_MODG = 8192;
constexpr unsigned WS_MODD = WS_MODG + NB * 1920 * 8;
constexpr unsigned WS_QP = WS_MODD + NB * 640 * 4;
constexpr unsigned WS_OX = WS_QP + NH * 8 * 8 * 128 * 4;
constexpr unsigned WS_T1P = WS_OX + NH * 8 * 256;
constexpr unsigned WS_X1B = WS_T1P + NB * NH * DM * 4;
constexpr unsigned WS_T5P = WS_X1B + NB * 320 * 4;
constexpr unsigned WS_END = WS_T5P + NMLP * NB * DM * 4;
constexpr unsigned WS_DIAG = 4u << 20;
constexpr unsigned SENT = 0xFFFFFFFFu;
static_assert(WS_END <= WS_DIAG, "workspace map");

constexpr unsigned L_RT = 0;
constexpr unsigned L_QT = 40960;
constexpr unsigned L_W2 = 61440;
constexpr unsigned L_T1 = 102400;
constexpr unsigned L_CS = 122880;
constexpr unsigned L_MISC = 143360;
constexpr unsigned L_FLAG = L_MISC;
constexpr unsigned L_Q = L_MISC + 64;
constexpr unsigned L_STAT = L_Q + 256;
constexpr unsigned L_VEC = L_STAT + 64;
constexpr unsigned L_X1O = L_VEC + 128;
constexpr unsigned L_SC = L_X1O + 128;
constexpr unsigned L_H2 = L_SC;
constexpr unsigned L_HS = L_SC + 8 * 1296;
constexpr unsigned L_MRG = L_HS + 8 * 176;
constexpr unsigned L_OS = L_MRG + 7 * 132 * 4;
constexpr unsigned L_T4P = L_MRG;
constexpr unsigned L_H1 = L_T4P + 4096;
constexpr unsigned L_END = 163840;
static_assert(L_OS + 8 * 272 <= L_END && L_H1 + 8 * 80 <= L_OS + 8 * 272, "LDS map");
static_assert((L_SC % 16) == 0 && (L_HS % 16) == 0 && (L_MRG % 16) == 0 && (L_OS % 16) == 0 && (L_H1 % 16) == 0, "LDS alignment");

constexpr unsigned SPIN_G = 1u << 16;
constexpr unsigned SPIN_L = 1u << 22;

struct Params {
  const u16 *x, *kc, *vc, *wq, *wo, *w1, *w2, *c, *wmod;
  u16* out;
  unsigned char* ws;
};

DI unsigned rfl(unsigned x) { return __builtin_amdgcn_readfirstlane(x); }
DI float asf(unsigned u) { return __uint_as_float(u); }
DI unsigned asu(float f) { return __float_as_uint(f); }
DI unsigned pk2(float lo, float hi) { bf2 v = {(__bf16)lo, (__bf16)hi}; return __builtin_bit_cast(unsigned, v); }
DI float rbf(float f) { return asf(pk2(f, 0.f) << 16); }
DI float blo(unsigned u) { return asf(u << 16); }
DI float bhi(unsigned u) { return asf(u & 0xffff0000u); }
DI float dot2(unsigned a, unsigned b, float c) {
  return __builtin_amdgcn_fdot2_f32_bf16(__builtin_bit_cast(bf2, a), __builtin_bit_cast(bf2, b), c, false);
}
template <int CTRL> DI float dpp(float x) {
  return __builtin_bit_cast(float, __builtin_amdgcn_mov_dpp(__builtin_bit_cast(int, x), CTRL, 0xf, 0xf, true));
}
DI float row16_sum(float x) { x += dpp<0x128>(x); x += dpp<0x124>(x); x += dpp<0x122>(x); x += dpp<0x121>(x); return x; }
DI float oct_sum(float x) { x += dpp<0xB1>(x); x += dpp<0x4E>(x); x += dpp<0x141>(x); return x; }
DI float x16_sum(float x) {
  auto s = __builtin_amdgcn_permlane16_swap(asu(x), asu(x), false, false);
  return asf(s[0]) + asf(s[1]);
}
DI float x32_sum(float x) {
  auto t = __builtin_amdgcn_permlane32_swap(asu(x), asu(x), false, false);
  return asf(t[0]) + asf(t[1]);
}
DI float xrow_sum(float x) { return x32_sum(x16_sum(x)); }
DI float xrow_max(float x) {
  auto s = __builtin_amdgcn_permlane16_swap(asu(x), asu(x), false, false);
  x = fmaxf(asf(s[0]), asf(s[1]));
  auto t = __builtin_amdgcn_permlane32_swap(asu(x), asu(x), false, false);
  return fmaxf(asf(t[0]), asf(t[1]));
}
DI float wave_sum(float x) { return xrow_sum(row16_sum(x)); }
DI f4 mfma32(v4u a, v4u b, f4 c) {
  return __builtin_amdgcn_mfma_f32_16x16x32_bf16(__builtin_bit_cast(bf8, a), __builtin_bit_cast(bf8, b), c, 0, 0, 0);
}
DI f4 mfma16(v2u a, v2u b, f4 c) {
  return __builtin_amdgcn_mfma_f32_16x16x16bf16_1k(__builtin_bit_cast(s4, a), __builtin_bit_cast(s4, b), c, 0, 0, 0);
}
DI v2u trread(const LAS unsigned char* p) {
  return __builtin_bit_cast(v2u, __builtin_amdgcn_ds_read_tr16_b64_v4i16((LAS s4*)p));
}
DI rsrc_t mkrs(const void* p, unsigned bytes) { return __builtin_amdgcn_make_buffer_rsrc((void*)p, 0, (int)bytes, 0x00020000); }
DI v4u ld_nt(rsrc_t r, unsigned vo, unsigned so) { return __builtin_amdgcn_raw_buffer_load_b128(r, vo, so, 2); }
#define KAUX 2
#define VAUX 2
DI v4u ld_k(rsrc_t r, unsigned vo, unsigned so) { return __builtin_amdgcn_raw_buffer_load_b128(r, vo, so, KAUX); }
DI v4u ld_v(rsrc_t r, unsigned vo, unsigned so) { return __builtin_amdgcn_raw_buffer_load_b128(r, vo, so, VAUX); }
DI v4u ld_sc(rsrc_t r, unsigned vo) { return __builtin_amdgcn_raw_buffer_load_b128(r, vo, 0, 16); }
DI v2u ld_sc2(rsrc_t r, unsigned vo) { return __builtin_amdgcn_raw_buffer_load_b64(r, vo, 0, 16); }
DI void st_sc(v4u v, rsrc_t r, unsigned vo) { __builtin_amdgcn_raw_buffer_store_b128(v, r, vo, 0, 16); }
DI void st_sc2(v2u v, rsrc_t r, unsigned vo) { __builtin_amdgcn_raw_buffer_store_b64(v, r, vo, 0, 16); }
DI unsigned canonu(unsigned u) { return u == 0xFFFFFFFFu ? 0x7FFF7FFFu : u; }
DI void dma16(rsrc_t r, unsigned lds, unsigned vo, unsigned so) {
  unsigned keep;
  asm volatile("s_nop 4\n\ts_mov_b32 %0, m0\n\ts_mov_b32 m0, %1\n\ts_nop 0\n\tbuffer_load_dwordx4 %2, %3, %4 offen nt lds\n\ts_mov_b32 m0, %0"
               : "=&s"(keep) : "s"(lds), "v"(vo), "s"(r), "s"(so) : "memory");
}
DI void dma16s(rsrc_t r, unsigned lds, unsigned vo, unsigned so) {
  unsigned keep;
  asm volatile("s_mov_b32 %0, m0\n\ts_mov_b32 m0, %1\n\ts_nop 0\n\tbuffer_load_dwordx4 %2, %3, %4 offen nt lds\n\ts_mov_b32 m0, %0"
               : "=&s"(keep) : "s"(lds), "v"(vo), "s"(r), "s"(so) : "memory");
}
template <int N> DI void vmwait() { asm volatile("s_waitcnt vmcnt(%0)" :: "n"(N) : "memory"); }
#define VMWAIT(n) vmwait<(n)>()
DI void bar() { asm volatile("s_waitcnt lgkmcnt(0)\n\ts_barrier" ::: "memory"); }
#define CFENCE() asm volatile("" ::: "memory")
#define SCHED() __builtin_amdgcn_sched_barrier(0)
DI unsigned canon(float f) { unsigned u = asu(f); return u == SENT ? 0x7FFFFFFFu : u; }
DI unsigned tg(float f, unsigned t10) { return (asu(f) & ~0x3FFu) | t10; }
DI v4u tg4(f4 v, unsigned t10) { return v4u{tg(v[0], t10), tg(v[1], t10), tg(v[2], t10), tg(v[3], t10)}; }
DI bool tagged4(v4u v, unsigned t10) { return (((v.x ^ t10) | (v.y ^ t10) | (v.z ^ t10) | (v.w ^ t10)) & 0x3FFu) == 0u; }
DI float untg(unsigned u) { return asf(u & ~0x3FFu); }
DI bool valid4(v4u v) { return v.x != SENT && v.y != SENT && v.z != SENT && v.w != SENT; }
DI float rcp(float x) { return __builtin_amdgcn_rcpf(x); }
DI float silu(float v) { return v * rcp(1.f + __builtin_amdgcn_exp2f(v * -1.4426950408889634f)); }

#define STAMP(k) do { } while (0)
#define STAMP2(k) do { } while (0)
#define STAMP3(k) do { } while (0)
#define COUNT3(k, v) do { } while (0)

__global__ void __launch_bounds__(512, 2) fused_block(Params p) {
  __shared__ __attribute__((aligned(16))) unsigned char smem[L_END];
  LAS unsigned char* L = (LAS unsigned char*)smem;
  const unsigned lds0 = (unsigned)(uintptr_t)L;
  const int tid = threadIdx.x, lane = tid & 63;
  const int w = (int)rfl(tid >> 6);
  const int id = blockIdx.x, xcd = id & 7, sl = id >> 3;
  const int h = xcd * 4 + (sl >> 3), b = sl & 7;
  const int g = lane >> 4, i16 = lane & 15;
  const int tq = i16 >> 2, tp = lane & 3;
  const rsrc_t rsws = mkrs(p.ws, WS_END);
  volatile LAS unsigned* flags = (volatile LAS unsigned*)(L + L_FLAG);
  const int mpr = (sl >> 1) * 8 + xcd;
  const bool has_mod = !ABL_ROLE && sl < 16 && mpr < 60;
  const bool crit = has_mod && mpr < 20;
  const int mcol0 = (2 * mpr + (sl & 1)) * 32;
  const int quad = ((sl - 16) >> 2) * 8 + xcd;
  const bool is_mlp = !ABL_ROLE && sl >= 16 && quad < 27;
  const int mm = 2 * quad + (sl & 1), hf = (sl >> 1) & 1;
  const bool dense = has_mod && mpr >= 30 && mpr < 50;

  const rsrc_t rswo = mkrs(p.wo + (size_t)h * DH * DM, DH * DM * 2);
  unsigned wov[5], w2v[5], w1v;
  static_assert(MPRE <= 40 && MPRE + MP0 <= 60 && MPRE + MP0 <= 63, "MLP pieces");
  STAMP(0);
  const unsigned tstart = (unsigned)__builtin_amdgcn_s_memrealtime();
  auto sched = [&](int i, bool light) {
    if (DELTA && light) { while ((unsigned)__builtin_amdgcn_s_memrealtime() - tstart < (unsigned)(i * DELTA / 10)) __builtin_amdgcn_s_sleep(1); }
  };

  unsigned t10 = 0;
  if (w < NSW) {
    const rsrc_t rsk = mkrs(p.kc + ((size_t)(b * NH + h) * SK + (SK - WIN)) * DH, WIN * DH * 2);
    const rsrc_t rsv = mkrs(p.vc + ((size_t)(b * NH + h) * SK + (SK - WIN)) * DH, WIN * DH * 2);
    const unsigned vo = (unsigned)(lane * 16 + w * 1024);
    v4u kr[NL], vr[NL], cr[2];
    if (has_mod && w < 5) {
      const GAS v4u* cp = (const GAS v4u*)p.c;
      cr[0] = cp[w * 128 + lane];
      cr[1] = cp[w * 128 + 64 + lane];
    }
    auto silu_c = [&]() {
      if (w < 5) {
#pragma unroll
        for (int u = 0; u < 2; ++u) {
          const int q = w * 128 + u * 64 + lane, j = q / 80, k8 = q - 80 * j;
          v4u o;
          o.x = pk2(silu(blo(cr[u].x)), silu(bhi(cr[u].x)));
          o.y = pk2(silu(blo(cr[u].y)), silu(bhi(cr[u].y)));
          o.z = pk2(silu(blo(cr[u].z)), silu(bhi(cr[u].z)));
          o.w = pk2(silu(blo(cr[u].w)), silu(bhi(cr[u].w)));
          *(LAS v4u*)(L + L_SC + j * 1296 + k8 * 16) = o;
        }
      }
    };
    const rsrc_t rsrt = mkrs(p.wmod, DM * 6 * DM * 2);
    const unsigned rt_vo = (unsigned)((lane >> 2) * 7680 + mcol0 * 2 + (lane & 3) * 16);
    auto dma_rt = [&](int m) { dma16(rsrt, lds0 + L_RT + m * 1024, rt_vo, (unsigned)m * 16u * 7680u); };
    if (!crit) {
#pragma unroll
      for (int i = 0; i < INIT; ++i) kr[i] = ld_k(rsk, vo, i * 7168);
    }
    if (has_mod) { for (int m = w; m < 40; m += NSW) dma_rt(m); }
    SCHED();
    bar();
    if (has_mod) {
      silu_c();
      VMWAIT(0);
      asm volatile("s_waitcnt lgkmcnt(0)" ::: "memory");
      if (lane == 0) __hip_atomic_fetch_add((LAS unsigned*)(L + L_FLAG), 1u, __ATOMIC_RELAXED, __HIP_MEMORY_SCOPE_WORKGROUP);
    }
    {
      const rsrc_t rsq = mkrs(p.wq, DM * NH * DH * 2);
      for (int n = w; n < 20; n += NSW) {
        const int rl = 4 * n + g;
        dma16(rsq, lds0 + L_QT + n * 1024, (unsigned)((80 * b + rl) * 8192 + h * 256 + ((i16 ^ (2 * (rl & 7))) * 16)), 0);
      }
    }
    if (crit) {
#pragma unroll
      for (int i = 0; i < INIT; ++i) kr[i] = ld_k(rsk, vo, i * 7168);
    }
    SCHED();
    const rsrc_t rsw1 = mkrs(p.w1, DM * FF * 2);
    const rsrc_t rsw2 = mkrs(p.w2 + (size_t)(is_mlp ? mm : 0) * 32 * DM, 32 * DM * 2);
    auto slot = [&](int s) {
      const int n = 7 * s + w;
      if (n < 40) {
        dma16(rsw1, lds0 + L_RT + n * 1024, (unsigned)((lane >> 2) * (FF * 2) + mm * 64 + (lane & 3) * 16), (unsigned)n * 16u * (FF * 2));
      } else if (n < 60) {
        const int ci = (n - 40) * 64 + lane, row = ci / 40, pos = ci - 40 * row;
        dma16(rsw2, lds0 + L_W2 + (n - 40) * 1024, (unsigned)(row * 1280 + hf * 640 + (pos ^ (2 * (row & 3))) * 16), 0);
      }
    };
    static_assert(SLOT0 + 8 * SLOTP < NL + NPRE - 1, "all slots inside phase A");
    const bool heavy = HEAVY == 1 ? (is_mlp || has_mod) : HEAVY == 2 ? is_mlp : HEAVY == 3 ? has_mod : false;
#pragma unroll
    for (int i = INIT; i < NL + NPRE; ++i) {
      sched(i, !is_mlp);
      if (heavy && i < HN) VMWAIT(PACE); else VMWAIT(PACE - 1);
      if (i < NL) kr[i] = ld_k(rsk, vo, i * 7168); else vr[i - NL] = ld_v(rsv, vo, (i - NL) * 7168);
      SCHED();
      if (i == INIT + 4) {
        asm volatile("s_waitcnt lgkmcnt(0)" ::: "memory");
        if (lane == 0) __hip_atomic_fetch_add((LAS unsigned*)(L + L_FLAG) + 2, 1u, __ATOMIC_RELAXED, __HIP_MEMORY_SCOPE_WORKGROUP);
        STAMP(1);
      }
      if (i >= SLOT0 && ((i - SLOT0) % SLOTP) == 0 && (i - SLOT0) / SLOTP < 9) { if (is_mlp && !MLPLATE) slot((i - SLOT0) / SLOTP); SCHED(); }
    }
    STAMP(2);
    {
      unsigned spins = 0;
      while (flags[1] == 0u) { __builtin_amdgcn_s_sleep(1); if (++spins > SPIN_L) break; }
    }
    CFENCE();
    STAMP(3);
    const v4u q4 = *(const LAS v4u*)(L + L_Q + i16 * 16);
    t10 = flags[4] & 0x3FFu;
    float s[NL];
#pragma unroll
    for (int i = 0; i < NL; ++i) {
      float d = dot2(kr[i].x, q4.x, 0.f);
      d = dot2(kr[i].y, q4.y, d); d = dot2(kr[i].z, q4.z, d); d = dot2(kr[i].w, q4.w, d);
      s[i] = row16_sum(d);
      if ((i & 3) == 3 && NPRE + (i >> 2) < NL) { SCHED(); sched(NL + NPRE + (i >> 2), !is_mlp); if (heavy && NL + NPRE + (i >> 2) < HN) VMWAIT(PACE); else VMWAIT(PACE - 1); vr[NPRE + (i >> 2)] = ld_v(rsv, vo, (NPRE + (i >> 2)) * 7168); SCHED(); }
    }
    constexpr int NV1 = NPRE + (NL >> 2);
    if (w >= 4) s[NL - 1] = -INFINITY;
    float m = s[0];
#pragma unroll
    for (int i = 1; i < NL; ++i) m = fmaxf(m, s[i]);
    m = xrow_max(m);
    constexpr float C2 = 0.08838834764831845f * 1.4426950408889634f;
    const float mc = m * C2;
    unsigned pp[NL];
    float l = 0.f;
#pragma unroll
    for (int i = 0; i < NL; ++i) {
      pp[i] = pk2(0.f, __builtin_amdgcn_exp2f(fmaf(s[i], C2, -mc)));
      l += asf(pp[i]);
    }
    l = xrow_sum(l);
    STAMP(4);
    float acc[8];
#pragma unroll
    for (int k = 0; k < 8; ++k) acc[k] = 0.f;
#pragma unroll
    for (int i = 0; i < NL; ++i) {
      const unsigned p1 = pp[i], p0 = p1 >> 16;
      acc[0] = dot2(vr[i].x, p0, acc[0]); acc[1] = dot2(vr[i].x, p1, acc[1]);
      acc[2] = dot2(vr[i].y, p0, acc[2]); acc[3] = dot2(vr[i].y, p1, acc[3]);
      acc[4] = dot2(vr[i].z, p0, acc[4]); acc[5] = dot2(vr[i].z, p1, acc[5]);
      acc[6] = dot2(vr[i].w, p0, acc[6]); acc[7] = dot2(vr[i].w, p1, acc[7]);
      SCHED();
      if (NV1 + i < NL) { sched(NL + NV1 + i, !is_mlp); if (heavy && NL + NV1 + i < HN) VMWAIT(PACE); else VMWAIT(PACE - 1); vr[NV1 + i] = ld_v(rsv, vo, (NV1 + i) * 7168); SCHED(); }
      if (NV1 + i == NL - 1 - LEAD) {
        if (lane == 0) __hip_atomic_fetch_add((LAS unsigned*)(L + L_FLAG) + 3, 1u, __ATOMIC_RELAXED, __HIP_MEMORY_SCOPE_WORKGROUP);
      }
    }
    STAMP(5);
#pragma unroll
    for (int k = 0; k < 8; ++k) acc[k] = xrow_sum(acc[k]);
    if (g == 0) {
      LAS float* mr = (LAS float*)(L + L_MRG) + w * 132;
      *(LAS f4*)(mr + 4 + 8 * i16) = f4{acc[0], acc[1], acc[2], acc[3]};
      *(LAS f4*)(mr + 8 + 8 * i16) = f4{acc[4], acc[5], acc[6], acc[7]};
      if (i16 == 0) { mr[0] = m; mr[1] = l; }
    }
    asm volatile("s_waitcnt lgkmcnt(0)" ::: "memory");
    unsigned arrived = 0;
    if (lane == 0) arrived = __hip_atomic_fetch_add((LAS unsigned*)(L + L_FLAG) + 8, 1u, __ATOMIC_RELAXED, __HIP_MEMORY_SCOPE_WORKGROUP);
    if (rfl(arrived) == (unsigned)(NSW - 1)) {
      CFENCE();
      if (lane < 32) {
        const LAS float* mr = (const LAS float*)(L + L_MRG);
        float mw[NSW], lw[NSW];
        f4 aw[NSW];
#pragma unroll
        for (int u = 0; u < NSW; ++u) { mw[u] = mr[u * 132]; lw[u] = mr[u * 132 + 1]; aw[u] = *(const LAS f4*)(mr + u * 132 + 4 + 4 * lane); }
        const float M = fmaxf(fmaxf(fmaxf(mw[0], mw[1]), fmaxf(mw[2], mw[3])), fmaxf(fmaxf(mw[4], mw[5]), mw[6]));
        float Lsum = 0.f;
        f4 o = {0, 0, 0, 0};
#pragma unroll
        for (int u = 0; u < NSW; ++u) {
          const float f = __builtin_amdgcn_exp2f((mw[u] - M) * C2);
          Lsum = fmaf(f, lw[u], Lsum);
          o += f * aw[u];
        }
        const float inv = rcp(Lsum);
        st_sc2(v2u{canonu(pk2(o[0] * inv, o[1] * inv)), canonu(pk2(o[2] * inv, o[3] * inv))}, rsws, WS_OX + (unsigned)(h * 8 + b) * 256 + lane * 8);
      }
    }
  } else {
    __builtin_amdgcn_s_setprio(3);
    if (lane == 0) { flags[0] = 0u; flags[1] = 0u; flags[2] = 0u; flags[3] = 0u; flags[5] = 0u; flags[6] = 0u; flags[7] = 0u; flags[8] = 0u; }
    if (ABL_CTRL) {
      bar();
      while ((unsigned)__builtin_amdgcn_s_memrealtime() - tstart < 800) __builtin_amdgcn_s_sleep(8);
      if (lane == 0) flags[1] = 1u;
    } else {
    const unsigned E = __builtin_amdgcn_raw_buffer_load_b32(rsws, WS_EPOCH, 0, 16);
    unsigned warm = 0;
    {
      const int n = sl * 64 + lane, pi = n / 640, row = n - 640 * pi, pr = xcd + 8 * pi;
      if (WARM && pr < 20) warm = __builtin_amdgcn_raw_buffer_load_b32(mkrs(p.wmod, DM * 6 * DM * 2), (unsigned)(row * 7680 + pr * 128), 0, 2);
    }
    const GAS v4u* xp = (const GAS v4u*)p.x;
    v4u xr[10];
    const int sj = lane >> 3, sub = lane & 7;
#pragma unroll
    for (int u = 0; u < 10; ++u) xr[u] = xp[sj * 80 + sub + 8 * u];
    SCHED();
    bar();
    unsigned xh[5];
#pragma unroll
    for (int u = 0; u < 5; ++u) { const int idx = u * 64 + lane, j = idx / 40, m = idx - 40 * j; xh[u] = ((const GAS unsigned*)p.x)[j * 320 + 40 * b + m]; }
    if (!ABL_SENT) {
      const v4u sv = {SENT, SENT, SENT, SENT};
      if (lane < 16) st_sc(sv, rsws, WS_OX + (unsigned)(h * 8 + b) * 256 + lane * 16);
      if (lane < 5) st_sc2(v2u{SENT, SENT}, rsws, WS_X1B + (unsigned)(b * 320 + 10 * h) * 4 + lane * 8);
      if (dense && lane < 32) st_sc(sv, rsws, WS_MODD + (unsigned)((lane >> 2) * 640 + (mcol0 - 1920) / 2 + (lane & 3) * 4) * 4);
    }
    STAMP(1);
    {
      float sm = 0.f;
#pragma unroll
      for (int u = 0; u < 10; ++u)
        sm += ((blo(xr[u].x) + bhi(xr[u].x)) + (blo(xr[u].y) + bhi(xr[u].y))) + ((blo(xr[u].z) + bhi(xr[u].z)) + (blo(xr[u].w) + bhi(xr[u].w)));
      const float mean = oct_sum(sm) * (1.f / DM);
      float sq = 0.f;
#pragma unroll
      for (int u = 0; u < 10; ++u) {
#pragma unroll
        for (int k = 0; k < 4; ++k) { const float a = blo(xr[u][k]) - mean, c2 = bhi(xr[u][k]) - mean; sq = fmaf(a, a, sq); sq = fmaf(c2, c2, sq); }
      }
      const float var = oct_sum(sq) * (1.f / DM);
      if (sub == 0) {
        LAS float* st = (LAS float*)(L + L_STAT) + sj * 2;
        st[0] = rbf(mean);
        st[1] = rcp(rbf(__builtin_amdgcn_sqrtf(rbf(rbf(var) + 1e-5f))));
      }
    }
    VMWAIT(0);
    asm volatile("" :: "v"(warm));
    const unsigned T = rfl(E) + 1u;
    t10 = T & 0x3FFu;
    if (lane == 0) flags[4] = T;
    if (lane == 0) st_sc2(v2u{T, ~T}, rsws, WS_INIT + (unsigned)(h * 8 + b) * 8);
    STAMP2(9);
    if (has_mod) {
      unsigned spins = 0;
      while (flags[0] < (unsigned)NSW) { __builtin_amdgcn_s_sleep(1); if (++spins > SPIN_L) break; }
      CFENCE();
      STAMP(2);
      f4 a0 = {0, 0, 0, 0}, a1 = {0, 0, 0, 0};
      const LAS unsigned char* ap = L + L_RT + (8 * g + tq) * 64 + tp * 8;
      const LAS unsigned char* bp = L + L_SC + (lane & 7) * 1296 + g * 16;
#pragma unroll 2
      for (int s = 0; s < 20; ++s) {
        const v4u bb = *(const LAS v4u*)(bp + s * 64);
        const v2u x0 = trread(ap + s * 2048), x1 = trread(ap + s * 2048 + 256);
        const v2u y0 = trread(ap + s * 2048 + 32), y1 = trread(ap + s * 2048 + 256 + 32);
        a0 = mfma32(v4u{x0.x, x0.y, x1.x, x1.y}, bb, a0);
        a1 = mfma32(v4u{y0.x, y0.y, y1.x, y1.y}, bb, a1);
      }
      if (i16 < 8) {
        if (dense) {
          const unsigned o = WS_MODD + (unsigned)(i16 * 640 + (mcol0 - 1920 + 4 * g) / 2) * 4;
          st_sc2(v2u{canonu(pk2(a0[0], a0[1])), canonu(pk2(a0[2], a0[3]))}, rsws, o);
          st_sc2(v2u{canonu(pk2(a1[0], a1[1])), canonu(pk2(a1[2], a1[3]))}, rsws, o + 32);
        } else {
          const unsigned o = WS_MODG + (unsigned)(i16 * 1920 + (mcol0 + 4 * g) / 2) * 8;
          st_sc(v4u{pk2(a0[0], a0[1]), T, pk2(a0[2], a0[3]), T}, rsws, o);
          st_sc(v4u{pk2(a1[0], a1[1]), T, pk2(a1[2], a1[3]), T}, rsws, o + 64);
        }
      }
    }
    STAMP(3);
    {
      v2u sa[5], ca[5], fl;
      if (D1) { while ((unsigned)__builtin_amdgcn_s_memrealtime() - tstart < D1) __builtin_amdgcn_s_sleep(2); }
      STAMP2(10);
      if (ABL_G1) { for (int u = 0; u < 5; ++u) { sa[u] = v2u{0u, T}; ca[u] = v2u{0u, T}; } }
      for (unsigned spins = 0; !ABL_G1; ++spins) {
        CFENCE();
        bool ok = true;
#pragma unroll
        for (int u = 0; u < 5; ++u) {
          const int idx = u * 64 + lane, j = idx / 40, m = idx - 40 * j;
          const unsigned o = WS_MODG + (unsigned)(j * 1920 + 40 * b + m) * 8;
          sa[u] = ld_sc2(rsws, o);
          ca[u] = ld_sc2(rsws, o + 320 * 8);
        }
        fl = ld_sc2(rsws, WS_INIT + (unsigned)(h * 8 + (lane & 7)) * 8);
#pragma unroll
        for (int u = 0; u < 5; ++u) ok = ok && sa[u].y == T && ca[u].y == T;
        ok = ok && fl.x == T && fl.y == ~T;
        if (__all(ok) || spins > SPIN_G) break;
        __builtin_amdgcn_s_sleep(PSLEEP);
      }
      STAMP2(11);
      asm volatile("s_waitcnt lgkmcnt(0)" ::: "memory");
#pragma unroll
      for (int u = 0; u < 5; ++u) {
        const int idx = u * 64 + lane, j = idx / 40, m = idx - 40 * j;
        const float mean = ((LAS float*)(L + L_STAT))[2 * j], den = ((LAS float*)(L + L_STAT))[2 * j + 1];
        const float n0 = rbf(rbf(blo(xh[u]) - mean) * den), n1 = rbf(rbf(bhi(xh[u]) - mean) * den);
        const float h0 = rbf(n0 * rbf(1.f + blo(ca[u].x))) + blo(sa[u].x), h1 = rbf(n1 * rbf(1.f + bhi(ca[u].x))) + bhi(sa[u].x);
        *(LAS unsigned*)(L + L_HS + j * 176 + m * 4) = pk2(h0, h1);
      }
    }
    STAMP(4);
    {
      unsigned spins = 0;
      while (flags[2] < (unsigned)NSW) { __builtin_amdgcn_s_sleep(1); if (++spins > SPIN_L) break; }
      CFENCE();
    }
    STAMP2(12);
    {
      f4 qa[8];
#pragma unroll
      for (int t = 0; t < 8; ++t) qa[t] = f4{0, 0, 0, 0};
#pragma unroll
      for (int s = 0; s < 5; ++s) {
        const v2u bb = *(const LAS v2u*)(L + L_HS + (lane & 7) * 176 + s * 32 + g * 8);
        const int row = 16 * s + 4 * g + tq;
        const LAS unsigned char* rp = L + L_QT + row * 256 + (tp & 1) * 8;
        const int sw = 2 * (row & 7);
#pragma unroll
        for (int t = 0; t < 8; ++t) qa[t] = mfma16(trread(rp + (((2 * t + (tp >> 1)) ^ sw) * 16)), bb, qa[t]);
      }
      if (i16 < 8 && !ABL_G2) {
        const unsigned o = WS_QP + (unsigned)(((h * 8 + b) * 8 + i16) * 128 + 4 * g) * 4;
#pragma unroll
        for (int t = 0; t < 8; ++t) st_sc(tg4(qa[t], t10), rsws, o + t * 64);
      }
    }
    STAMP(5);
    const unsigned tpub = (unsigned)__builtin_amdgcn_s_memrealtime();
    {
      v4u qv[4];
      if (D2) { while ((unsigned)__builtin_amdgcn_s_memrealtime() - tpub < D2) __builtin_amdgcn_s_sleep(2); }
      STAMP2(13);
      if (ABL_G2) { for (int u = 0; u < 4; ++u) qv[u] = v4u{t10, t10, t10, t10}; }
      for (unsigned spins = 0; !ABL_G2; ++spins) {
        CFENCE();
        bool ok = true;
#pragma unroll
        for (int u = 0; u < 4; ++u) qv[u] = ld_sc(rsws, WS_QP + (unsigned)(((h * 8 + 2 * u + (lane >> 5)) * 8 + b) * 128 + 4 * (lane & 31)) * 4);
#pragma unroll
        for (int u = 0; u < 4; ++u) ok = ok && tagged4(qv[u], t10);
        if (__all(ok) || spins > SPIN_G) break;
        __builtin_amdgcn_s_sleep(PSLEEP);
      }
      f4 qs;
#pragma unroll
      for (int k = 0; k < 4; ++k) qs[k] = x32_sum((untg(qv[0][k]) + untg(qv[1][k])) + (untg(qv[2][k]) + untg(qv[3][k])));
      if (lane < 32) *(LAS v2u*)(L + L_Q + lane * 8) = v2u{pk2(qs[0], qs[1]), pk2(qs[2], qs[3])};
      asm volatile("s_waitcnt lgkmcnt(0)" ::: "memory");
      if (lane == 0) flags[1] = 1u;
    }
    STAMP(6);
    if (!ABL_G3) {
      const unsigned go = WS_MODG + (unsigned)(b * 1920 + (lane < 10 ? 640 + 10 * h + lane : 1600 + 10 * h + (lane < 20 ? lane - 10 : 0))) * 8;
      const unsigned xv = ((const GAS unsigned*)p.x)[b * 320 + 10 * h + (lane < 10 ? lane : 0)];
      v2u gv;
      for (unsigned spins = 0;; ++spins) {
        CFENCE();
        gv = ld_sc2(rsws, go);
        if (__all(gv.y == T) || spins > SPIN_G) break;
        __builtin_amdgcn_s_sleep(32);
      }
      if (lane < 20) *(LAS unsigned*)(L + L_VEC + lane * 4) = gv.x;
      if (lane < 10) *(LAS unsigned*)(L + L_VEC + 80 + lane * 4) = xv;
      if (is_mlp) {
        for (unsigned spins = 0;; ++spins) {
          CFENCE();
          bool ok = true;
#pragma unroll
          for (int u = 0; u < 4; ++u) { const v2u f = ld_sc2(rsws, WS_INIT + (unsigned)(u * 64 + lane) * 8); ok = ok && f.x == T && f.y == ~T; }
          if (__all(ok) || spins > SPIN_G) break;
          __builtin_amdgcn_s_sleep(32);
        }
        for (int jj = 0; jj < 4; ++jj) {
          v4u cv[5];
          for (unsigned spins = 0;; ++spins) {
            CFENCE();
            bool ok = true;
#pragma unroll
            for (int u = 0; u < 5; ++u) cv[u] = ld_sc(rsws, WS_MODD + (unsigned)jj * 5120 + (unsigned)(u * 64 + lane) * 16);
#pragma unroll
            for (int u = 0; u < 5; ++u) ok = ok && valid4(cv[u]);
            if (__all(ok) || spins > SPIN_G) break;
            __builtin_amdgcn_s_sleep(32);
          }
#pragma unroll
          for (int u = 0; u < 5; ++u) {
            const int pc = u * 64 + lane, j = 2 * jj + pc / 160, r = pc % 160;
            v4u o = cv[u];
            if (r >= 80) {
#pragma unroll
              for (int k = 0; k < 4; ++k) o[k] = pk2(1.f + blo(o[k]), 1.f + bhi(o[k]));
            }
            *(LAS v4u*)(L + L_CS + (r < 80 ? 10240 + (j * 320 + 4 * r) * 4 : (j * 320 + 4 * (r - 80)) * 4)) = o;
          }
        }
      }
    }
    STAMP(7);
#pragma unroll
    for (int c = 0; c < 5; ++c) { const int ci = c * 64 + lane, row = ci / 10, ch = ci - 10 * row; wov[c] = (unsigned)(row * 1280 + b * 160 + ch * 16); asm volatile("" : "+v"(wov[c])); }
#pragma unroll
    for (int c = 0; c < 5; ++c) {
      const int ci = c * 64 + lane, row = ci / 40, pos = ci - 40 * row;
      w2v[c] = (unsigned)(row * 1280 + hf * 640 + (pos ^ (2 * (row & 3))) * 16); asm volatile("" : "+v"(w2v[c]));
    }
    w1v = (unsigned)((lane >> 2) * (FF * 2) + mm * 64 + (lane & 3) * 16); asm volatile("" : "+v"(w1v));
    if (MLPLATE == 2 && is_mlp) {
      const rsrc_t rsw1t = mkrs(p.w1, DM * FF * 2);
      const rsrc_t rsw2t = mkrs(p.w2 + (size_t)mm * 32 * DM, 32 * DM * 2);
#pragma unroll
      for (int n = 0; n < 60; ++n) {
        if (n >= CPACE) VMWAIT(CPACE - 1);
        if (n < 40) dma16s(rsw1t, lds0 + L_RT + n * 1024, w1v, (unsigned)n * 16u * (FF * 2));
        else dma16s(rsw2t, lds0 + L_W2 + (n - 40) * 1024, w2v[(n - 40) % 5], (unsigned)((n - 40) / 5) * 8u * 1280u);
      }
    }
    if (!WOLATE || (MLPLATE == 1 && MPRE > 0)) {
      unsigned spins = 0;
      while (flags[3] < (unsigned)NSW) { __builtin_amdgcn_s_sleep(1); if (++spins > SPIN_L) break; }
      CFENCE();
      if (!WOLATE) {
#pragma unroll
        for (int n = 0; n < 20; ++n) dma16s(rswo, lds0 + L_T1 + n * 1024, wov[n % 5], (unsigned)(n / 5) * 32u * 1280u);
      }
      if (MLPLATE == 1 && is_mlp) {
        const rsrc_t rsw1t = mkrs(p.w1, DM * FF * 2);
#pragma unroll
        for (int n = 0; n < MPRE; ++n) dma16s(rsw1t, lds0 + L_RT + n * 1024, w1v, (unsigned)n * 16u * (FF * 2));
      }
    }
    }
  }

  bar();
  STAMP(8);
  STAMP3(1);
  if (ABL_CTRL || ABL_TAIL) {
    STAMP(15);
    return;
  }
  auto until = [&](unsigned t0, unsigned ticks) { while ((unsigned)__builtin_amdgcn_s_memrealtime() - t0 < ticks) __builtin_amdgcn_s_sleep(1); };
  auto lds_wait = [&](int k, unsigned v) { unsigned spins = 0; while (flags[k] < v) { if (++spins > SPIN_L) break; } CFENCE(); };
  const rsrc_t rsw1t = mkrs(p.w1, DM * FF * 2);
  const rsrc_t rsw2t = mkrs(p.w2 + (size_t)(is_mlp ? mm : 0) * 32 * DM, 32 * DM * 2);
  auto mlp_piece = [&](int n) {
    if (n < 40) dma16s(rsw1t, lds0 + L_RT + n * 1024, w1v, (unsigned)n * 16u * (FF * 2));
    else dma16s(rsw2t, lds0 + L_W2 + (n - 40) * 1024, w2v[(n - 40) % 5], (unsigned)((n - 40) / 5) * 8u * 1280u);
  };
  const int t1t = w == 0 ? 0 : w - 2;
  v4u af[4];
  if (w == 1 || w == 2) {
    const int t = tid - 64;
    __builtin_amdgcn_s_sleep(TD_A);
    STAMP3(2);
    v4u v;
    for (unsigned spins = 0;; ++spins) {
      CFENCE();
      v = ld_sc(rsws, WS_OX + (unsigned)h * 2048 + t * 16);
      if (__all(valid4(v)) || spins > SPIN_G) { COUNT3(4, spins + 1); break; }
      __builtin_amdgcn_s_sleep(TS);
    }
    STAMP3(3);
    *(LAS v4u*)(L + L_OS + (t >> 4) * 272 + (t & 15) * 16) = v;
  } else if (w == 7) {
    if (WOLATE) {
#pragma unroll
      for (int n = 0; n < 20; ++n) dma16s(rswo, lds0 + L_T1 + n * 1024, wov[n % 5], (unsigned)(n / 5) * 32u * 1280u);
    }
    if (MLPLATE == 1 && is_mlp) {
#pragma unroll
      for (int n = MPRE; n < MPRE + MP0; ++n) mlp_piece(n);
      if (WOLATE) VMWAIT(MP0); else VMWAIT(MPRE + MP0);
    } else {
      VMWAIT(0);
    }
    if (lane == 0) flags[5] = 1u;
  } else {
    lds_wait(5, 1u);
    const LAS unsigned char* ap = L + L_T1 + (8 * g + tq) * 160 + (16 * t1t + 4 * tp) * 2;
#pragma unroll
    for (int s = 0; s < 4; ++s) {
      const v2u x0 = trread(ap + s * 32 * 160), x1 = trread(ap + s * 32 * 160 + 4 * 160);
      af[s] = v4u{x0.x, x0.y, x1.x, x1.y};
    }
  }
  bar();
  const unsigned tB2 = (unsigned)__builtin_amdgcn_s_memrealtime();
  STAMP(9);
  STAMP3(5);
  if (w == 0 || (w >= 3 && w < 7)) {
    const LAS unsigned char* bp = L + L_OS + (lane & 7) * 272 + g * 16;
    v4u bb[4];
#pragma unroll
    for (int s = 0; s < 4; ++s) bb[s] = *(const LAS v4u*)(bp + s * 64);
    f4 y0 = mfma32(af[0], bb[0], f4{0, 0, 0, 0}), y1 = mfma32(af[1], bb[1], f4{0, 0, 0, 0});
    y0 = mfma32(af[2], bb[2], y0); y1 = mfma32(af[3], bb[3], y1);
    const int cc = 4 * t1t + g;
    if (i16 < 8) st_sc(tg4(y0 + y1, t10), rsws, WS_T1P + (unsigned)((((i16 * 32 + 4 * b + cc / 5) * 32 + h) * 5 + cc % 5) * 16));
    if (w == 0 || w == 3) STAMP3(6);
  }
  v4u a4[5], a5[4];
  unsigned o5 = WS_T5P + (unsigned)((mm * 8 + (lane & 7)) * 640 + 320 * hf + 64 * w + 16 * (i16 >> 3) + 4 * g) * 4;
  asm volatile("" : "+v"(o5));
  if (MLPLATE == 1 && is_mlp && w == 7) {
#pragma unroll
    for (int n = MPRE + MP0; n < 60; ++n) { VMWAIT(MPACE); mlp_piece(n); }
    VMWAIT(0);
    if (lane == 0) flags[7] = 1u;
  }
  auto preload_mlp = [&]() {
    if (MLPLATE == 1) lds_wait(7, 1u);
    const LAS unsigned char* ap = L + L_RT + (160 * (w >> 1) + 8 * g + tq) * 64 + (w & 1) * 32 + tp * 8;
#pragma unroll
    for (int s = 0; s < 5; ++s) {
      const v2u x0 = trread(ap + s * 2048), x1 = trread(ap + s * 2048 + 256);
      a4[s] = v4u{x0.x, x0.y, x1.x, x1.y};
    }
    const int r0 = 8 * g + tq;
    const LAS unsigned char* wp = L + L_W2 + r0 * 640 + (tp & 1) * 8;
    const int f = 2 * (r0 & 3);
#pragma unroll
    for (int tt = 0; tt < 4; ++tt) {
      const int ch = (2 * (4 * (w < 5 ? w : 0) + tt) + (tp >> 1)) ^ f;
      const v2u x0 = trread(wp + ch * 16), x1 = trread(wp + 4 * 640 + ch * 16);
      a5[tt] = v4u{x0.x, x0.y, x1.x, x1.y};
    }
  };
  if (is_mlp && !(w >= 4 && w < 7 && MLPLATE == 1)) preload_mlp();
  if (w >= 4 && w < 7) {
    const int c = 2 * (w - 4) + (lane >> 5), pr = lane & 31;
    const bool act = c < 5;
    const unsigned o = WS_T1P + (unsigned)(((b * 32 + h) * 32 + pr) * 5 + (act ? c : 0)) * 16;
    const LAS unsigned* vec = (const LAS unsigned*)(L + L_VEC);
    const int cv = act ? c : 0;
    const unsigned g0 = vec[2 * cv], g1 = vec[2 * cv + 1], x0 = vec[20 + 2 * cv], x1 = vec[20 + 2 * cv + 1];
    until(tB2, GB);
    STAMP3(6);
    v4u v;
    for (unsigned spins = 0;; ++spins) {
      CFENCE();
      v = ld_sc(rsws, o);
      if (__all(tagged4(v, t10)) || spins > SPIN_G) { COUNT3(8, spins + 1); STAMP3(7); break; }
      __builtin_amdgcn_s_sleep(TS);
    }
    f4 sm;
#pragma unroll
    for (int k = 0; k < 4; ++k) sm[k] = x16_sum(row16_sum(untg(v[k])));
    const float r0 = rbf(blo(x0) + rbf(blo(g0) * rbf(sm[0]))), r1 = rbf(bhi(x0) + rbf(bhi(g0) * rbf(sm[1])));
    const float r2 = rbf(blo(x1) + rbf(blo(g1) * rbf(sm[2]))), r3 = rbf(bhi(x1) + rbf(bhi(g1) * rbf(sm[3])));
    if (pr == 0 && act) {
      st_sc2(v2u{canonu(pk2(r0, r1)), canonu(pk2(r2, r3))}, rsws, WS_X1B + (unsigned)(b * 320 + 10 * h + 2 * c) * 4);
      *(LAS f4*)(L + L_X1O + c * 16) = f4{r0, r1, r2, r3};
    }
    asm volatile("s_waitcnt lgkmcnt(0)" ::: "memory");
    if (lane == 0) __hip_atomic_fetch_add((LAS unsigned*)(L + L_FLAG) + 6, 1u, __ATOMIC_RELAXED, __HIP_MEMORY_SCOPE_WORKGROUP);
    if (is_mlp && MLPLATE == 1) preload_mlp();
  }
  lds_wait(6, 3u);
  const unsigned tB = (unsigned)__builtin_amdgcn_s_memrealtime();
  STAMP(10);
  STAMP3(10);
  if (is_mlp) {
    {
      const unsigned o = WS_X1B + (unsigned)w * 1280 + lane * 4;
      unsigned cs[5], sh[5];
#pragma unroll
      for (int u = 0; u < 5; ++u) {
        cs[u] = *(const LAS unsigned*)(L + L_CS + (w * 320 + u * 64 + lane) * 4);
        sh[u] = *(const LAS unsigned*)(L + L_CS + 10240 + (w * 320 + u * 64 + lane) * 4);
      }
      until(tB, GC);
      STAMP3(2);
      unsigned xv[5];
      for (unsigned spins = 0;; ++spins) {
        CFENCE();
        bool ok = true;
#pragma unroll
        for (int u = 0; u < 5; ++u) xv[u] = __builtin_amdgcn_raw_buffer_load_b32(rsws, o + u * 256, 0, 16);
#pragma unroll
        for (int u = 0; u < 5; ++u) ok = ok && xv[u] != SENT;
        if (__all(ok) || spins > SPIN_G) { COUNT3(4, spins + 1); STAMP3(3); break; }
        __builtin_amdgcn_s_sleep(TS);
      }
      const float x00 = blo(rfl(xv[0]));
      float s1 = 0.f, s2 = 0.f;
#pragma unroll
      for (int u = 0; u < 5; ++u) {
        const float d0 = blo(xv[u]) - x00, d1 = bhi(xv[u]) - x00;
        s1 += d0 + d1; s2 = fmaf(d0, d0, fmaf(d1, d1, s2));
      }
      s1 = row16_sum(s1); s2 = row16_sum(s2);
      s1 = xrow_sum(s1) * (1.f / DM); s2 = xrow_sum(s2) * (1.f / DM);
      const float rs = __builtin_amdgcn_rsqf(fmaxf(s2 - s1 * s1, 0.f) + 1e-5f), mr = -(x00 + s1) * rs;
#pragma unroll
      for (int u = 0; u < 5; ++u) {
        const float n0 = fmaf(blo(xv[u]), rs, mr), n1 = fmaf(bhi(xv[u]), rs, mr);
        *(LAS unsigned*)(L + L_H2 + w * 1296 + (u * 64 + lane) * 4) = pk2(fmaf(n0, blo(cs[u]), blo(sh[u])), fmaf(n1, bhi(cs[u]), bhi(sh[u])));
      }
    }
    STAMP3(11);
    bar();
    STAMP(11);
    {
      const LAS unsigned char* bp = L + L_H2 + (lane & 7) * 1296 + (160 * (w >> 1) + 8 * g) * 2;
      v4u bb[5];
#pragma unroll
      for (int s = 0; s < 5; ++s) bb[s] = *(const LAS v4u*)(bp + s * 64);
      f4 a = mfma32(a4[0], bb[0], f4{0, 0, 0, 0}), a2 = mfma32(a4[1], bb[1], f4{0, 0, 0, 0});
      a = mfma32(a4[2], bb[2], a); a2 = mfma32(a4[3], bb[3], a2); a = mfma32(a4[4], bb[4], a);
      *(LAS f4*)(L + L_T4P + ((w * 4 + g) * 8 + (lane & 7)) * 16) = a + a2;
    }
    bar();
    if (tid < 128) {
      const int j = tid >> 4, c = 2 * (tid & 15), t = c >> 4, gg = (c & 15) >> 2, r = c & 3;
      const LAS float* z = (const LAS float*)(L + L_T4P + ((t * 4 + gg) * 8 + j) * 16) + r;
      const float z0 = (z[0] + z[256]) + (z[512] + z[768]), z1 = (z[1] + z[257]) + (z[513] + z[769]);
      *(LAS unsigned*)(L + L_H1 + j * 80 + c * 2) = pk2(silu(rbf(z0)), silu(rbf(z1)));
    }
    bar();
    STAMP(12);
    if (w < 5) {
      const v4u bb = *(const LAS v4u*)(L + L_H1 + (lane & 7) * 80 + g * 16);
      f4 y[4];
#pragma unroll
      for (int tt = 0; tt < 4; ++tt) y[tt] = mfma32(a5[tt], bb, f4{0, 0, 0, 0});
      const bool up = i16 >= 8;
      st_sc(tg4(up ? y[1] : y[0], t10), rsws, o5);
      st_sc(tg4(up ? y[3] : y[2], t10), rsws, o5 + 128);
    }
    STAMP3(12);
  }
  const unsigned tD = (unsigned)__builtin_amdgcn_s_memrealtime();
  STAMP(13);
  STAMP3(13);
  if (w < 5) {
    const bool act = lane < NMLP;
    const unsigned o = WS_T5P + (unsigned)(((act ? lane : 0) * 8 + b) * 640 + 20 * h + 4 * w) * 4;
    const LAS unsigned* vec = (const LAS unsigned*)(L + L_VEC);
    const unsigned g0 = vec[10 + 2 * w], g1 = vec[10 + 2 * w + 1];
    const f4 x1 = *(const LAS f4*)(L + L_X1O + w * 16);
    if (is_mlp) until(tD, GD1); else until(tB, GD2);
    if (w == 3) STAMP3(6);
    v4u v;
    for (unsigned spins = 0;; ++spins) {
      CFENCE();
      v = ld_sc(rsws, o);
      if (__all(tagged4(v, t10)) || spins > SPIN_G) { if (w == 3) { COUNT3(8, spins + 1); STAMP3(7); } break; }
      __builtin_amdgcn_s_sleep(TS);
    }
    f4 sm;
#pragma unroll
    for (int k = 0; k < 4; ++k) sm[k] = wave_sum(act ? untg(v[k]) : 0.f);
    if (lane == 0) {
      const float r0 = x1[0] + rbf(blo(g0) * rbf(sm[0])), r1 = x1[1] + rbf(bhi(g0) * rbf(sm[1]));
      const float r2 = x1[2] + rbf(blo(g1) * rbf(sm[2])), r3 = x1[3] + rbf(bhi(g1) * rbf(sm[3]));
      *(GAS v2u*)(p.out + b * DM + 20 * h + 4 * w) = v2u{pk2(r0, r1), pk2(r2, r3)};
    }
  }
  if (id == 0 && tid == 448) {
    const unsigned E = __builtin_amdgcn_raw_buffer_load_b32(rsws, WS_EPOCH, 0, 16);
    __builtin_amdgcn_raw_buffer_store_b32(E + 1u, rsws, WS_EPOCH, 0, 16);
  }
  STAMP(15);
}

}

extern "C" void kernel_launch(void* const* d_in, const int* in_sizes, int n_in, void* d_out, int out_size, void* d_ws,
                              size_t ws_size, hipStream_t stream) {
  Params p;
  p.x = (const u16*)d_in[0];
  p.kc = (const u16*)d_in[1];
  p.vc = (const u16*)d_in[2];
  p.wq = (const u16*)d_in[3];
  p.wo = (const u16*)d_in[4];
  p.w1 = (const u16*)d_in[6];
  p.w2 = (const u16*)d_in[7];
  p.c = (const u16*)d_in[8];
  p.wmod = (const u16*)d_in[9];
  p.out = (u16*)d_out;
  p.ws = (unsigned char*)d_ws;
  hipLaunchKernelGGL(fused_block, dim3(256), dim3(512), 0, stream, p);
}
```

```cpp
#include <hip/hip_runtime.h>
#include <stdint.h>

#define DIAG 0

namespace {

typedef unsigned short u16;
typedef unsigned v4u __attribute__((ext_vector_type(4)));
typedef unsigned v2u __attribute__((ext_vector_type(2)));
typedef float f4 __attribute__((ext_vector_type(4)));
typedef short s4 __attribute__((ext_vector_type(4)));
typedef short s8 __attribute__((ext_vector_type(8)));
typedef __bf16 bf2 __attribute__((ext_vector_type(2)));
typedef __bf16 bf8 __attribute__((ext_vector_type(8)));
typedef __amdgpu_buffer_rsrc_t rsrc_t;
#define LAS __attribute__((address_space(3)))
#define GAS __attribute__((address_space(1)))
#define DI __device__ __forceinline__

constexpr int NB = 8, DM = 640, NH = 32, DH = 128, SK = 4096, WIN = 1024, FF = 1728;
constexpr int NSW = 7;
constexpr int NL = 37;
#define TD_A 6
#define TD_B 8
#define TD_C 16
#define TD_D 8
#define TD_D2 4
#define TS 4
#define ABL_CTRL 0
#define ABL_SLOT 0
#define ABL_ROLE 0
#define ABL_SENT 0
#define LEAD 3
#define MP0 12
#define MPACE 11
#define DELTA 0
#define HEAVY 3
#define ABL_G1 0
#define ABL_G2 0
#define ABL_G3 0
#define ABL_TAIL 0
#define GB 25
#define GC 30
#define GD1 40
#define GD2 220
#define SLOT0 20
#define SLOTP 3
#define WOLATE 0
#define MLPLATE 2
#define HN 45
#define MPRE 0
#define INIT PACE_
#define CPACE 12
#define WARM 1
#define INDIC 1
#define ABL_WO 0
#define ABL_W12 0
#define D1 600
#define D2 100
#define PSLEEP 32
#define DPACE 15
#define PACE_ 3
constexpr int PACE = PACE_;
#define NPRE_ 16
constexpr int NPRE = NPRE_;

constexpr int NMLP = 54;
constexpr unsigned WS_EPOCH = 0;
constexpr unsigned WS_INIT = 4096;
constexpr unsigned WS_MODG = 8192;
constexpr unsigned WS_MODD = WS_MODG + NB * 1920 * 8;
constexpr unsigned WS_QP = WS_MODD + NB * 640 * 4;
constexpr unsigned WS_OX = WS_QP + NH * 8 * 8 * 128 * 4;
constexpr unsigned WS_T1P = WS_OX + NH * 8 * 256;
constexpr unsigned WS_X1B = WS_T1P + NB * NH * DM * 4;
constexpr unsigned WS_T5P = WS_X1B + NB * 320 * 4;
constexpr unsigned WS_END = WS_T5P + NMLP * NB * DM * 4;
constexpr unsigned WS_DIAG = 4u << 20;
constexpr unsigned SENT = 0xFFFFFFFFu;
static_assert(WS_END <= WS_DIAG, "workspace map");

constexpr unsigned L_RT = 0;
constexpr unsigned L_QT = 40960;
constexpr unsigned L_W2 = 61440;
constexpr unsigned L_T1 = 102400;
constexpr unsigned L_CS = 122880;
constexpr unsigned L_MISC = 143360;
constexpr unsigned L_FLAG = L_MISC;
constexpr unsigned L_Q = L_MISC + 64;
constexpr unsigned L_STAT = L_Q + 256;
constexpr unsigned L_VEC = L_STAT + 64;
constexpr unsigned L_X1O = L_VEC + 128;
constexpr unsigned L_SC = L_X1O + 128;
constexpr unsigned L_H2 = L_SC;
constexpr unsigned L_HS = L_SC + 8 * 1296;
constexpr unsigned L_MRG = L_HS + 8 * 176;
constexpr unsigned L_OS = L_MRG + 7 * 132 * 4;
constexpr unsigned L_T4P = L_MRG;
constexpr unsigned L_H1 = L_T4P + 4096;
constexpr unsigned L_END = 163840;
static_assert(L_OS + 8 * 272 <= L_END && L_H1 + 8 * 80 <= L_OS + 8 * 272, "LDS map");
static_assert((L_SC % 16) == 0 && (L_HS % 16) == 0 && (L_MRG % 16) == 0 && (L_OS % 16) == 0 && (L_H1 % 16) == 0, "LDS alignment");

constexpr unsigned SPIN_G = 1u << 16;
constexpr unsigned SPIN_L = 1u << 22;

struct Params {
  const u16 *x, *kc, *vc, *wq, *wo, *w1, *w2, *c, *wmod;
  u16* out;
  unsigned char* ws;
};

DI unsigned rfl(unsigned x) { return __builtin_amdgcn_readfirstlane(x); }
DI float asf(unsigned u) { return __uint_as_float(u); }
DI unsigned asu(float f) { return __float_as_uint(f); }
DI unsigned pk2(float lo, float hi) { bf2 v = {(__bf16)lo, (__bf16)hi}; return __builtin_bit_cast(unsigned, v); }
DI float rbf(float f) { return asf(pk2(f, 0.f) << 16); }
DI float blo(unsigned u) { return asf(u << 16); }
DI float bhi(unsigned u) { return asf(u & 0xffff0000u); }
DI float dot2(unsigned a, unsigned b, float c) {
  return __builtin_amdgcn_fdot2_f32_bf16(__builtin_bit_cast(bf2, a), __builtin_bit_cast(bf2, b), c, false);
}
template <int CTRL> DI float dpp(float x) {
  return __builtin_bit_cast(float, __builtin_amdgcn_mov_dpp(__builtin_bit_cast(int, x), CTRL, 0xf, 0xf, true));
}
DI float row16_sum(float x) { x += dpp<0x128>(x); x += dpp<0x124>(x); x += dpp<0x122>(x); x += dpp<0x121>(x); return x; }
DI float oct_sum(float x) { x += dpp<0xB1>(x); x += dpp<0x4E>(x); x += dpp<0x141>(x); return x; }
DI float x16_sum(float x) {
  auto s = __builtin_amdgcn_permlane16_swap(asu(x), asu(x), false, false);
  return asf(s[0]) + asf(s[1]);
}
DI float x32_sum(float x) {
  auto t = __builtin_amdgcn_permlane32_swap(asu(x), asu(x), false, false);
  return asf(t[0]) + asf(t[1]);
}
DI float xrow_sum(float x) { return x32_sum(x16_sum(x)); }
DI float xrow_max(float x) {
  auto s = __builtin_amdgcn_permlane16_swap(asu(x), asu(x), false, false);
  x = fmaxf(asf(s[0]), asf(s[1]));
  auto t = __builtin_amdgcn_permlane32_swap(asu(x), asu(x), false, false);
  return fmaxf(asf(t[0]), asf(t[1]));
}
DI float wave_sum(float x) { return xrow_sum(row16_sum(x)); }
DI f4 mfma32(v4u a, v4u b, f4 c) {
  return __builtin_amdgcn_mfma_f32_16x16x32_bf16(__builtin_bit_cast(bf8, a), __builtin_bit_cast(bf8, b), c, 0, 0, 0);
}
DI f4 mfma16(v2u a, v2u b, f4 c) {
  return __builtin_amdgcn_mfma_f32_16x16x16bf16_1k(__builtin_bit_cast(s4, a), __builtin_bit_cast(s4, b), c, 0, 0, 0);
}
DI v2u trread(const LAS unsigned char* p) {
  return __builtin_bit_cast(v2u, __builtin_amdgcn_ds_read_tr16_b64_v4i16((LAS s4*)p));
}
DI rsrc_t mkrs(const void* p, unsigned bytes) { return __builtin_amdgcn_make_buffer_rsrc((void*)p, 0, (int)bytes, 0x00020000); }
DI v4u ld_nt(rsrc_t r, unsigned vo, unsigned so) { return __builtin_amdgcn_raw_buffer_load_b128(r, vo, so, 2); }
#define KAUX 2
#define VAUX 2
DI v4u ld_k(rsrc_t r, unsigned vo, unsigned so) { return __builtin_amdgcn_raw_buffer_load_b128(r, vo, so, KAUX); }
DI v4u ld_v(rsrc_t r, unsigned vo, unsigned so) { return __builtin_amdgcn_raw_buffer_load_b128(r, vo, so, VAUX); }
DI v4u ld_sc(rsrc_t r, unsigned vo) { return __builtin_amdgcn_raw_buffer_load_b128(r, vo, 0, 16); }
DI v2u ld_sc2(rsrc_t r, unsigned vo) { return __builtin_amdgcn_raw_buffer_load_b64(r, vo, 0, 16); }
DI void st_sc(v4u v, rsrc_t r, unsigned vo) { __builtin_amdgcn_raw_buffer_store_b128(v, r, vo, 0, 16); }
DI void st_sc2(v2u v, rsrc_t r, unsigned vo) { __builtin_amdgcn_raw_buffer_store_b64(v, r, vo, 0, 16); }
DI unsigned canonu(unsigned u) { return u == 0xFFFFFFFFu ? 0x7FFF7FFFu : u; }
DI void dma16(rsrc_t r, unsigned lds, unsigned vo, unsigned so) {
  unsigned keep;
  asm volatile("s_nop 4\n\ts_mov_b32 %0, m0\n\ts_mov_b32 m0, %1\n\ts_nop 0\n\tbuffer_load_dwordx4 %2, %3, %4 offen nt lds\n\ts_mov_b32 m0, %0"
               : "=&s"(keep) : "s"(lds), "v"(vo), "s"(r), "s"(so) : "memory");
}
DI void dma16s(rsrc_t r, unsigned lds, unsigned vo, unsigned so) {
  unsigned keep;
  asm volatile("s_mov_b32 %0, m0\n\ts_mov_b32 m0, %1\n\ts_nop 0\n\tbuffer_load_dwordx4 %2, %3, %4 offen nt lds\n\ts_mov_b32 m0, %0"
               : "=&s"(keep) : "s"(lds), "v"(vo), "s"(r), "s"(so) : "memory");
}
template <int N> DI void vmwait() { asm volatile("s_waitcnt vmcnt(%0)" :: "n"(N) : "memory"); }
#define VMWAIT(n) vmwait<(n)>()
DI void bar() { asm volatile("s_waitcnt lgkmcnt(0)\n\ts_barrier" ::: "memory"); }
#define CFENCE() asm volatile("" ::: "memory")
#define SCHED() __builtin_amdgcn_sched_barrier(0)
DI unsigned canon(float f) { unsigned u = asu(f); return u == SENT ? 0x7FFFFFFFu : u; }
DI unsigned tg(float f, unsigned t10) { return (asu(f) & ~0x3FFu) | t10; }
DI v4u tg4(f4 v, unsigned t10) { return v4u{tg(v[0], t10), tg(v[1], t10), tg(v[2], t10), tg(v[3], t10)}; }
DI bool tagged4(v4u v, unsigned t10) { return (((v.x ^ t10) | (v.y ^ t10) | (v.z ^ t10) | (v.w ^ t10)) & 0x3FFu) == 0u; }
DI float untg(unsigned u) { return asf(u & ~0x3FFu); }
DI bool valid4(v4u v) { return v.x != SENT && v.y != SENT && v.z != SENT && v.w != SENT; }
DI float rcp(float x) { return __builtin_amdgcn_rcpf(x); }
DI float silu(float v) { return v * rcp(1.f + __builtin_amdgcn_exp2f(v * -1.4426950408889634f)); }

#define STAMP(k) do { } while (0)
#define STAMP2(k) do { } while (0)
#define STAMP3(k) do { } while (0)
#define COUNT3(k, v) do { } while (0)

__global__ void __launch_bounds__(512, 2) fused_block(Params p) {
  __shared__ __attribute__((aligned(16))) unsigned char smem[L_END];
  LAS unsigned char* L = (LAS unsigned char*)smem;
  const unsigned lds0 = (unsigned)(uintptr_t)L;
  const int tid = threadIdx.x, lane = tid & 63;
  const int w = (int)rfl(tid >> 6);
  const int id = blockIdx.x, xcd = id & 7, sl = id >> 3;
  const int h = xcd * 4 + (sl >> 3), b = sl & 7;
  const int g = lane >> 4, i16 = lane & 15;
  const int tq = i16 >> 2, tp = lane & 3;
  const rsrc_t rsws = mkrs(p.ws, WS_END);
  volatile LAS unsigned* flags = (volatile LAS unsigned*)(L + L_FLAG);
  const int mpr = (sl >> 1) * 8 + xcd;
  const bool has_mod = !ABL_ROLE && sl < 16 && mpr < 60;
  const bool crit = has_mod && mpr < 20;
  const int mcol0 = (2 * mpr + (sl & 1)) * 32;
  const int quad = ((sl - 16) >> 2) * 8 + xcd;
  const bool is_mlp = !ABL_ROLE && sl >= 16 && quad < 27;
  const int mm = 2 * quad + (sl & 1), hf = (sl >> 1) & 1;
  const bool dense = has_mod && mpr >= 30 && mpr < 50;

  const rsrc_t rswo = mkrs(p.wo + (size_t)h * DH * DM, DH * DM * 2);
  unsigned wov[5], w2v[5], w1v;
  static_assert(MPRE <= 40 && MPRE + MP0 <= 60 && MPRE + MP0 <= 63, "MLP pieces");
  STAMP(0);
  const unsigned tstart = (unsigned)__builtin_amdgcn_s_memrealtime();
  auto sched = [&](int i, bool light) {
    if (DELTA && light) { while ((unsigned)__builtin_amdgcn_s_memrealtime() - tstart < (unsigned)(i * DELTA / 10)) __builtin_amdgcn_s_sleep(1); }
  };

  unsigned t10 = 0;
  if (w < NSW) {
    const rsrc_t rsk = mkrs(p.kc + ((size_t)(b * NH + h) * SK + (SK - WIN)) * DH, WIN * DH * 2);
    const rsrc_t rsv = mkrs(p.vc + ((size_t)(b * NH + h) * SK + (SK - WIN)) * DH, WIN * DH * 2);
    const unsigned vo = (unsigned)(lane * 16 + w * 1024);
    v4u kr[NL], vr[NL], cr[2];
    if (has_mod && w < 5) {
      const GAS v4u* cp = (const GAS v4u*)p.c;
      cr[0] = cp[w * 128 + lane];
      cr[1] = cp[w * 128 + 64 + lane];
    }
    auto silu_c = [&]() {
      if (w < 5) {
#pragma unroll
        for (int u = 0; u < 2; ++u) {
          const int q = w * 128 + u * 64 + lane, j = q / 80, k8 = q - 80 * j;
          v4u o;
          o.x = pk2(silu(blo(cr[u].x)), silu(bhi(cr[u].x)));
          o.y = pk2(silu(blo(cr[u].y)), silu(bhi(cr[u].y)));
          o.z = pk2(silu(blo(cr[u].z)), silu(bhi(cr[u].z)));
          o.w = pk2(silu(blo(cr[u].w)), silu(bhi(cr[u].w)));
          *(LAS v4u*)(L + L_SC + j * 1296 + k8 * 16) = o;
        }
      }
    };
    const rsrc_t rsrt = mkrs(p.wmod, DM * 6 * DM * 2);
    const unsigned rt_vo = (unsigned)((lane >> 2) * 7680 + mcol0 * 2 + (lane & 3) * 16);
    auto dma_rt = [&](int m) { dma16(rsrt, lds0 + L_RT + m * 1024, rt_vo, (unsigned)m * 16u * 7680u); };
    if (!crit) {
#pragma unroll
      for (int i = 0; i < INIT; ++i) kr[i] = ld_k(rsk, vo, i * 7168);
    }
    if (has_mod) { for (int m = w; m < 40; m += NSW) dma_rt(m); }
    SCHED();
    bar();
    if (has_mod) {
      silu_c();
      VMWAIT(0);
      asm volatile("s_waitcnt lgkmcnt(0)" ::: "memory");
      if (lane == 0) __hip_atomic_fetch_add((LAS unsigned*)(L + L_FLAG), 1u, __ATOMIC_RELAXED, __HIP_MEMORY_SCOPE_WORKGROUP);
    }
    {
      const rsrc_t rsq = mkrs(p.wq, DM * NH * DH * 2);
      for (int n = w; n < 20; n += NSW) {
        const int rl = 4 * n + g;
        dma16(rsq, lds0 + L_QT + n * 1024, (unsigned)((80 * b + rl) * 8192 + h * 256 + ((i16 ^ (2 * (rl & 7))) * 16)), 0);
      }
    }
    if (crit) {
#pragma unroll
      for (int i = 0; i < INIT; ++i) kr[i] = ld_k(rsk, vo, i * 7168);
    }
    SCHED();
    const rsrc_t rsw1 = mkrs(p.w1, DM * FF * 2);
    const rsrc_t rsw2 = mkrs(p.w2 + (size_t)(is_mlp ? mm : 0) * 32 * DM, 32 * DM * 2);
    auto slot = [&](int s) {
      const int n = 7 * s + w;
      if (n < 40) {
        dma16(rsw1, lds0 + L_RT + n * 1024, (unsigned)((lane >> 2) * (FF * 2) + mm * 64 + (lane & 3) * 16), (unsigned)n * 16u * (FF * 2));
      } else if (n < 60) {
        const int ci = (n - 40) * 64 + lane, row = ci / 40, pos = ci - 40 * row;
        dma16(rsw2, lds0 + L_W2 + (n - 40) * 1024, (unsigned)(row * 1280 + hf * 640 + (pos ^ (2 * (row & 3))) * 16), 0);
      }
    };
    static_assert(SLOT0 + 8 * SLOTP < NL + NPRE - 1, "all slots inside phase A");
    const bool heavy = HEAVY == 1 ? (is_mlp || has_mod) : HEAVY == 2 ? is_mlp : HEAVY == 3 ? has_mod : false;
#pragma unroll
    for (int i = INIT; i < NL + NPRE; ++i) {
      sched(i, !is_mlp);
      if (heavy && i < HN) VMWAIT(PACE); else VMWAIT(PACE - 1);
      if (i < NL) kr[i] = ld_k(rsk, vo, i * 7168); else vr[i - NL] = ld_v(rsv, vo, (i - NL) * 7168);
      SCHED();
      if (i == INIT + 4) {
        asm volatile("s_waitcnt lgkmcnt(0)" ::: "memory");
        if (lane == 0) __hip_atomic_fetch_add((LAS unsigned*)(L + L_FLAG) + 2, 1u, __ATOMIC_RELAXED, __HIP_MEMORY_SCOPE_WORKGROUP);
        STAMP(1);
      }
      if (i >= SLOT0 && ((i - SLOT0) % SLOTP) == 0 && (i - SLOT0) / SLOTP < 9) { if (is_mlp && !MLPLATE) slot((i - SLOT0) / SLOTP); SCHED(); }
    }
    STAMP(2);
    {
      unsigned spins = 0;
      while (flags[1] == 0u) { __builtin_amdgcn_s_sleep(1); if (++spins > SPIN_L) break; }
    }
    CFENCE();
    STAMP(3);
    const v4u q4 = *(const LAS v4u*)(L + L_Q + i16 * 16);
    t10 = flags[4] & 0x3FFu;
    float s[NL];
#pragma unroll
    for (int i = 0; i < NL; ++i) {
      float d = dot2(kr[i].x, q4.x, 0.f);
      d = dot2(kr[i].y, q4.y, d); d = dot2(kr[i].z, q4.z, d); d = dot2(kr[i].w, q4.w, d);
      s[i] = row16_sum(d);
      if ((i & 3) == 3 && NPRE + (i >> 2) < NL) { SCHED(); sched(NL + NPRE + (i >> 2), !is_mlp); if (heavy && NL + NPRE + (i >> 2) < HN) VMWAIT(PACE); else VMWAIT(PACE - 1); vr[NPRE + (i >> 2)] = ld_v(rsv, vo, (NPRE + (i >> 2)) * 7168); SCHED(); }
    }
    constexpr int NV1 = NPRE + (NL >> 2);
    if (w >= 4) s[NL - 1] = -INFINITY;
    float m = s[0];
#pragma unroll
    for (int i = 1; i < NL; ++i) m = fmaxf(m, s[i]);
    m = xrow_max(m);
    constexpr float C2 = 0.08838834764831845f * 1.4426950408889634f;
    const float mc = m * C2;
    unsigned pp[NL];
    float l = 0.f;
#pragma unroll
    for (int i = 0; i < NL; ++i) {
      pp[i] = pk2(0.f, __builtin_amdgcn_exp2f(fmaf(s[i], C2, -mc)));
      l += asf(pp[i]);
    }
    l = xrow_sum(l);
    STAMP(4);
    float acc[8];
#pragma unroll
    for (int k = 0; k < 8; ++k) acc[k] = 0.f;
#pragma unroll
    for (int i = 0; i < NL; ++i) {
      const unsigned p1 = pp[i], p0 = p1 >> 16;
      acc[0] = dot2(vr[i].x, p0, acc[0]); acc[1] = dot2(vr[i].x, p1, acc[1]);
      acc[2] = dot2(vr[i].y, p0, acc[2]); acc[3] = dot2(vr[i].y, p1, acc[3]);
      acc[4] = dot2(vr[i].z, p0, acc[4]); acc[5] = dot2(vr[i].z, p1, acc[5]);
      acc[6] = dot2(vr[i].w, p0, acc[6]); acc[7] = dot2(vr[i].w, p1, acc[7]);
      SCHED();
      if (NV1 + i < NL) { sched(NL + NV1 + i, !is_mlp); if (heavy && NL + NV1 + i < HN) VMWAIT(PACE); else VMWAIT(PACE - 1); vr[NV1 + i] = ld_v(rsv, vo, (NV1 + i) * 7168); SCHED(); }
      if (NV1 + i == NL - 1 - LEAD) {
        if (lane == 0) __hip_atomic_fetch_add((LAS unsigned*)(L + L_FLAG) + 3, 1u, __ATOMIC_RELAXED, __HIP_MEMORY_SCOPE_WORKGROUP);
      }
    }
    STAMP(5);
#pragma unroll
    for (int k = 0; k < 8; ++k) acc[k] = xrow_sum(acc[k]);
    if (g == 0) {
      LAS float* mr = (LAS float*)(L + L_MRG) + w * 132;
      *(LAS f4*)(mr + 4 + 8 * i16) = f4{acc[0], acc[1], acc[2], acc[3]};
      *(LAS f4*)(mr + 8 + 8 * i16) = f4{acc[4], acc[5], acc[6], acc[7]};
      if (i16 == 0) { mr[0] = m; mr[1] = l; }
    }
    asm volatile("s_waitcnt lgkmcnt(0)" ::: "memory");
    unsigned arrived = 0;
    if (lane == 0) arrived = __hip_atomic_fetch_add((LAS unsigned*)(L + L_FLAG) + 8, 1u, __ATOMIC_RELAXED, __HIP_MEMORY_SCOPE_WORKGROUP);
    if (rfl(arrived) == (unsigned)(NSW - 1)) {
      CFENCE();
      if (lane < 32) {
        const LAS float* mr = (const LAS float*)(L + L_MRG);
        float mw[NSW], lw[NSW];
        f4 aw[NSW];
#pragma unroll
        for (int u = 0; u < NSW; ++u) { mw[u] = mr[u * 132]; lw[u] = mr[u * 132 + 1]; aw[u] = *(const LAS f4*)(mr + u * 132 + 4 + 4 * lane); }
        const float M = fmaxf(fmaxf(fmaxf(mw[0], mw[1]), fmaxf(mw[2], mw[3])), fmaxf(fmaxf(mw[4], mw[5]), mw[6]));
        float Lsum = 0.f;
        f4 o = {0, 0, 0, 0};
#pragma unroll
        for (int u = 0; u < NSW; ++u) {
          const float f = __builtin_amdgcn_exp2f((mw[u] - M) * C2);
          Lsum = fmaf(f, lw[u], Lsum);
          o += f * aw[u];
        }
        const float inv = rcp(Lsum);
        st_sc2(v2u{canonu(pk2(o[0] * inv, o[1] * inv)), canonu(pk2(o[2] * inv, o[3] * inv))}, rsws, WS_OX + (unsigned)(h * 8 + b) * 256 + lane * 8);
      }
    }
  } else {
    __builtin_amdgcn_s_setprio(3);
    if (lane == 0) { flags[0] = 0u; flags[1] = 0u; flags[2] = 0u; flags[3] = 0u; flags[5] = 0u; flags[6] = 0u; flags[7] = 0u; flags[8] = 0u; }
    if (ABL_CTRL) {
      bar();
      while ((unsigned)__builtin_amdgcn_s_memrealtime() - tstart < 800) __builtin_amdgcn_s_sleep(8);
      if (lane == 0) flags[1] = 1u;
    } else {
    const unsigned E = __builtin_amdgcn_raw_buffer_load_b32(rsws, WS_EPOCH, 0, 16);
    unsigned warm = 0;
    {
      const int n = sl * 64 + lane, pi = n / 640, row = n - 640 * pi, pr = xcd + 8 * pi;
      if (WARM && pr < 20) warm = __builtin_amdgcn_raw_buffer_load_b32(mkrs(p.wmod, DM * 6 * DM * 2), (unsigned)(row * 7680 + pr * 128), 0, 2);
    }
    const GAS v4u* xp = (const GAS v4u*)p.x;
    v4u xr[10];
    const int sj = lane >> 3, sub = lane & 7;
#pragma unroll
    for (int u = 0; u < 10; ++u) xr[u] = xp[sj * 80 + sub + 8 * u];
    SCHED();
    bar();
    unsigned xh[5];
#pragma unroll
    for (int u = 0; u < 5; ++u) { const int idx = u * 64 + lane, j = idx / 40, m = idx - 40 * j; xh[u] = ((const GAS unsigned*)p.x)[j * 320 + 40 * b + m]; }
    if (!ABL_SENT) {
      const v4u sv = {SENT, SENT, SENT, SENT};
      if (lane < 16) st_sc(sv, rsws, WS_OX + (unsigned)(h * 8 + b) * 256 + lane * 16);
      if (lane < 5) st_sc2(v2u{SENT, SENT}, rsws, WS_X1B + (unsigned)(b * 320 + 10 * h) * 4 + lane * 8);
      if (dense && lane < 32) st_sc(sv, rsws, WS_MODD + (unsigned)((lane >> 2) * 640 + (mcol0 - 1920) / 2 + (lane & 3) * 4) * 4);
    }
    STAMP(1);
    {
      float sm = 0.f;
#pragma unroll
      for (int u = 0; u < 10; ++u)
        sm += ((blo(xr[u].x) + bhi(xr[u].x)) + (blo(xr[u].y) + bhi(xr[u].y))) + ((blo(xr[u].z) + bhi(xr[u].z)) + (blo(xr[u].w) + bhi(xr[u].w)));
      const float mean = oct_sum(sm) * (1.f / DM);
      float sq = 0.f;
#pragma unroll
      for (int u = 0; u < 10; ++u) {
#pragma unroll
        for (int k = 0; k < 4; ++k) { const float a = blo(xr[u][k]) - mean, c2 = bhi(xr[u][k]) - mean; sq = fmaf(a, a, sq); sq = fmaf(c2, c2, sq); }
      }
      const float var = oct_sum(sq) * (1.f / DM);
      if (sub == 0) {
        LAS float* st = (LAS float*)(L + L_STAT) + sj * 2;
        st[0] = rbf(mean);
        st[1] = rcp(rbf(__builtin_amdgcn_sqrtf(rbf(rbf(var) + 1e-5f))));
      }
    }
    VMWAIT(0);
    asm volatile("" :: "v"(warm));
    const unsigned T = rfl(E) + 1u;
    t10 = T & 0x3FFu;
    if (lane == 0) flags[4] = T;
    if (lane == 0) st_sc2(v2u{T, ~T}, rsws, WS_INIT + (unsigned)(h * 8 + b) * 8);
    STAMP2(9);
    if (has_mod) {
      unsigned spins = 0;
      while (flags[0] < (unsigned)NSW) { __builtin_amdgcn_s_sleep(1); if (++spins > SPIN_L) break; }
      CFENCE();
      STAMP(2);
      f4 a0 = {0, 0, 0, 0}, a1 = {0, 0, 0, 0};
      const LAS unsigned char* ap = L + L_RT + (8 * g + tq) * 64 + tp * 8;
      const LAS unsigned char* bp = L + L_SC + (lane & 7) * 1296 + g * 16;
#pragma unroll 2
      for (int s = 0; s < 20; ++s) {
        const v4u bb = *(const LAS v4u*)(bp + s * 64);
        const v2u x0 = trread(ap + s * 2048), x1 = trread(ap + s * 2048 + 256);
        const v2u y0 = trread(ap + s * 2048 + 32), y1 = trread(ap + s * 2048 + 256 + 32);
        a0 = mfma32(v4u{x0.x, x0.y, x1.x, x1.y}, bb, a0);
        a1 = mfma32(v4u{y0.x, y0.y, y1.x, y1.y}, bb, a1);
      }
      if (i16 < 8) {
        if (dense) {
          const unsigned o = WS_MODD + (unsigned)(i16 * 640 + (mcol0 - 1920 + 4 * g) / 2) * 4;
          st_sc2(v2u{canonu(pk2(a0[0], a0[1])), canonu(pk2(a0[2], a0[3]))}, rsws, o);
          st_sc2(v2u{canonu(pk2(a1[0], a1[1])), canonu(pk2(a1[2], a1[3]))}, rsws, o + 32);
        } else {
          const unsigned o = WS_MODG + (unsigned)(i16 * 1920 + (mcol0 + 4 * g) / 2) * 8;
          st_sc(v4u{pk2(a0[0], a0[1]), T, pk2(a0[2], a0[3]), T}, rsws, o);
          st_sc(v4u{pk2(a1[0], a1[1]), T, pk2(a1[2], a1[3]), T}, rsws, o + 64);
        }
      }
    }
    STAMP(3);
    {
      v2u sa[5], ca[5], fl;
      if (D1) { while ((unsigned)__builtin_amdgcn_s_memrealtime() - tstart < D1) __builtin_amdgcn_s_sleep(2); }
      STAMP2(10);
      if (ABL_G1) { for (int u = 0; u < 5; ++u) { sa[u] = v2u{0u, T}; ca[u] = v2u{0u, T}; } }
      for (unsigned spins = 0; !ABL_G1; ++spins) {
        CFENCE();
        bool ok = true;
#pragma unroll
        for (int u = 0; u < 5; ++u) {
          const int idx = u * 64 + lane, j = idx / 40, m = idx - 40 * j;
          const unsigned o = WS_MODG + (unsigned)(j * 1920 + 40 * b + m) * 8;
          sa[u] = ld_sc2(rsws, o);
          ca[u] = ld_sc2(rsws, o + 320 * 8);
        }
        fl = ld_sc2(rsws, WS_INIT + (unsigned)(h * 8 + (lane & 7)) * 8);
#pragma unroll
        for (int u = 0; u < 5; ++u) ok = ok && sa[u].y == T && ca[u].y == T;
        ok = ok && fl.x == T && fl.y == ~T;
        if (__all(ok) || spins > SPIN_G) break;
        __builtin_amdgcn_s_sleep(PSLEEP);
      }
      STAMP2(11);
      asm volatile("s_waitcnt lgkmcnt(0)" ::: "memory");
#pragma unroll
      for (int u = 0; u < 5; ++u) {
        const int idx = u * 64 + lane, j = idx / 40, m = idx - 40 * j;
        const float mean = ((LAS float*)(L + L_STAT))[2 * j], den = ((LAS float*)(L + L_STAT))[2 * j + 1];
        const float n0 = rbf(rbf(blo(xh[u]) - mean) * den), n1 = rbf(rbf(bhi(xh[u]) - mean) * den);
        const float h0 = rbf(n0 * rbf(1.f + blo(ca[u].x))) + blo(sa[u].x), h1 = rbf(n1 * rbf(1.f + bhi(ca[u].x))) + bhi(sa[u].x);
        *(LAS unsigned*)(L + L_HS + j * 176 + m * 4) = pk2(h0, h1);
      }
    }
    STAMP(4);
    {
      unsigned spins = 0;
      while (flags[2] < (unsigned)NSW) { __builtin_amdgcn_s_sleep(1); if (++spins > SPIN_L) break; }
      CFENCE();
    }
    STAMP2(12);
    {
      f4 qa[8];
#pragma unroll
      for (int t = 0; t < 8; ++t) qa[t] = f4{0, 0, 0, 0};
#pragma unroll
      for (int s = 0; s < 5; ++s) {
        const v2u bb = *(const LAS v2u*)(L + L_HS + (lane & 7) * 176 + s * 32 + g * 8);
        const int row = 16 * s + 4 * g + tq;
        const LAS unsigned char* rp = L + L_QT + row * 256 + (tp & 1) * 8;
        const int sw = 2 * (row & 7);
#pragma unroll
        for (int t = 0; t < 8; ++t) qa[t] = mfma16(trread(rp + (((2 * t + (tp >> 1)) ^ sw) * 16)), bb, qa[t]);
      }
      if (i16 < 8 && !ABL_G2) {
        const unsigned o = WS_QP + (unsigned)(((h * 8 + b) * 8 + i16) * 128 + 4 * g) * 4;
#pragma unroll
        for (int t = 0; t < 8; ++t) st_sc(tg4(qa[t], t10), rsws, o + t * 64);
      }
    }
    STAMP(5);
    const unsigned tpub = (unsigned)__builtin_amdgcn_s_memrealtime();
    {
      v4u qv[4];
      if (D2) { while ((unsigned)__builtin_amdgcn_s_memrealtime() - tpub < D2) __builtin_amdgcn_s_sleep(2); }
      STAMP2(13);
      if (ABL_G2) { for (int u = 0; u < 4; ++u) qv[u] = v4u{t10, t10, t10, t10}; }
      for (unsigned spins = 0; !ABL_G2; ++spins) {
        CFENCE();
        bool ok = true;
#pragma unroll
        for (int u = 0; u < 4; ++u) qv[u] = ld_sc(rsws, WS_QP + (unsigned)(((h * 8 + 2 * u + (lane >> 5)) * 8 + b) * 128 + 4 * (lane & 31)) * 4);
#pragma unroll
        for (int u = 0; u < 4; ++u) ok = ok && tagged4(qv[u], t10);
        if (__all(ok) || spins > SPIN_G) break;
        __builtin_amdgcn_s_sleep(PSLEEP);
      }
      f4 qs;
#pragma unroll
      for (int k = 0; k < 4; ++k) qs[k] = x32_sum((untg(qv[0][k]) + untg(qv[1][k])) + (untg(qv[2][k]) + untg(qv[3][k])));
      if (lane < 32) *(LAS v2u*)(L + L_Q + lane * 8) = v2u{pk2(qs[0], qs[1]), pk2(qs[2], qs[3])};
      asm volatile("s_waitcnt lgkmcnt(0)" ::: "memory");
      if (lane == 0) flags[1] = 1u;
    }
    STAMP(6);
    if (!ABL_G3) {
      const unsigned go = WS_MODG + (unsigned)(b * 1920 + (lane < 10 ? 640 + 10 * h + lane : 1600 + 10 * h + (lane < 20 ? lane - 10 : 0))) * 8;
      const unsigned xv = ((const GAS unsigned*)p.x)[b * 320 + 10 * h + (lane < 10 ? lane : 0)];
      v2u gv;
      for (unsigned spins = 0;; ++spins) {
        CFENCE();
        gv = ld_sc2(rsws, go);
        if (__all(gv.y == T) || spins > SPIN_G) break;
        __builtin_amdgcn_s_sleep(32);
      }
      if (lane < 20) *(LAS unsigned*)(L + L_VEC + lane * 4) = gv.x;
      if (lane < 10) *(LAS unsigned*)(L + L_VEC + 80 + lane * 4) = xv;
      if (is_mlp) {
        for (unsigned spins = 0;; ++spins) {
          CFENCE();
          bool ok = true;
#pragma unroll
          for (int u = 0; u < 4; ++u) { const v2u f = ld_sc2(rsws, WS_INIT + (unsigned)(u * 64 + lane) * 8); ok = ok && f.x == T && f.y == ~T; }
          if (__all(ok) || spins > SPIN_G) break;
          __builtin_amdgcn_s_sleep(32);
        }
        for (int jj = 0; jj < 4; ++jj) {
          v4u cv[5];
          for (unsigned spins = 0;; ++spins) {
            CFENCE();
            bool ok = true;
#pragma unroll
            for (int u = 0; u < 5; ++u) cv[u] = ld_sc(rsws, WS_MODD + (unsigned)jj * 5120 + (unsigned)(u * 64 + lane) * 16);
#pragma unroll
            for (int u = 0; u < 5; ++u) ok = ok && valid4(cv[u]);
            if (__all(ok) || spins > SPIN_G) break;
            __builtin_amdgcn_s_sleep(32);
          }
#pragma unroll
          for (int u = 0; u < 5; ++u) {
            const int pc = u * 64 + lane, j = 2 * jj + pc / 160, r = pc % 160;
            v4u o = cv[u];
            if (r >= 80) {
#pragma unroll
              for (int k = 0; k < 4; ++k) o[k] = pk2(1.f + blo(o[k]), 1.f + bhi(o[k]));
            }
            *(LAS v4u*)(L + L_CS + (r < 80 ? 10240 + (j * 320 + 4 * r) * 4 : (j * 320 + 4 * (r - 80)) * 4)) = o;
          }
        }
      }
    }
    STAMP(7);
#pragma unroll
    for (int c = 0; c < 5; ++c) { const int ci = c * 64 + lane, row = ci / 10, ch = ci - 10 * row; wov[c] = (unsigned)(row * 1280 + b * 160 + ch * 16); asm volatile("" : "+v"(wov[c])); }
#pragma unroll
    for (int c = 0; c < 5; ++c) {
      const int ci = c * 64 + lane, row = ci / 40, pos = ci - 40 * row;
      w2v[c] = (unsigned)(row * 1280 + hf * 640 + (pos ^ (2 * (row & 3))) * 16); asm volatile("" : "+v"(w2v[c]));
    }
    w1v = (unsigned)((lane >> 2) * (FF * 2) + mm * 64 + (lane & 3) * 16); asm volatile("" : "+v"(w1v));
    if (MLPLATE == 2 && is_mlp) {
      const rsrc_t rsw1t = mkrs(p.w1, DM * FF * 2);
      const rsrc_t rsw2t = mkrs(p.w2 + (size_t)mm * 32 * DM, 32 * DM * 2);
#pragma unroll
      for (int n = 0; n < 60; ++n) {
        if (n >= CPACE) VMWAIT(CPACE - 1);
        if (n < 40) dma16s(rsw1t, lds0 + L_RT + n * 1024, w1v, (unsigned)n * 16u * (FF * 2));
        else dma16s(rsw2t, lds0 + L_W2 + (n - 40) * 1024, w2v[(n - 40) % 5], (unsigned)((n - 40) / 5) * 8u * 1280u);
      }
    }
    if (!WOLATE || (MLPLATE == 1 && MPRE > 0)) {
      unsigned spins = 0;
      while (flags[3] < (unsigned)NSW) { __builtin_amdgcn_s_sleep(1); if (++spins > SPIN_L) break; }
      CFENCE();
      if (!WOLATE) {
#pragma unroll
        for (int n = 0; n < 20; ++n) dma16s(rswo, lds0 + L_T1 + n * 1024, wov[n % 5], (unsigned)(n / 5) * 32u * 1280u);
      }
      if (MLPLATE == 1 && is_mlp) {
        const rsrc_t rsw1t = mkrs(p.w1, DM * FF * 2);
#pragma unroll
        for (int n = 0; n < MPRE; ++n) dma16s(rsw1t, lds0 + L_RT + n * 1024, w1v, (unsigned)n * 16u * (FF * 2));
      }
    }
    }
  }

  bar();
  STAMP(8);
  STAMP3(1);
  if (ABL_CTRL || ABL_TAIL) {
    STAMP(15);
    return;
  }
  auto until = [&](unsigned t0, unsigned ticks) { while ((unsigned)__builtin_amdgcn_s_memrealtime() - t0 < ticks) __builtin_amdgcn_s_sleep(1); };
  auto lds_wait = [&](int k, unsigned v) { unsigned spins = 0; while (flags[k] < v) { if (++spins > SPIN_L) break; } CFENCE(); };
  const rsrc_t rsw1t = mkrs(p.w1, DM * FF * 2);
  const rsrc_t rsw2t = mkrs(p.w2 + (size_t)(is_mlp ? mm : 0) * 32 * DM, 32 * DM * 2);
  auto mlp_piece = [&](int n) {
    if (n < 40) dma16s(rsw1t, lds0 + L_RT + n * 1024, w1v, (unsigned)n * 16u * (FF * 2));
    else dma16s(rsw2t, lds0 + L_W2 + (n - 40) * 1024, w2v[(n - 40) % 5], (unsigned)((n - 40) / 5) * 8u * 1280u);
  };
  const int t1t = w == 0 ? 0 : w - 2;
  v4u af[4];
  unsigned o1;
  { const int cc = 4 * t1t + g; o1 = WS_T1P + (unsigned)((((i16 * 32 + 4 * b + cc / 5) * 32 + h) * 5 + cc % 5) * 16); asm volatile("" : "+v"(o1)); }
  if (w == 1 || w == 2) {
    const int t = tid - 64;
    __builtin_amdgcn_s_sleep(TD_A);
    STAMP3(2);
    v4u v;
    for (unsigned spins = 0;; ++spins) {
      CFENCE();
      v = ld_sc(rsws, WS_OX + (unsigned)h * 2048 + t * 16);
      if (__all(valid4(v)) || spins > SPIN_G) { COUNT3(4, spins + 1); break; }
      __builtin_amdgcn_s_sleep(TS);
    }
    STAMP3(3);
    *(LAS v4u*)(L + L_OS + (t >> 4) * 272 + (t & 15) * 16) = v;
  } else if (w == 7) {
    if (WOLATE) {
#pragma unroll
      for (int n = 0; n < 20; ++n) dma16s(rswo, lds0 + L_T1 + n * 1024, wov[n % 5], (unsigned)(n / 5) * 32u * 1280u);
    }
    if (MLPLATE == 1 && is_mlp) {
#pragma unroll
      for (int n = MPRE; n < MPRE + MP0; ++n) mlp_piece(n);
      if (WOLATE) VMWAIT(MP0); else VMWAIT(MPRE + MP0);
    } else {
      VMWAIT(0);
    }
    if (lane == 0) flags[5] = 1u;
  } else {
    lds_wait(5, 1u);
    const LAS unsigned char* ap = L + L_T1 + (8 * g + tq) * 160 + (16 * t1t + 4 * tp) * 2;
#pragma unroll
    for (int s = 0; s < 4; ++s) {
      const v2u x0 = trread(ap + s * 32 * 160), x1 = trread(ap + s * 32 * 160 + 4 * 160);
      af[s] = v4u{x0.x, x0.y, x1.x, x1.y};
    }
  }
  bar();
  STAMP(9);
  STAMP3(5);
  if (w == 0 || (w >= 3 && w < 7)) {
    const LAS unsigned char* bp = L + L_OS + (lane & 7) * 272 + g * 16;
    v4u bb[4];
#pragma unroll
    for (int s = 0; s < 4; ++s) bb[s] = *(const LAS v4u*)(bp + s * 64);
    SCHED();
    f4 y0 = mfma32(af[0], bb[0], f4{0, 0, 0, 0}), y1 = mfma32(af[1], bb[1], f4{0, 0, 0, 0});
    y0 = mfma32(af[2], bb[2], y0); y1 = mfma32(af[3], bb[3], y1);
    if (i16 < 8) st_sc(tg4(y0 + y1, t10), rsws, o1);
    if (w == 0 || w == 3) STAMP3(6);
  }
  v4u a4[5], a5[4];
  unsigned o5 = WS_T5P + (unsigned)((mm * 8 + (lane & 7)) * 640 + 320 * hf + 64 * w + 16 * (i16 >> 3) + 4 * g) * 4;
  asm volatile("" : "+v"(o5));
  if (MLPLATE == 1 && is_mlp && w == 7) {
#pragma unroll
    for (int n = MPRE + MP0; n < 60; ++n) { VMWAIT(MPACE); mlp_piece(n); }
    VMWAIT(0);
    if (lane == 0) flags[7] = 1u;
  }
  auto preload_mlp = [&]() {
    if (MLPLATE == 1) lds_wait(7, 1u);
    const LAS unsigned char* ap = L + L_RT + (160 * (w >> 1) + 8 * g + tq) * 64 + (w & 1) * 32 + tp * 8;
#pragma unroll
    for (int s = 0; s < 5; ++s) {
      const v2u x0 = trread(ap + s * 2048), x1 = trread(ap + s * 2048 + 256);
      a4[s] = v4u{x0.x, x0.y, x1.x, x1.y};
    }
    const int r0 = 8 * g + tq;
    const LAS unsigned char* wp = L + L_W2 + r0 * 640 + (tp & 1) * 8;
    const int f = 2 * (r0 & 3);
#pragma unroll
    for (int tt = 0; tt < 4; ++tt) {
      const int ch = (2 * (4 * (w < 5 ? w : 0) + tt) + (tp >> 1)) ^ f;
      const v2u x0 = trread(wp + ch * 16), x1 = trread(wp + 4 * 640 + ch * 16);
      a5[tt] = v4u{x0.x, x0.y, x1.x, x1.y};
    }
  };
  if (is_mlp && !(w >= 4 && w < 7 && MLPLATE == 1)) preload_mlp();
  if (w >= 4 && w < 7) {
    const unsigned tB2 = (unsigned)__builtin_amdgcn_s_memrealtime();
    const int c = 2 * (w - 4) + (lane >> 5), pr = lane & 31;
    const bool act = c < 5;
    const unsigned o = WS_T1P + (unsigned)(((b * 32 + h) * 32 + pr) * 5 + (act ? c : 0)) * 16;
    const LAS unsigned* vec = (const LAS unsigned*)(L + L_VEC);
    const int cv = act ? c : 0;
    const unsigned g0 = vec[2 * cv], g1 = vec[2 * cv + 1], x0 = vec[20 + 2 * cv], x1 = vec[20 + 2 * cv + 1];
    until(tB2, GB);
    STAMP3(6);
    v4u v;
    for (unsigned spins = 0;; ++spins) {
      CFENCE();
      v = ld_sc(rsws, o);
      if (__all(tagged4(v, t10)) || spins > SPIN_G) { COUNT3(8, spins + 1); STAMP3(7); break; }
      __builtin_amdgcn_s_sleep(TS);
    }
    f4 sm;
#pragma unroll
    for (int k = 0; k < 4; ++k) sm[k] = x16_sum(row16_sum(untg(v[k])));
    const float r0 = rbf(blo(x0) + rbf(blo(g0) * rbf(sm[0]))), r1 = rbf(bhi(x0) + rbf(bhi(g0) * rbf(sm[1])));
    const float r2 = rbf(blo(x1) + rbf(blo(g1) * rbf(sm[2]))), r3 = rbf(bhi(x1) + rbf(bhi(g1) * rbf(sm[3])));
    if (pr == 0 && act) {
      st_sc2(v2u{canonu(pk2(r0, r1)), canonu(pk2(r2, r3))}, rsws, WS_X1B + (unsigned)(b * 320 + 10 * h + 2 * c) * 4);
      *(LAS f4*)(L + L_X1O + c * 16) = f4{r0, r1, r2, r3};
    }
    asm volatile("s_waitcnt lgkmcnt(0)" ::: "memory");
    if (lane == 0) __hip_atomic_fetch_add((LAS unsigned*)(L + L_FLAG) + 6, 1u, __ATOMIC_RELAXED, __HIP_MEMORY_SCOPE_WORKGROUP);
    if (is_mlp && MLPLATE == 1) preload_mlp();
  }
  lds_wait(6, 3u);
  const unsigned tB = (unsigned)__builtin_amdgcn_s_memrealtime();
  STAMP(10);
  STAMP3(10);
  if (is_mlp) {
    {
      const unsigned o = WS_X1B + (unsigned)w * 1280 + lane * 4;
      float cs[10], sh[10];
#pragma unroll
      for (int u = 0; u < 5; ++u) {
        const unsigned c = *(const LAS unsigned*)(L + L_CS + (w * 320 + u * 64 + lane) * 4);
        const unsigned d = *(const LAS unsigned*)(L + L_CS + 10240 + (w * 320 + u * 64 + lane) * 4);
        cs[2 * u] = blo(c); cs[2 * u + 1] = bhi(c); sh[2 * u] = blo(d); sh[2 * u + 1] = bhi(d);
        asm volatile("" : "+v"(cs[2 * u]), "+v"(cs[2 * u + 1]), "+v"(sh[2 * u]), "+v"(sh[2 * u + 1]));
      }
      unsigned oh2 = (unsigned)(w * 1296 + lane * 4);
      asm volatile("" : "+v"(oh2));
      until(tB, GC);
      STAMP3(2);
      unsigned xv[5];
      for (unsigned spins = 0;; ++spins) {
        CFENCE();
        bool ok = true;
#pragma unroll
        for (int u = 0; u < 5; ++u) xv[u] = __builtin_amdgcn_raw_buffer_load_b32(rsws, o + u * 256, 0, 16);
#pragma unroll
        for (int u = 0; u < 5; ++u) ok = ok && xv[u] != SENT;
        if (__all(ok) || spins > SPIN_G) { COUNT3(4, spins + 1); STAMP3(3); break; }
        __builtin_amdgcn_s_sleep(TS);
      }
      const float x00 = blo(rfl(xv[0]));
      float s1 = 0.f, s2 = 0.f;
#pragma unroll
      for (int u = 0; u < 5; ++u) {
        const float d0 = blo(xv[u]) - x00, d1 = bhi(xv[u]) - x00;
        s1 += d0 + d1; s2 = fmaf(d0, d0, fmaf(d1, d1, s2));
      }
      s1 = row16_sum(s1); s2 = row16_sum(s2);
      s1 = xrow_sum(s1) * (1.f / DM); s2 = xrow_sum(s2) * (1.f / DM);
      const float rs = __builtin_amdgcn_rsqf(fmaxf(s2 - s1 * s1, 0.f) + 1e-5f), mr = -(x00 + s1) * rs;
#pragma unroll
      for (int u = 0; u < 5; ++u) {
        const float n0 = fmaf(blo(xv[u]), rs, mr), n1 = fmaf(bhi(xv[u]), rs, mr);
        *(LAS unsigned*)(L + L_H2 + oh2 + u * 256) = pk2(fmaf(n0, cs[2 * u], sh[2 * u]), fmaf(n1, cs[2 * u + 1], sh[2 * u + 1]));
      }
    }
    STAMP3(11);
    bar();
    STAMP(11);
    {
      const LAS unsigned char* bp = L + L_H2 + (lane & 7) * 1296 + (160 * (w >> 1) + 8 * g) * 2;
      v4u bb[5];
#pragma unroll
      for (int s = 0; s < 5; ++s) bb[s] = *(const LAS v4u*)(bp + s * 64);
      SCHED();
      f4 a = mfma32(a4[0], bb[0], f4{0, 0, 0, 0}), a2 = mfma32(a4[1], bb[1], f4{0, 0, 0, 0});
      a = mfma32(a4[2], bb[2], a); a2 = mfma32(a4[3], bb[3], a2); a = mfma32(a4[4], bb[4], a);
      *(LAS f4*)(L + L_T4P + ((w * 4 + g) * 8 + (lane & 7)) * 16) = a + a2;
    }
    bar();
    if (tid < 128) {
      const int j = tid >> 4, c = 2 * (tid & 15), t = c >> 4, gg = (c & 15) >> 2, r = c & 3;
      const LAS float* z = (const LAS float*)(L + L_T4P + ((t * 4 + gg) * 8 + j) * 16) + r;
      const float z0 = (z[0] + z[256]) + (z[512] + z[768]), z1 = (z[1] + z[257]) + (z[513] + z[769]);
      *(LAS unsigned*)(L + L_H1 + j * 80 + c * 2) = pk2(silu(rbf(z0)), silu(rbf(z1)));
    }
    bar();
    STAMP(12);
    if (w < 5) {
      const v4u bb = *(const LAS v4u*)(L + L_H1 + (lane & 7) * 80 + g * 16);
      f4 y[4];
#pragma unroll
      for (int tt = 0; tt < 4; ++tt) y[tt] = mfma32(a5[tt], bb, f4{0, 0, 0, 0});
      const bool up = i16 >= 8;
      st_sc(tg4(up ? y[1] : y[0], t10), rsws, o5);
      st_sc(tg4(up ? y[3] : y[2], t10), rsws, o5 + 128);
    }
    STAMP3(12);
  }
  const unsigned tD = (unsigned)__builtin_amdgcn_s_memrealtime();
  STAMP(13);
  STAMP3(13);
  if (w < 5) {
    const bool act = lane < NMLP;
    const unsigned o = WS_T5P + (unsigned)(((act ? lane : 0) * 8 + b) * 640 + 20 * h + 4 * w) * 4;
    const LAS unsigned* vec = (const LAS unsigned*)(L + L_VEC);
    const unsigned g0 = vec[10 + 2 * w], g1 = vec[10 + 2 * w + 1];
    const f4 x1 = *(const LAS f4*)(L + L_X1O + w * 16);
    if (is_mlp) until(tD, GD1); else until(tB, GD2);
    if (w == 3) STAMP3(6);
    v4u v;
    for (unsigned spins = 0;; ++spins) {
      CFENCE();
      v = ld_sc(rsws, o);
      if (__all(tagged4(v, t10)) || spins > SPIN_G) { if (w == 3) { COUNT3(8, spins + 1); STAMP3(7); } break; }
      __builtin_amdgcn_s_sleep(TS);
    }
    f4 sm;
#pragma unroll
    for (int k = 0; k < 4; ++k) sm[k] = wave_sum(act ? untg(v[k]) : 0.f);
    if (lane == 0) {
      const float r0 = x1[0] + rbf(blo(g0) * rbf(sm[0])), r1 = x1[1] + rbf(bhi(g0) * rbf(sm[1]));
      const float r2 = x1[2] + rbf(blo(g1) * rbf(sm[2])), r3 = x1[3] + rbf(bhi(g1) * rbf(sm[3]));
      *(GAS v2u*)(p.out + b * DM + 20 * h + 4 * w) = v2u{pk2(r0, r1), pk2(r2, r3)};
    }
  }
  if (id == 0 && tid == 448) {
    const unsigned E = __builtin_amdgcn_raw_buffer_load_b32(rsws, WS_EPOCH, 0, 16);
    __builtin_amdgcn_raw_buffer_store_b32(E + 1u, rsws, WS_EPOCH, 0, 16);
  }
  STAMP(15);
}

}

extern "C" void kernel_launch(void* const* d_in, const int* in_sizes, int n_in, void* d_out, int out_size, void* d_ws,
                              size_t ws_size, hipStream_t stream) {
  Params p;
  p.x = (const u16*)d_in[0];
  p.kc = (const u16*)d_in[1];
  p.vc = (const u16*)d_in[2];
  p.wq = (const u16*)d_in[3];
  p.wo = (const u16*)d_in[4];
  p.w1 = (const u16*)d_in[6];
  p.w2 = (const u16*)d_in[7];
  p.c = (const u16*)d_in[8];
  p.wmod = (const u16*)d_in[9];
  p.out = (u16*)d_out;
  p.ws = (unsigned char*)d_ws;
  hipLaunchKernelGGL(fused_block, dim3(256), dim3(512), 0, stream, p);
}
```

```cpp
#include <hip/hip_runtime.h>
#include <stdint.h>

namespace {

typedef unsigned short u16;
typedef unsigned v4u __attribute__((ext_vector_type(4)));
typedef unsigned v2u __attribute__((ext_vector_type(2)));
typedef float f4 __attribute__((ext_vector_type(4)));
typedef short s4 __attribute__((ext_vector_type(4)));
typedef short s8 __attribute__((ext_vector_type(8)));
typedef __bf16 bf2 __attribute__((ext_vector_type(2)));
typedef __bf16 bf8 __attribute__((ext_vector_type(8)));
typedef __amdgpu_buffer_rsrc_t rsrc_t;
#define LAS __attribute__((address_space(3)))
#define GAS __attribute__((address_space(1)))
#define DI __device__ __forceinline__

constexpr int NB = 8, DM = 640, NH = 32, DH = 128, SK = 4096, WIN = 1024, FF = 1728;
constexpr int NSW = 7;
constexpr int NL = 37;
constexpr int PACE = 3;
constexpr int NPRE = 16;
constexpr int HN = 45;
constexpr int LEAD = 3;
constexpr int CPACE = 14;
constexpr unsigned D1 = 600;
constexpr unsigned D2 = 100;
constexpr unsigned GB = 25;
constexpr unsigned GC = 30;
constexpr unsigned GD1 = 40;
constexpr unsigned GD2 = 200;

constexpr int NMLP = 54;
constexpr unsigned WS_EPOCH = 0;
constexpr unsigned WS_INIT = 4096;
constexpr unsigned WS_MODG = 8192;
constexpr unsigned WS_MODD = WS_MODG + NB * 1920 * 8;
constexpr unsigned WS_QP = WS_MODD + NB * 640 * 4;
constexpr unsigned WS_OX = WS_QP + NH * 8 * 8 * 128 * 4;
constexpr unsigned WS_T1P = WS_OX + NH * 8 * 256;
constexpr unsigned WS_X1B = WS_T1P + NB * NH * DM * 4;
constexpr unsigned WS_T5P = WS_X1B + NB * 320 * 4;
constexpr unsigned WS_END = WS_T5P + NMLP * NB * DM * 4;
constexpr unsigned SENT = 0xFFFFFFFFu;

constexpr unsigned L_RT = 0;
constexpr unsigned L_QT = 40960;
constexpr unsigned L_W2 = 61440;
constexpr unsigned L_T1 = 102400;
constexpr unsigned L_CS = 122880;
constexpr unsigned L_MISC = 143360;
constexpr unsigned L_FLAG = L_MISC;
constexpr unsigned L_Q = L_MISC + 64;
constexpr unsigned L_STAT = L_Q + 256;
constexpr unsigned L_VEC = L_STAT + 64;
constexpr unsigned L_X1O = L_VEC + 128;
constexpr unsigned L_SC = L_X1O + 128;
constexpr unsigned L_H2 = L_SC;
constexpr unsigned L_HS = L_SC + 8 * 1296;
constexpr unsigned L_MRG = L_HS + 8 * 176;
constexpr unsigned L_OS = L_MRG + 7 * 132 * 4;
constexpr unsigned L_T4P = L_MRG;
constexpr unsigned L_H1 = L_T4P + 4096;
constexpr unsigned L_END = 163840;
static_assert(L_OS + 8 * 272 <= L_END && L_H1 + 8 * 80 <= L_OS + 8 * 272, "LDS map");
static_assert((L_SC % 16) == 0 && (L_HS % 16) == 0 && (L_MRG % 16) == 0 && (L_OS % 16) == 0 && (L_H1 % 16) == 0, "LDS alignment");

constexpr unsigned SPIN_G = 1u << 16;
constexpr unsigned SPIN_L = 1u << 22;

struct Params {
  const u16 *x, *kc, *vc, *wq, *wo, *w1, *w2, *c, *wmod;
  u16* out;
  unsigned char* ws;
};

DI unsigned rfl(unsigned x) { return __builtin_amdgcn_readfirstlane(x); }
DI float asf(unsigned u) { return __uint_as_float(u); }
DI unsigned asu(float f) { return __float_as_uint(f); }
DI unsigned pk2(float lo, float hi) { bf2 v = {(__bf16)lo, (__bf16)hi}; return __builtin_bit_cast(unsigned, v); }
DI float rbf(float f) { return asf(pk2(f, 0.f) << 16); }
DI float blo(unsigned u) { return asf(u << 16); }
DI float bhi(unsigned u) { return asf(u & 0xffff0000u); }
DI float dot2(unsigned a, unsigned b, float c) {
  return __builtin_amdgcn_fdot2_f32_bf16(__builtin_bit_cast(bf2, a), __builtin_bit_cast(bf2, b), c, false);
}
template <int CTRL> DI float dpp(float x) {
  return __builtin_bit_cast(float, __builtin_amdgcn_mov_dpp(__builtin_bit_cast(int, x), CTRL, 0xf, 0xf, true));
}
DI float row16_sum(float x) { x += dpp<0x128>(x); x += dpp<0x124>(x); x += dpp<0x122>(x); x += dpp<0x121>(x); return x; }
DI float oct_sum(float x) { x += dpp<0xB1>(x); x += dpp<0x4E>(x); x += dpp<0x141>(x); return x; }
DI float x16_sum(float x) {
  auto s = __builtin_amdgcn_permlane16_swap(asu(x), asu(x), false, false);
  return asf(s[0]) + asf(s[1]);
}
DI float x32_sum(float x) {
  auto t = __builtin_amdgcn_permlane32_swap(asu(x), asu(x), false, false);
  return asf(t[0]) + asf(t[1]);
}
DI float xrow_sum(float x) { return x32_sum(x16_sum(x)); }
DI float xrow_max(float x) {
  auto s = __builtin_amdgcn_permlane16_swap(asu(x), asu(x), false, false);
  x = fmaxf(asf(s[0]), asf(s[1]));
  auto t = __builtin_amdgcn_permlane32_swap(asu(x), asu(x), false, false);
  return fmaxf(asf(t[0]), asf(t[1]));
}
DI float wave_sum(float x) { return xrow_sum(row16_sum(x)); }
DI f4 mfma32(v4u a, v4u b, f4 c) {
  return __builtin_amdgcn_mfma_f32_16x16x32_bf16(__builtin_bit_cast(bf8, a), __builtin_bit_cast(bf8, b), c, 0, 0, 0);
}
DI f4 mfma16(v2u a, v2u b, f4 c) {
  return __builtin_amdgcn_mfma_f32_16x16x16bf16_1k(__builtin_bit_cast(s4, a), __builtin_bit_cast(s4, b), c, 0, 0, 0);
}
DI v2u trread(const LAS unsigned char* p) {
  return __builtin_bit_cast(v2u, __builtin_amdgcn_ds_read_tr16_b64_v4i16((LAS s4*)p));
}
DI rsrc_t mkrs(const void* p, unsigned bytes) { return __builtin_amdgcn_make_buffer_rsrc((void*)p, 0, (int)bytes, 0x00020000); }
DI v4u ld_nt(rsrc_t r, unsigned vo, unsigned so) { return __builtin_amdgcn_raw_buffer_load_b128(r, vo, so, 2); }
DI v4u ld_sc(rsrc_t r, unsigned vo) { return __builtin_amdgcn_raw_buffer_load_b128(r, vo, 0, 16); }
DI v2u ld_sc2(rsrc_t r, unsigned vo) { return __builtin_amdgcn_raw_buffer_load_b64(r, vo, 0, 16); }
DI void st_sc(v4u v, rsrc_t r, unsigned vo) { __builtin_amdgcn_raw_buffer_store_b128(v, r, vo, 0, 16); }
DI void st_sc2(v2u v, rsrc_t r, unsigned vo) { __builtin_amdgcn_raw_buffer_store_b64(v, r, vo, 0, 16); }
DI unsigned canonu(unsigned u) { return u == 0xFFFFFFFFu ? 0x7FFF7FFFu : u; }
DI void dma16(rsrc_t r, unsigned lds, unsigned vo, unsigned so) {
  unsigned keep;
  asm volatile("s_nop 4\n\ts_mov_b32 %0, m0\n\ts_mov_b32 m0, %1\n\ts_nop 0\n\tbuffer_load_dwordx4 %2, %3, %4 offen nt lds\n\ts_mov_b32 m0, %0"
               : "=&s"(keep) : "s"(lds), "v"(vo), "s"(r), "s"(so) : "memory");
}
template <int N> DI void vmwait() { asm volatile("s_waitcnt vmcnt(%0)" :: "n"(N) : "memory"); }
#define VMWAIT(n) vmwait<(n)>()
DI void bar() { asm volatile("s_waitcnt lgkmcnt(0)\n\ts_barrier" ::: "memory"); }
#define CFENCE() asm volatile("" ::: "memory")
#define SCHED() __builtin_amdgcn_sched_barrier(0)
DI unsigned tg(float f, unsigned t10) { return (asu(f) & ~0x3FFu) | t10; }
DI v4u tg4(f4 v, unsigned t10) { return v4u{tg(v[0], t10), tg(v[1], t10), tg(v[2], t10), tg(v[3], t10)}; }
DI bool tagged4(v4u v, unsigned t10) { return (((v.x ^ t10) | (v.y ^ t10) | (v.z ^ t10) | (v.w ^ t10)) & 0x3FFu) == 0u; }
DI float untg(unsigned u) { return asf(u & ~0x3FFu); }
DI bool valid4(v4u v) { return v.x != SENT && v.y != SENT && v.z != SENT && v.w != SENT; }
DI float rcp(float x) { return __builtin_amdgcn_rcpf(x); }
DI float silu(float v) { return v * rcp(1.f + __builtin_amdgcn_exp2f(v * -1.4426950408889634f)); }

__global__ void __launch_bounds__(512, 2) fused_block(Params p) {
  __shared__ __attribute__((aligned(16))) unsigned char smem[L_END];
  LAS unsigned char* L = (LAS unsigned char*)smem;
  const unsigned lds0 = (unsigned)(uintptr_t)L;
  const int tid = threadIdx.x, lane = tid & 63;
  const int w = (int)rfl(tid >> 6);
  const int id = blockIdx.x, xcd = id & 7, sl = id >> 3;
  const int h = xcd * 4 + (sl >> 3), b = sl & 7;
  const int mpr = (sl >> 1) * 8 + xcd;
  const bool has_mod = sl < 16 && mpr < 60;
  const bool crit = has_mod && mpr < 20;
  const int mcol0 = (2 * mpr + (sl & 1)) * 32;
  const rsrc_t rsk = mkrs(p.kc + ((size_t)(b * NH + h) * SK + (SK - WIN)) * DH, WIN * DH * 2);
  const unsigned vo = (unsigned)(lane * 16 + w * 1024);
  v4u kr[NL], cr[2];
  if (w < NSW) {
    if (!crit) {
#pragma unroll
      for (int i = 0; i < PACE; ++i) kr[i] = ld_nt(rsk, vo, i * 7168);
    }
    if (has_mod) {
      if (w < 5) {
        const GAS v4u* cp = (const GAS v4u*)p.c;
        cr[0] = cp[w * 128 + lane];
        cr[1] = cp[w * 128 + 64 + lane];
      }
      const rsrc_t rsrt = mkrs(p.wmod, DM * 6 * DM * 2);
      const unsigned rt_vo = (unsigned)((lane >> 2) * 7680 + mcol0 * 2 + (lane & 3) * 16);
      const unsigned l0 = (unsigned)(uintptr_t)(LAS unsigned char*)smem;
#pragma unroll
      for (int u = 0; u < 6; ++u) {
        const int m = w + NSW * u;
        if (m < 40) dma16(rsrt, l0 + L_RT + m * 1024, rt_vo, (unsigned)m * 16u * 7680u);
      }
    }
  }
  SCHED();
  const int g = lane >> 4, i16 = lane & 15;
  const int tq = i16 >> 2, tp = lane & 3;
  const rsrc_t rsws = mkrs(p.ws, WS_END);
  volatile LAS unsigned* flags = (volatile LAS unsigned*)(L + L_FLAG);
  const int quad = ((sl - 16) >> 2) * 8 + xcd;
  const bool is_mlp = sl >= 16 && quad < 27;
  const int mm = 2 * quad + (sl & 1), hf = (sl >> 1) & 1;
  const bool dense = has_mod && mpr >= 30 && mpr < 50;

  const rsrc_t rswo = mkrs(p.wo + (size_t)h * DH * DM, DH * DM * 2);
  unsigned wov[5], w2v[5], w1v;
  const unsigned tstart = (unsigned)__builtin_amdgcn_s_memrealtime();

  unsigned t10 = 0;
  if (w < NSW) {
    const rsrc_t rsv = mkrs(p.vc + ((size_t)(b * NH + h) * SK + (SK - WIN)) * DH, WIN * DH * 2);
    v4u vr[NL];
    if (w >= 4) __builtin_amdgcn_s_setprio(1);
    auto silu_c = [&]() {
      if (w < 5) {
#pragma unroll
        for (int u = 0; u < 2; ++u) {
          const int q = w * 128 + u * 64 + lane, j = q / 80, k8 = q - 80 * j;
          v4u o;
          o.x = pk2(silu(blo(cr[u].x)), silu(bhi(cr[u].x)));
          o.y = pk2(silu(blo(cr[u].y)), silu(bhi(cr[u].y)));
          o.z = pk2(silu(blo(cr[u].z)), silu(bhi(cr[u].z)));
          o.w = pk2(silu(blo(cr[u].w)), silu(bhi(cr[u].w)));
          *(LAS v4u*)(L + L_SC + j * 1296 + k8 * 16) = o;
        }
      }
    };
    bar();
    if (has_mod) {
      silu_c();
      VMWAIT(0);
      asm volatile("s_waitcnt lgkmcnt(0)" ::: "memory");
      if (lane == 0) __hip_atomic_fetch_add((LAS unsigned*)(L + L_FLAG), 1u, __ATOMIC_RELAXED, __HIP_MEMORY_SCOPE_WORKGROUP);
    }
    {
      const rsrc_t rsq = mkrs(p.wq, DM * NH * DH * 2);
      for (int n = w; n < 20; n += NSW) {
        const int rl = 4 * n + g;
        dma16(rsq, lds0 + L_QT + n * 1024, (unsigned)((80 * b + rl) * 8192 + h * 256 + ((i16 ^ (2 * (rl & 7))) * 16)), 0);
      }
    }
    if (crit) {
#pragma unroll
      for (int i = 0; i < PACE; ++i) kr[i] = ld_nt(rsk, vo, i * 7168);
    }
    SCHED();
#pragma unroll
    for (int i = PACE; i < NL + NPRE; ++i) {
      if (has_mod && i < HN) VMWAIT(PACE); else VMWAIT(PACE - 1);
      if (i < NL) kr[i] = ld_nt(rsk, vo, i * 7168); else vr[i - NL] = ld_nt(rsv, vo, (i - NL) * 7168);
      SCHED();
      if (i == PACE + 4) {
        asm volatile("s_waitcnt lgkmcnt(0)" ::: "memory");
        if (lane == 0) __hip_atomic_fetch_add((LAS unsigned*)(L + L_FLAG) + 2, 1u, __ATOMIC_RELAXED, __HIP_MEMORY_SCOPE_WORKGROUP);
      }
    }
    {
      unsigned spins = 0;
      while (flags[1] == 0u) { __builtin_amdgcn_s_sleep(1); if (++spins > SPIN_L) break; }
    }
    CFENCE();
    const v4u q4 = *(const LAS v4u*)(L + L_Q + i16 * 16);
    t10 = flags[4] & 0x3FFu;
    float s[NL];
#pragma unroll
    for (int i = 0; i < NL; ++i) {
      float d = dot2(kr[i].x, q4.x, 0.f);
      d = dot2(kr[i].y, q4.y, d); d = dot2(kr[i].z, q4.z, d); d = dot2(kr[i].w, q4.w, d);
      s[i] = row16_sum(d);
      if ((i & 3) == 3 && NPRE + (i >> 2) < NL) { SCHED(); VMWAIT(PACE - 1); vr[NPRE + (i >> 2)] = ld_nt(rsv, vo, (NPRE + (i >> 2)) * 7168); SCHED(); }
    }
    constexpr int NV1 = NPRE + (NL >> 2);
    if (w >= 4) s[NL - 1] = -INFINITY;
    float m = s[0];
#pragma unroll
    for (int i = 1; i < NL; ++i) m = fmaxf(m, s[i]);
    m = xrow_max(m);
    constexpr float C2 = 0.08838834764831845f * 1.4426950408889634f;
    const float mc = m * C2;
    unsigned pp[NL];
    float l = 0.f;
#pragma unroll
    for (int i = 0; i < NL; ++i) {
      pp[i] = pk2(0.f, __builtin_amdgcn_exp2f(fmaf(s[i], C2, -mc)));
      l += asf(pp[i]);
    }
    l = xrow_sum(l);
    float acc[8];
#pragma unroll
    for (int k = 0; k < 8; ++k) acc[k] = 0.f;
#pragma unroll
    for (int i = 0; i < NL; ++i) {
      const unsigned p1 = pp[i], p0 = p1 >> 16;
      acc[0] = dot2(vr[i].x, p0, acc[0]); acc[1] = dot2(vr[i].x, p1, acc[1]);
      acc[2] = dot2(vr[i].y, p0, acc[2]); acc[3] = dot2(vr[i].y, p1, acc[3]);
      acc[4] = dot2(vr[i].z, p0, acc[4]); acc[5] = dot2(vr[i].z, p1, acc[5]);
      acc[6] = dot2(vr[i].w, p0, acc[6]); acc[7] = dot2(vr[i].w, p1, acc[7]);
      SCHED();
      if (NV1 + i < NL) { VMWAIT(PACE - 1); vr[NV1 + i] = ld_nt(rsv, vo, (NV1 + i) * 7168); SCHED(); }
      if (NV1 + i == NL - 1 - LEAD) {
        if (lane == 0) __hip_atomic_fetch_add((LAS unsigned*)(L + L_FLAG) + 3, 1u, __ATOMIC_RELAXED, __HIP_MEMORY_SCOPE_WORKGROUP);
      }
    }
#pragma unroll
    for (int k = 0; k < 8; ++k) acc[k] = xrow_sum(acc[k]);
    if (g == 0) {
      LAS float* mr = (LAS float*)(L + L_MRG) + w * 132;
      *(LAS f4*)(mr + 4 + 8 * i16) = f4{acc[0], acc[1], acc[2], acc[3]};
      *(LAS f4*)(mr + 8 + 8 * i16) = f4{acc[4], acc[5], acc[6], acc[7]};
      if (i16 == 0) { mr[0] = m; mr[1] = l; }
    }
    asm volatile("s_waitcnt lgkmcnt(0)" ::: "memory");
    unsigned arrived = 0;
    if (lane == 0) arrived = __hip_atomic_fetch_add((LAS unsigned*)(L + L_FLAG) + 8, 1u, __ATOMIC_RELAXED, __HIP_MEMORY_SCOPE_WORKGROUP);
    if (rfl(arrived) == (unsigned)(NSW - 1)) {
      CFENCE();
      if (lane < 32) {
        const LAS float* mr = (const LAS float*)(L + L_MRG);
        float mw[NSW], lw[NSW];
        f4 aw[NSW];
#pragma unroll
        for (int u = 0; u < NSW; ++u) { mw[u] = mr[u * 132]; lw[u] = mr[u * 132 + 1]; aw[u] = *(const LAS f4*)(mr + u * 132 + 4 + 4 * lane); }
        const float M = fmaxf(fmaxf(fmaxf(mw[0], mw[1]), fmaxf(mw[2], mw[3])), fmaxf(fmaxf(mw[4], mw[5]), mw[6]));
        float Lsum = 0.f;
        f4 o = {0, 0, 0, 0};
#pragma unroll
        for (int u = 0; u < NSW; ++u) {
          const float f = __builtin_amdgcn_exp2f((mw[u] - M) * C2);
          Lsum = fmaf(f, lw[u], Lsum);
          o += f * aw[u];
        }
        const float inv = rcp(Lsum);
        st_sc2(v2u{canonu(pk2(o[0] * inv, o[1] * inv)), canonu(pk2(o[2] * inv, o[3] * inv))}, rsws, WS_OX + (unsigned)(h * 8 + b) * 256 + lane * 8);
      }
    }
  } else {
    __builtin_amdgcn_s_setprio(3);
    if (lane == 0) { flags[0] = 0u; flags[1] = 0u; flags[2] = 0u; flags[3] = 0u; flags[5] = 0u; flags[6] = 0u; flags[8] = 0u; }
    const unsigned E = __builtin_amdgcn_raw_buffer_load_b32(rsws, WS_EPOCH, 0, 16);
    unsigned warm = 0;
    {
      const int n = sl * 64 + lane, pi = n / 640, row = n - 640 * pi, pr = xcd + 8 * pi;
      if (pr < 20) warm = __builtin_amdgcn_raw_buffer_load_b32(mkrs(p.wmod, DM * 6 * DM * 2), (unsigned)(row * 7680 + pr * 128), 0, 2);
    }
    const GAS v4u* xp = (const GAS v4u*)p.x;
    v4u xr[10];
    const int sj = lane >> 3, sub = lane & 7;
#pragma unroll
    for (int u = 0; u < 10; ++u) xr[u] = xp[sj * 80 + sub + 8 * u];
    SCHED();
    bar();
    unsigned xh[5];
#pragma unroll
    for (int u = 0; u < 5; ++u) { const int idx = u * 64 + lane, j = idx / 40, m = idx - 40 * j; xh[u] = ((const GAS unsigned*)p.x)[j * 320 + 40 * b + m]; }
    {
      const v4u sv = {SENT, SENT, SENT, SENT};
      if (lane < 16) st_sc(sv, rsws, WS_OX + (unsigned)(h * 8 + b) * 256 + lane * 16);
      if (lane < 5) st_sc2(v2u{SENT, SENT}, rsws, WS_X1B + (unsigned)(b * 320 + 10 * h) * 4 + lane * 8);
      if (dense && lane < 32) st_sc(sv, rsws, WS_MODD + (unsigned)((lane >> 2) * 640 + (mcol0 - 1920) / 2 + (lane & 3) * 4) * 4);
    }
    {
      float sm = 0.f;
#pragma unroll
      for (int u = 0; u < 10; ++u)
        sm += ((blo(xr[u].x) + bhi(xr[u].x)) + (blo(xr[u].y) + bhi(xr[u].y))) + ((blo(xr[u].z) + bhi(xr[u].z)) + (blo(xr[u].w) + bhi(xr[u].w)));
      const float mean = oct_sum(sm) * (1.f / DM);
      float sq = 0.f;
#pragma unroll
      for (int u = 0; u < 10; ++u) {
#pragma unroll
        for (int k = 0; k < 4; ++k) { const float a = blo(xr[u][k]) - mean, c2 = bhi(xr[u][k]) - mean; sq = fmaf(a, a, sq); sq = fmaf(c2, c2, sq); }
      }
      const float var = oct_sum(sq) * (1.f / DM);
      if (sub == 0) {
        LAS float* st = (LAS float*)(L + L_STAT) + sj * 2;
        st[0] = rbf(mean);
        st[1] = rcp(rbf(__builtin_amdgcn_sqrtf(rbf(rbf(var) + 1e-5f))));
      }
    }
    VMWAIT(0);
    asm volatile("" :: "v"(warm));
    const unsigned T = rfl(E) + 1u;
    t10 = T & 0x3FFu;
    if (lane == 0) flags[4] = T;
    if (lane == 0) st_sc2(v2u{T, ~T}, rsws, WS_INIT + (unsigned)(h * 8 + b) * 8);
    if (has_mod) {
      unsigned spins = 0;
      while (flags[0] < (unsigned)NSW) { __builtin_amdgcn_s_sleep(1); if (++spins > SPIN_L) break; }
      CFENCE();
      f4 a0 = {0, 0, 0, 0}, a1 = {0, 0, 0, 0};
      const LAS unsigned char* ap = L + L_RT + (8 * g + tq) * 64 + tp * 8;
      const LAS unsigned char* bp = L + L_SC + (lane & 7) * 1296 + g * 16;
#pragma unroll 2
      for (int s = 0; s < 20; ++s) {
        const v4u bb = *(const LAS v4u*)(bp + s * 64);
        const v2u x0 = trread(ap + s * 2048), x1 = trread(ap + s * 2048 + 256);
        const v2u y0 = trread(ap + s * 2048 + 32), y1 = trread(ap + s * 2048 + 256 + 32);
        a0 = mfma32(v4u{x0.x, x0.y, x1.x, x1.y}, bb, a0);
        a1 = mfma32(v4u{y0.x, y0.y, y1.x, y1.y}, bb, a1);
      }
      if (i16 < 8) {
        if (dense) {
          const unsigned o = WS_MODD + (unsigned)(i16 * 640 + (mcol0 - 1920 + 4 * g) / 2) * 4;
          st_sc2(v2u{canonu(pk2(a0[0], a0[1])), canonu(pk2(a0[2], a0[3]))}, rsws, o);
          st_sc2(v2u{canonu(pk2(a1[0], a1[1])), canonu(pk2(a1[2], a1[3]))}, rsws, o + 32);
        } else {
          const unsigned o = WS_MODG + (unsigned)(i16 * 1920 + (mcol0 + 4 * g) / 2) * 8;
          st_sc(v4u{pk2(a0[0], a0[1]), T, pk2(a0[2], a0[3]), T}, rsws, o);
          st_sc(v4u{pk2(a1[0], a1[1]), T, pk2(a1[2], a1[3]), T}, rsws, o + 64);
        }
      }
    }
    {
      v2u sa[5], ca[5], fl;
      { while ((unsigned)__builtin_amdgcn_s_memrealtime() - tstart < D1) __builtin_amdgcn_s_sleep(2); }
      for (unsigned spins = 0;; ++spins) {
        CFENCE();
        bool ok = true;
#pragma unroll
        for (int u = 0; u < 5; ++u) {
          const int idx = u * 64 + lane, j = idx / 40, m = idx - 40 * j;
          const unsigned o = WS_MODG + (unsigned)(j * 1920 + 40 * b + m) * 8;
          sa[u] = ld_sc2(rsws, o);
          ca[u] = ld_sc2(rsws, o + 320 * 8);
        }
        fl = ld_sc2(rsws, WS_INIT + (unsigned)(h * 8 + (lane & 7)) * 8);
#pragma unroll
        for (int u = 0; u < 5; ++u) ok = ok && sa[u].y == T && ca[u].y == T;
        ok = ok && fl.x == T && fl.y == ~T;
        if (__all(ok) || spins > SPIN_G) break;
        __builtin_amdgcn_s_sleep(32);
      }
      asm volatile("s_waitcnt lgkmcnt(0)" ::: "memory");
#pragma unroll
      for (int u = 0; u < 5; ++u) {
        const int idx = u * 64 + lane, j = idx / 40, m = idx - 40 * j;
        const float mean = ((LAS float*)(L + L_STAT))[2 * j], den = ((LAS float*)(L + L_STAT))[2 * j + 1];
        const float n0 = rbf(rbf(blo(xh[u]) - mean) * den), n1 = rbf(rbf(bhi(xh[u]) - mean) * den);
        const float h0 = rbf(n0 * rbf(1.f + blo(ca[u].x))) + blo(sa[u].x), h1 = rbf(n1 * rbf(1.f + bhi(ca[u].x))) + bhi(sa[u].x);
        *(LAS unsigned*)(L + L_HS + j * 176 + m * 4) = pk2(h0, h1);
      }
    }
    {
      unsigned spins = 0;
      while (flags[2] < (unsigned)NSW) { __builtin_amdgcn_s_sleep(1); if (++spins > SPIN_L) break; }
      CFENCE();
    }
    {
      f4 qa[8];
#pragma unroll
      for (int t = 0; t < 8; ++t) qa[t] = f4{0, 0, 0, 0};
#pragma unroll
      for (int s = 0; s < 5; ++s) {
        const v2u bb = *(const LAS v2u*)(L + L_HS + (lane & 7) * 176 + s * 32 + g * 8);
        const int row = 16 * s + 4 * g + tq;
        const LAS unsigned char* rp = L + L_QT + row * 256 + (tp & 1) * 8;
        const int sw = 2 * (row & 7);
#pragma unroll
        for (int t = 0; t < 8; ++t) qa[t] = mfma16(trread(rp + (((2 * t + (tp >> 1)) ^ sw) * 16)), bb, qa[t]);
      }
      if (i16 < 8) {
        const unsigned o = WS_QP + (unsigned)(((h * 8 + b) * 8 + i16) * 128 + 4 * g) * 4;
#pragma unroll
        for (int t = 0; t < 8; ++t) st_sc(tg4(qa[t], t10), rsws, o + t * 64);
      }
    }
    const unsigned tpub = (unsigned)__builtin_amdgcn_s_memrealtime();
    {
      v4u qv[4];
      { while ((unsigned)__builtin_amdgcn_s_memrealtime() - tpub < D2) __builtin_amdgcn_s_sleep(2); }
      for (unsigned spins = 0;; ++spins) {
        CFENCE();
        bool ok = true;
#pragma unroll
        for (int u = 0; u < 4; ++u) qv[u] = ld_sc(rsws, WS_QP + (unsigned)(((h * 8 + 2 * u + (lane >> 5)) * 8 + b) * 128 + 4 * (lane & 31)) * 4);
#pragma unroll
        for (int u = 0; u < 4; ++u) ok = ok && tagged4(qv[u], t10);
        if (__all(ok) || spins > SPIN_G) break;
        __builtin_amdgcn_s_sleep(32);
      }
      f4 qs;
#pragma unroll
      for (int k = 0; k < 4; ++k) qs[k] = x32_sum((untg(qv[0][k]) + untg(qv[1][k])) + (untg(qv[2][k]) + untg(qv[3][k])));
      if (lane < 32) *(LAS v2u*)(L + L_Q + lane * 8) = v2u{pk2(qs[0], qs[1]), pk2(qs[2], qs[3])};
      asm volatile("s_waitcnt lgkmcnt(0)" ::: "memory");
      if (lane == 0) flags[1] = 1u;
    }
    {
      const unsigned go = WS_MODG + (unsigned)(b * 1920 + (lane < 10 ? 640 + 10 * h + lane : 1600 + 10 * h + (lane < 20 ? lane - 10 : 0))) * 8;
      const unsigned xv = ((const GAS unsigned*)p.x)[b * 320 + 10 * h + (lane < 10 ? lane : 0)];
      v2u gv;
      for (unsigned spins = 0;; ++spins) {
        CFENCE();
        gv = ld_sc2(rsws, go);
        if (__all(gv.y == T) || spins > SPIN_G) break;
        __builtin_amdgcn_s_sleep(32);
      }
      if (lane < 20) *(LAS unsigned*)(L + L_VEC + lane * 4) = gv.x;
      if (lane < 10) *(LAS unsigned*)(L + L_VEC + 80 + lane * 4) = xv;
      if (is_mlp) {
        for (unsigned spins = 0;; ++spins) {
          CFENCE();
          bool ok = true;
#pragma unroll
          for (int u = 0; u < 4; ++u) { const v2u f = ld_sc2(rsws, WS_INIT + (unsigned)(u * 64 + lane) * 8); ok = ok && f.x == T && f.y == ~T; }
          if (__all(ok) || spins > SPIN_G) break;
          __builtin_amdgcn_s_sleep(32);
        }
        {
          v4u cv[20];
          for (unsigned spins = 0;; ++spins) {
            CFENCE();
            bool ok = true;
#pragma unroll
            for (int u = 0; u < 20; ++u) cv[u] = ld_sc(rsws, WS_MODD + (unsigned)(u * 64 + lane) * 16);
#pragma unroll
            for (int u = 0; u < 20; ++u) ok = ok && valid4(cv[u]);
            if (__all(ok) || spins > SPIN_G) break;
            __builtin_amdgcn_s_sleep(32);
          }
#pragma unroll
          for (int u = 0; u < 20; ++u) {
            const int pc = u * 64 + lane, j = pc / 160, r = pc - 160 * j;
            v4u o = cv[u];
            if (r >= 80) {
#pragma unroll
              for (int k = 0; k < 4; ++k) o[k] = pk2(1.f + blo(o[k]), 1.f + bhi(o[k]));
            }
            *(LAS v4u*)(L + L_CS + (r < 80 ? 10240 + (j * 320 + 4 * r) * 4 : (j * 320 + 4 * (r - 80)) * 4)) = o;
          }
        }
      }
    }
#pragma unroll
    for (int c = 0; c < 5; ++c) { const int ci = c * 64 + lane, row = ci / 10, ch = ci - 10 * row; wov[c] = (unsigned)(row * 1280 + b * 160 + ch * 16); asm volatile("" : "+v"(wov[c])); }
#pragma unroll
    for (int c = 0; c < 5; ++c) {
      const int ci = c * 64 + lane, row = ci / 40, pos = ci - 40 * row;
      w2v[c] = (unsigned)(row * 1280 + hf * 640 + (pos ^ (2 * (row & 3))) * 16); asm volatile("" : "+v"(w2v[c]));
    }
    w1v = (unsigned)((lane >> 2) * (FF * 2) + mm * 64 + (lane & 3) * 16); asm volatile("" : "+v"(w1v));
    if (is_mlp) {
      const rsrc_t rsw1t = mkrs(p.w1, DM * FF * 2);
      const rsrc_t rsw2t = mkrs(p.w2 + (size_t)mm * 32 * DM, 32 * DM * 2);
#pragma unroll
      for (int n = 0; n < 60; ++n) {
        if (n >= CPACE) VMWAIT(CPACE - 1);
        if (n < 40) dma16(rsw1t, lds0 + L_RT + n * 1024, w1v, (unsigned)n * 16u * (FF * 2));
        else dma16(rsw2t, lds0 + L_W2 + (n - 40) * 1024, w2v[(n - 40) % 5], (unsigned)((n - 40) / 5) * 8u * 1280u);
      }
    }
    {
      unsigned spins = 0;
      while (flags[3] < (unsigned)NSW) { __builtin_amdgcn_s_sleep(1); if (++spins > SPIN_L) break; }
      CFENCE();
#pragma unroll
      for (int n = 0; n < 20; ++n) dma16(rswo, lds0 + L_T1 + n * 1024, wov[n % 5], (unsigned)(n / 5) * 32u * 1280u);
    }
  }

  bar();
  auto until = [&](unsigned t0, unsigned ticks) { while ((unsigned)__builtin_amdgcn_s_memrealtime() - t0 < ticks) __builtin_amdgcn_s_sleep(1); };
  auto lds_wait = [&](int k, unsigned v) { unsigned spins = 0; while (flags[k] < v) { if (++spins > SPIN_L) break; } CFENCE(); };
  const int t1t = w == 0 ? 0 : w - 2;
  v4u af[4];
  unsigned o1;
  { const int cc = 4 * t1t + g; o1 = WS_T1P + (unsigned)((((i16 * 32 + 4 * b + cc / 5) * 32 + h) * 5 + cc % 5) * 16); asm volatile("" : "+v"(o1)); }
  if (w == 1 || w == 2) {
    const int t = tid - 64;
    __builtin_amdgcn_s_sleep(6);
    v4u v;
    for (unsigned spins = 0;; ++spins) {
      CFENCE();
      v = ld_sc(rsws, WS_OX + (unsigned)h * 2048 + t * 16);
      if (__all(valid4(v)) || spins > SPIN_G) break;
      __builtin_amdgcn_s_sleep(4);
    }
    *(LAS v4u*)(L + L_OS + (t >> 4) * 272 + (t & 15) * 16) = v;
  } else if (w == 7) {
    VMWAIT(0);
    if (lane == 0) flags[5] = 1u;
  } else {
    lds_wait(5, 1u);
    const LAS unsigned char* ap = L + L_T1 + (8 * g + tq) * 160 + (16 * t1t + 4 * tp) * 2;
#pragma unroll
    for (int s = 0; s < 4; ++s) {
      const v2u x0 = trread(ap + s * 32 * 160), x1 = trread(ap + s * 32 * 160 + 4 * 160);
      af[s] = v4u{x0.x, x0.y, x1.x, x1.y};
    }
  }
  bar();
  if (w == 0 || (w >= 3 && w < 7)) {
    const LAS unsigned char* bp = L + L_OS + (lane & 7) * 272 + g * 16;
    v4u bb[4];
#pragma unroll
    for (int s = 0; s < 4; ++s) bb[s] = *(const LAS v4u*)(bp + s * 64);
    SCHED();
    f4 y0 = mfma32(af[0], bb[0], f4{0, 0, 0, 0}), y1 = mfma32(af[1], bb[1], f4{0, 0, 0, 0});
    y0 = mfma32(af[2], bb[2], y0); y1 = mfma32(af[3], bb[3], y1);
    if (i16 < 8) st_sc(tg4(y0 + y1, t10), rsws, o1);
  }
  v4u a4[5], a5[4];
  unsigned o5 = WS_T5P + (unsigned)((mm * 8 + (lane & 7)) * 640 + 320 * hf + 64 * w + 16 * (i16 >> 3) + 4 * g) * 4;
  asm volatile("" : "+v"(o5));
  if (is_mlp) {
    const LAS unsigned char* ap = L + L_RT + (160 * (w >> 1) + 8 * g + tq) * 64 + (w & 1) * 32 + tp * 8;
#pragma unroll
    for (int s = 0; s < 5; ++s) {
      const v2u x0 = trread(ap + s * 2048), x1 = trread(ap + s * 2048 + 256);
      a4[s] = v4u{x0.x, x0.y, x1.x, x1.y};
    }
    const int r0 = 8 * g + tq;
    const LAS unsigned char* wp = L + L_W2 + r0 * 640 + (tp & 1) * 8;
    const int f = 2 * (r0 & 3);
#pragma unroll
    for (int tt = 0; tt < 4; ++tt) {
      const int ch = (2 * (4 * (w < 5 ? w : 0) + tt) + (tp >> 1)) ^ f;
      const v2u x0 = trread(wp + ch * 16), x1 = trread(wp + 4 * 640 + ch * 16);
      a5[tt] = v4u{x0.x, x0.y, x1.x, x1.y};
    }
  }
  if (w >= 4 && w < 7) {
    const unsigned tB2 = (unsigned)__builtin_amdgcn_s_memrealtime();
    const int c = 2 * (w - 4) + (lane >> 5), pr = lane & 31;
    const bool act = c < 5;
    const unsigned o = WS_T1P + (unsigned)(((b * 32 + h) * 32 + pr) * 5 + (act ? c : 0)) * 16;
    const LAS unsigned* vec = (const LAS unsigned*)(L + L_VEC);
    const int cv = act ? c : 0;
    const unsigned g0 = vec[2 * cv], g1 = vec[2 * cv + 1], x0 = vec[20 + 2 * cv], x1 = vec[20 + 2 * cv + 1];
    until(tB2, GB);
    v4u v;
    for (unsigned spins = 0;; ++spins) {
      CFENCE();
      v = ld_sc(rsws, o);
      if (__all(tagged4(v, t10)) || spins > SPIN_G) break;
      __builtin_amdgcn_s_sleep(4);
    }
    f4 sm;
#pragma unroll
    for (int k = 0; k < 4; ++k) sm[k] = x16_sum(row16_sum(untg(v[k])));
    const float r0 = rbf(blo(x0) + rbf(blo(g0) * rbf(sm[0]))), r1 = rbf(bhi(x0) + rbf(bhi(g0) * rbf(sm[1])));
    const float r2 = rbf(blo(x1) + rbf(blo(g1) * rbf(sm[2]))), r3 = rbf(bhi(x1) + rbf(bhi(g1) * rbf(sm[3])));
    if (pr == 0 && act) {
      st_sc2(v2u{canonu(pk2(r0, r1)), canonu(pk2(r2, r3))}, rsws, WS_X1B + (unsigned)(b * 320 + 10 * h + 2 * c) * 4);
      *(LAS f4*)(L + L_X1O + c * 16) = f4{r0, r1, r2, r3};
    }
    asm volatile("s_waitcnt lgkmcnt(0)" ::: "memory");
    if (lane == 0) __hip_atomic_fetch_add((LAS unsigned*)(L + L_FLAG) + 6, 1u, __ATOMIC_RELAXED, __HIP_MEMORY_SCOPE_WORKGROUP);
  }
  lds_wait(6, 3u);
  const unsigned tB = (unsigned)__builtin_amdgcn_s_memrealtime();
  if (is_mlp) {
    {
      const unsigned o = WS_X1B + (unsigned)w * 1280 + lane * 4;
      float cs[10], sh[10];
#pragma unroll
      for (int u = 0; u < 5; ++u) {
        const unsigned c = *(const LAS unsigned*)(L + L_CS + (w * 320 + u * 64 + lane) * 4);
        const unsigned d = *(const LAS unsigned*)(L + L_CS + 10240 + (w * 320 + u * 64 + lane) * 4);
        cs[2 * u] = blo(c); cs[2 * u + 1] = bhi(c); sh[2 * u] = blo(d); sh[2 * u + 1] = bhi(d);
        asm volatile("" : "+v"(cs[2 * u]), "+v"(cs[2 * u + 1]), "+v"(sh[2 * u]), "+v"(sh[2 * u + 1]));
      }
      unsigned oh2 = (unsigned)(w * 1296 + lane * 4);
      asm volatile("" : "+v"(oh2));
      until(tB, GC);
      unsigned xv[5];
      for (unsigned spins = 0;; ++spins) {
        CFENCE();
        bool ok = true;
#pragma unroll
        for (int u = 0; u < 5; ++u) xv[u] = __builtin_amdgcn_raw_buffer_load_b32(rsws, o + u * 256, 0, 16);
#pragma unroll
        for (int u = 0; u < 5; ++u) ok = ok && xv[u] != SENT;
        if (__all(ok) || spins > SPIN_G) break;
        __builtin_amdgcn_s_sleep(4);
      }
      const float x00 = blo(rfl(xv[0]));
      float s1 = 0.f, s2 = 0.f;
#pragma unroll
      for (int u = 0; u < 5; ++u) {
        const float d0 = blo(xv[u]) - x00, d1 = bhi(xv[u]) - x00;
        s1 += d0 + d1; s2 = fmaf(d0, d0, fmaf(d1, d1, s2));
      }
      s1 = row16_sum(s1); s2 = row16_sum(s2);
      s1 = xrow_sum(s1) * (1.f / DM); s2 = xrow_sum(s2) * (1.f / DM);
      const float rs = __builtin_amdgcn_rsqf(fmaxf(s2 - s1 * s1, 0.f) + 1e-5f), mr = -(x00 + s1) * rs;
#pragma unroll
      for (int u = 0; u < 5; ++u) {
        const float n0 = fmaf(blo(xv[u]), rs, mr), n1 = fmaf(bhi(xv[u]), rs, mr);
        *(LAS unsigned*)(L + L_H2 + oh2 + u * 256) = pk2(fmaf(n0, cs[2 * u], sh[2 * u]), fmaf(n1, cs[2 * u + 1], sh[2 * u + 1]));
      }
    }
    bar();
    {
      const LAS unsigned char* bp = L + L_H2 + (lane & 7) * 1296 + (160 * (w >> 1) + 8 * g) * 2;
      v4u bb[5];
#pragma unroll
      for (int s = 0; s < 5; ++s) bb[s] = *(const LAS v4u*)(bp + s * 64);
      SCHED();
      f4 a = mfma32(a4[0], bb[0], f4{0, 0, 0, 0}), a2 = mfma32(a4[1], bb[1], f4{0, 0, 0, 0});
      a = mfma32(a4[2], bb[2], a); a2 = mfma32(a4[3], bb[3], a2); a = mfma32(a4[4], bb[4], a);
      *(LAS f4*)(L + L_T4P + ((w * 4 + g) * 8 + (lane & 7)) * 16) = a + a2;
    }
    bar();
    if (tid < 128) {
      const int j = tid >> 4, c = 2 * (tid & 15), t = c >> 4, gg = (c & 15) >> 2, r = c & 3;
      const LAS float* z = (const LAS float*)(L + L_T4P + ((t * 4 + gg) * 8 + j) * 16) + r;
      const float z0 = (z[0] + z[256]) + (z[512] + z[768]), z1 = (z[1] + z[257]) + (z[513] + z[769]);
      *(LAS unsigned*)(L + L_H1 + j * 80 + c * 2) = pk2(silu(rbf(z0)), silu(rbf(z1)));
    }
    bar();
    if (w < 5) {
      const v4u bb = *(const LAS v4u*)(L + L_H1 + (lane & 7) * 80 + g * 16);
      f4 y[4];
#pragma unroll
      for (int tt = 0; tt < 4; ++tt) y[tt] = mfma32(a5[tt], bb, f4{0, 0, 0, 0});
      const bool up = i16 >= 8;
      st_sc(tg4(up ? y[1] : y[0], t10), rsws, o5);
      st_sc(tg4(up ? y[3] : y[2], t10), rsws, o5 + 128);
    }
  }
  const unsigned tD = (unsigned)__builtin_amdgcn_s_memrealtime();
  if (w < 5) {
    const bool act = lane < NMLP;
    const unsigned o = WS_T5P + (unsigned)(((act ? lane : 0) * 8 + b) * 640 + 20 * h + 4 * w) * 4;
    const LAS unsigned* vec = (const LAS unsigned*)(L + L_VEC);
    const unsigned g0 = vec[10 + 2 * w], g1 = vec[10 + 2 * w + 1];
    const f4 x1 = *(const LAS f4*)(L + L_X1O + w * 16);
    if (is_mlp) until(tD, GD1); else until(tB, GD2);
    v4u v;
    for (unsigned spins = 0;; ++spins) {
      CFENCE();
      v = ld_sc(rsws, o);
      if (__all(tagged4(v, t10)) || spins > SPIN_G) break;
      __builtin_amdgcn_s_sleep(4);
    }
    f4 sm;
#pragma unroll
    for (int k = 0; k < 4; ++k) sm[k] = wave_sum(act ? untg(v[k]) : 0.f);
    if (lane == 0) {
      const float r0 = x1[0] + rbf(blo(g0) * rbf(sm[0])), r1 = x1[1] + rbf(bhi(g0) * rbf(sm[1]));
      const float r2 = x1[2] + rbf(blo(g1) * rbf(sm[2])), r3 = x1[3] + rbf(bhi(g1) * rbf(sm[3]));
      *(GAS v2u*)(p.out + b * DM + 20 * h + 4 * w) = v2u{pk2(r0, r1), pk2(r2, r3)};
    }
  }
  if (id == 0 && tid == 448) {
    const unsigned E = __builtin_amdgcn_raw_buffer_load_b32(rsws, WS_EPOCH, 0, 16);
    __builtin_amdgcn_raw_buffer_store_b32(E + 1u, rsws, WS_EPOCH, 0, 16);
  }
}

}

extern "C" void kernel_launch(void* const* d_in, const int* in_sizes, int n_in, void* d_out, int out_size, void* d_ws,
                              size_t ws_size, hipStream_t stream) {
  Params p;
  p.x = (const u16*)d_in[0];
  p.kc = (const u16*)d_in[1];
  p.vc = (const u16*)d_in[2];
  p.wq = (const u16*)d_in[3];
  p.wo = (const u16*)d_in[4];
  p.w1 = (const u16*)d_in[6];
  p.w2 = (const u16*)d_in[7];
  p.c = (const u16*)d_in[8];
  p.wmod = (const u16*)d_in[9];
  p.out = (u16*)d_out;
  p.ws = (unsigned char*)d_ws;
  hipLaunchKernelGGL(fused_block, dim3(256), dim3(512), 0, stream, p);
}
```

```cpp
#include <hip/hip_runtime.h>
#include <stdint.h>

namespace {

typedef unsigned short u16;
typedef unsigned v4u __attribute__((ext_vector_type(4)));
typedef unsigned v2u __attribute__((ext_vector_type(2)));
typedef float f4 __attribute__((ext_vector_type(4)));
typedef short s4 __attribute__((ext_vector_type(4)));
typedef __bf16 bf2 __attribute__((ext_vector_type(2)));
typedef __bf16 bf8 __attribute__((ext_vector_type(8)));
typedef __amdgpu_buffer_rsrc_t rsrc_t;
#define LAS __attribute__((address_space(3)))
#define GAS __attribute__((address_space(1)))
#define DI __device__ __forceinline__

constexpr int NB = 8, DM = 640, NH = 32, DH = 128, SK = 4096, WIN = 1024, FF = 1728;
constexpr int NSW = 7;
constexpr int NL = 37;
constexpr int PACE = 3;
constexpr int NPRE = 16;
constexpr int HN = 45;
constexpr int LEAD = 3;
constexpr int CPACE = 14;
constexpr unsigned D1 = 600;
constexpr unsigned D2 = 100;
constexpr unsigned GB = 25;
constexpr unsigned GC = 30;
constexpr unsigned GD1 = 40;
constexpr unsigned GD2 = 200;

constexpr int NMLP = 54;
constexpr unsigned WS_EPOCH = 0;
constexpr unsigned WS_INIT = 4096;
constexpr unsigned WS_MODG = 8192;
constexpr unsigned WS_MODD = WS_MODG + NB * 1920 * 8;
constexpr unsigned WS_QP = WS_MODD + NB * 640 * 4;
constexpr unsigned WS_OX = WS_QP + NH * 8 * 8 * 128 * 4;
constexpr unsigned WS_T1P = WS_OX + NH * 8 * 256;
constexpr unsigned WS_X1B = WS_T1P + NB * NH * DM * 4;
constexpr unsigned WS_T5P = WS_X1B + NB * 320 * 4;
constexpr unsigned WS_END = WS_T5P + NMLP * NB * DM * 4;
constexpr unsigned SENT = 0xFFFFFFFFu;

constexpr unsigned L_RT = 0;
constexpr unsigned L_QT = 40960;
constexpr unsigned L_W2 = 61440;
constexpr unsigned L_T1 = 102400;
constexpr unsigned L_CS = 122880;
constexpr unsigned L_MISC = 143360;
constexpr unsigned L_FLAG = L_MISC;
constexpr unsigned L_Q = L_MISC + 64;
constexpr unsigned L_STAT = L_Q + 256;
constexpr unsigned L_VEC = L_STAT + 64;
constexpr unsigned L_X1O = L_VEC + 128;
constexpr unsigned L_SC = L_X1O + 128;
constexpr unsigned L_H2 = L_SC;
constexpr unsigned L_HS = L_SC + 8 * 1296;
constexpr unsigned L_MRG = L_HS + 8 * 176;
constexpr unsigned L_OS = L_MRG + 7 * 132 * 4;
constexpr unsigned L_T4P = L_MRG;
constexpr unsigned L_H1 = L_T4P + 4096;
constexpr unsigned L_END = 163840;
static_assert(L_OS + 8 * 272 <= L_END && L_H1 + 8 * 80 <= L_OS + 8 * 272, "LDS map");
static_assert((L_SC % 16) == 0 && (L_HS % 16) == 0 && (L_MRG % 16) == 0 && (L_OS % 16) == 0 && (L_H1 % 16) == 0, "LDS alignment");

constexpr unsigned SPIN_G = 1u << 16;
constexpr unsigned SPIN_L = 1u << 22;

struct Params {
  const u16 *x, *kc, *vc, *wq, *wo, *w1, *w2, *c, *wmod;
  u16* out;
  unsigned char* ws;
};

DI unsigned rfl(unsigned x) { return __builtin_amdgcn_readfirstlane(x); }
DI float asf(unsigned u) { return __uint_as_float(u); }
DI unsigned asu(float f) { return __float_as_uint(f); }
DI unsigned pk2(float lo, float hi) { bf2 v = {(__bf16)lo, (__bf16)hi}; return __builtin_bit_cast(unsigned, v); }
DI float rbf(float f) { return asf(pk2(f, 0.f) << 16); }
DI float blo(unsigned u) { return asf(u << 16); }
DI float bhi(unsigned u) { return asf(u & 0xffff0000u); }
DI float dot2(unsigned a, unsigned b, float c) {
  return __builtin_amdgcn_fdot2_f32_bf16(__builtin_bit_cast(bf2, a), __builtin_bit_cast(bf2, b), c, false);
}
template <int CTRL> DI float dpp(float x) {
  return __builtin_bit_cast(float, __builtin_amdgcn_mov_dpp(__builtin_bit_cast(int, x), CTRL, 0xf, 0xf, true));
}
DI float row16_sum(float x) { x += dpp<0x128>(x); x += dpp<0x124>(x); x += dpp<0x122>(x); x += dpp<0x121>(x); return x; }
DI float oct_sum(float x) { x += dpp<0xB1>(x); x += dpp<0x4E>(x); x += dpp<0x141>(x); return x; }
DI float x16_sum(float x) {
  auto s = __builtin_amdgcn_permlane16_swap(asu(x), asu(x), false, false);
  return asf(s[0]) + asf(s[1]);
}
DI float x32_sum(float x) {
  auto t = __builtin_amdgcn_permlane32_swap(asu(x), asu(x), false, false);
  return asf(t[0]) + asf(t[1]);
}
DI float xrow_sum(float x) { return x32_sum(x16_sum(x)); }
DI float xrow_max(float x) {
  auto s = __builtin_amdgcn_permlane16_swap(asu(x), asu(x), false, false);
  x = fmaxf(asf(s[0]), asf(s[1]));
  auto t = __builtin_amdgcn_permlane32_swap(asu(x), asu(x), false, false);
  return fmaxf(asf(t[0]), asf(t[1]));
}
DI float wave_sum(float x) { return xrow_sum(row16_sum(x)); }
DI f4 mfma32(v4u a, v4u b, f4 c) {
  return __builtin_amdgcn_mfma_f32_16x16x32_bf16(__builtin_bit_cast(bf8, a), __builtin_bit_cast(bf8, b), c, 0, 0, 0);
}
DI f4 mfma16(v2u a, v2u b, f4 c) {
  return __builtin_amdgcn_mfma_f32_16x16x16bf16_1k(__builtin_bit_cast(s4, a), __builtin_bit_cast(s4, b), c, 0, 0, 0);
}
DI v2u trread(const LAS unsigned char* p) {
  return __builtin_bit_cast(v2u, __builtin_amdgcn_ds_read_tr16_b64_v4i16((LAS s4*)p));
}
DI rsrc_t mkrs(const void* p, unsigned bytes) { return __builtin_amdgcn_make_buffer_rsrc((void*)p, 0, (int)bytes, 0x00020000); }
DI v4u ld_nt(rsrc_t r, unsigned vo, unsigned so) { return __builtin_amdgcn_raw_buffer_load_b128(r, vo, so, 2); }
DI v4u ld_sc(rsrc_t r, unsigned vo) { return __builtin_amdgcn_raw_buffer_load_b128(r, vo, 0, 16); }
DI v2u ld_sc2(rsrc_t r, unsigned vo) { return __builtin_amdgcn_raw_buffer_load_b64(r, vo, 0, 16); }
DI void st_sc(v4u v, rsrc_t r, unsigned vo) { __builtin_amdgcn_raw_buffer_store_b128(v, r, vo, 0, 16); }
DI void st_sc2(v2u v, rsrc_t r, unsigned vo) { __builtin_amdgcn_raw_buffer_store_b64(v, r, vo, 0, 16); }
DI unsigned canonu(unsigned u) { return u == 0xFFFFFFFFu ? 0x7FFF7FFFu : u; }
DI void dma16(rsrc_t r, unsigned lds, unsigned vo, unsigned so) {
  unsigned keep;
  asm volatile("s_nop 4\n\ts_mov_b32 %0, m0\n\ts_mov_b32 m0, %1\n\ts_nop 0\n\tbuffer_load_dwordx4 %2, %3, %4 offen nt lds\n\ts_mov_b32 m0, %0"
               : "=&s"(keep) : "s"(lds), "v"(vo), "s"(r), "s"(so) : "memory");
}
template <int N> DI void vmwait() { asm volatile("s_waitcnt vmcnt(%0)" :: "n"(N) : "memory"); }
#define VMWAIT(n) vmwait<(n)>()
DI void bar() { asm volatile("s_waitcnt lgkmcnt(0)\n\ts_barrier" ::: "memory"); }
#define CFENCE() asm volatile("" ::: "memory")
#define SCHED() __builtin_amdgcn_sched_barrier(0)
DI unsigned tg(float f, unsigned t10) { return (asu(f) & ~0x3FFu) | t10; }
DI v4u tg4(f4 v, unsigned t10) { return v4u{tg(v[0], t10), tg(v[1], t10), tg(v[2], t10), tg(v[3], t10)}; }
DI bool tagged4(v4u v, unsigned t10) { return (((v.x ^ t10) | (v.y ^ t10) | (v.z ^ t10) | (v.w ^ t10)) & 0x3FFu) == 0u; }
DI float untg(unsigned u) { return asf(u & ~0x3FFu); }
DI bool valid4(v4u v) { return v.x != SENT && v.y != SENT && v.z != SENT && v.w != SENT; }
DI float rcp(float x) { return __builtin_amdgcn_rcpf(x); }
DI float silu(float v) { return v * rcp(1.f + __builtin_amdgcn_exp2f(v * -1.4426950408889634f)); }

__global__ void __launch_bounds__(512, 2) fused_block(Params p) {
  __shared__ __attribute__((aligned(16))) unsigned char smem[L_END];
  LAS unsigned char* L = (LAS unsigned char*)smem;
  const unsigned lds0 = (unsigned)(uintptr_t)L;
  const int tid = threadIdx.x, lane = tid & 63;
  const int w = (int)rfl(tid >> 6);
  const int id = blockIdx.x, xcd = id & 7, sl = id >> 3;
  const int h = xcd * 4 + (sl >> 3), b = sl & 7;
  const int mpr = (sl >> 1) * 8 + xcd;
  const bool has_mod = sl < 16 && mpr < 60;
  const bool crit = has_mod && mpr < 20;
  const int mcol0 = (2 * mpr + (sl & 1)) * 32;
  const rsrc_t rsk = mkrs(p.kc + ((size_t)(b * NH + h) * SK + (SK - WIN)) * DH, WIN * DH * 2);
  const unsigned vo = (unsigned)(lane * 16 + w * 1024);
  v4u kr[NL], cr[2];
  if (w < NSW) {
    if (!crit) {
#pragma unroll
      for (int i = 0; i < PACE; ++i) kr[i] = ld_nt(rsk, vo, i * 7168);
    }
    if (has_mod) {
      if (w < 5) {
        const GAS v4u* cp = (const GAS v4u*)p.c;
        cr[0] = cp[w * 128 + lane];
        cr[1] = cp[w * 128 + 64 + lane];
      }
      const rsrc_t rsrt = mkrs(p.wmod, DM * 6 * DM * 2);
      const unsigned rt_vo = (unsigned)((lane >> 2) * 7680 + mcol0 * 2 + (lane & 3) * 16);
      const unsigned l0 = (unsigned)(uintptr_t)(LAS unsigned char*)smem;
#pragma unroll
      for (int u = 0; u < 6; ++u) {
        const int m = w + NSW * u;
        if (m < 40) dma16(rsrt, l0 + L_RT + m * 1024, rt_vo, (unsigned)m * 16u * 7680u);
      }
    }
  }
  SCHED();
  const int g = lane >> 4, i16 = lane & 15;
  const int tq = i16 >> 2, tp = lane & 3;
  const rsrc_t rsws = mkrs(p.ws, WS_END);
  volatile LAS unsigned* flags = (volatile LAS unsigned*)(L + L_FLAG);
  const int quad = ((sl - 16) >> 2) * 8 + xcd;
  const bool is_mlp = sl >= 16 && quad < 27;
  const int mm = 2 * quad + (sl & 1), hf = (sl >> 1) & 1;
  const bool dense = has_mod && mpr >= 30 && mpr < 50;

  const rsrc_t rswo = mkrs(p.wo + (size_t)h * DH * DM, DH * DM * 2);
  unsigned wov[5], w2v[5], w1v;
  const unsigned tstart = (unsigned)__builtin_amdgcn_s_memrealtime();

  unsigned t10 = 0;
  if (w < NSW) {
    const rsrc_t rsv = mkrs(p.vc + ((size_t)(b * NH + h) * SK + (SK - WIN)) * DH, WIN * DH * 2);
    v4u vr[NL];
    if (w >= 4) __builtin_amdgcn_s_setprio(1);
    auto silu_c = [&]() {
      if (w < 5) {
#pragma unroll
        for (int u = 0; u < 2; ++u) {
          const int q = w * 128 + u * 64 + lane, j = q / 80, k8 = q - 80 * j;
          v4u o;
          o.x = pk2(silu(blo(cr[u].x)), silu(bhi(cr[u].x)));
          o.y = pk2(silu(blo(cr[u].y)), silu(bhi(cr[u].y)));
          o.z = pk2(silu(blo(cr[u].z)), silu(bhi(cr[u].z)));
          o.w = pk2(silu(blo(cr[u].w)), silu(bhi(cr[u].w)));
          *(LAS v4u*)(L + L_SC + j * 1296 + k8 * 16) = o;
        }
      }
    };
    bar();
    if (has_mod) {
      silu_c();
      VMWAIT(0);
      asm volatile("s_waitcnt lgkmcnt(0)" ::: "memory");
      if (lane == 0) __hip_atomic_fetch_add((LAS unsigned*)(L + L_FLAG), 1u, __ATOMIC_RELAXED, __HIP_MEMORY_SCOPE_WORKGROUP);
    }
    {
      const rsrc_t rsq = mkrs(p.wq, DM * NH * DH * 2);
      for (int n = w; n < 20; n += NSW) {
        const int rl = 4 * n + g;
        dma16(rsq, lds0 + L_QT + n * 1024, (unsigned)((80 * b + rl) * 8192 + h * 256 + ((i16 ^ (2 * (rl & 7))) * 16)), 0);
      }
    }
    if (crit) {
#pragma unroll
      for (int i = 0; i < PACE; ++i) kr[i] = ld_nt(rsk, vo, i * 7168);
    }
    SCHED();
#pragma unroll
    for (int i = PACE; i < NL + NPRE; ++i) {
      if (has_mod && i < HN) VMWAIT(PACE); else VMWAIT(PACE - 1);
      if (i < NL) kr[i] = ld_nt(rsk, vo, i * 7168); else vr[i - NL] = ld_nt(rsv, vo, (i - NL) * 7168);
      SCHED();
      if (i == PACE + 4) {
        asm volatile("s_waitcnt lgkmcnt(0)" ::: "memory");
        if (lane == 0) __hip_atomic_fetch_add((LAS unsigned*)(L + L_FLAG) + 2, 1u, __ATOMIC_RELAXED, __HIP_MEMORY_SCOPE_WORKGROUP);
      }
    }
    {
      unsigned spins = 0;
      while (flags[1] == 0u) { __builtin_amdgcn_s_sleep(1); if (++spins > SPIN_L) break; }
    }
    CFENCE();
    const v4u q4 = *(const LAS v4u*)(L + L_Q + i16 * 16);
    t10 = flags[4] & 0x3FFu;
    float s[NL];
#pragma unroll
    for (int i = 0; i < NL; ++i) {
      float d = dot2(kr[i].x, q4.x, 0.f);
      d = dot2(kr[i].y, q4.y, d); d = dot2(kr[i].z, q4.z, d); d = dot2(kr[i].w, q4.w, d);
      s[i] = row16_sum(d);
      if ((i & 3) == 3 && NPRE + (i >> 2) < NL) { SCHED(); VMWAIT(PACE - 1); vr[NPRE + (i >> 2)] = ld_nt(rsv, vo, (NPRE + (i >> 2)) * 7168); SCHED(); }
    }
    constexpr int NV1 = NPRE + (NL >> 2);
    if (w >= 4) s[NL - 1] = -INFINITY;
    float m = s[0];
#pragma unroll
    for (int i = 1; i < NL; ++i) m = fmaxf(m, s[i]);
    m = xrow_max(m);
    constexpr float C2 = 0.08838834764831845f * 1.4426950408889634f;
    const float mc = m * C2;
    unsigned pp[NL];
    float l = 0.f;
#pragma unroll
    for (int i = 0; i < NL; ++i) {
      pp[i] = pk2(0.f, __builtin_amdgcn_exp2f(fmaf(s[i], C2, -mc)));
      l += asf(pp[i]);
    }
    l = xrow_sum(l);
    float acc[8];
#pragma unroll
    for (int k = 0; k < 8; ++k) acc[k] = 0.f;
#pragma unroll
    for (int i = 0; i < NL; ++i) {
      const unsigned p1 = pp[i], p0 = p1 >> 16;
      acc[0] = dot2(vr[i].x, p0, acc[0]); acc[1] = dot2(vr[i].x, p1, acc[1]);
      acc[2] = dot2(vr[i].y, p0, acc[2]); acc[3] = dot2(vr[i].y, p1, acc[3]);
      acc[4] = dot2(vr[i].z, p0, acc[4]); acc[5] = dot2(vr[i].z, p1, acc[5]);
      acc[6] = dot2(vr[i].w, p0, acc[6]); acc[7] = dot2(vr[i].w, p1, acc[7]);
      SCHED();
      if (NV1 + i < NL) { VMWAIT(PACE - 1); vr[NV1 + i] = ld_nt(rsv, vo, (NV1 + i) * 7168); SCHED(); }
      if (NV1 + i == NL - 1 - LEAD) {
        if (lane == 0) __hip_atomic_fetch_add((LAS unsigned*)(L + L_FLAG) + 3, 1u, __ATOMIC_RELAXED, __HIP_MEMORY_SCOPE_WORKGROUP);
      }
    }
#pragma unroll
    for (int k = 0; k < 8; ++k) acc[k] = xrow_sum(acc[k]);
    if (g == 0) {
      LAS float* mr = (LAS float*)(L + L_MRG) + w * 132;
      *(LAS f4*)(mr + 4 + 8 * i16) = f4{acc[0], acc[1], acc[2], acc[3]};
      *(LAS f4*)(mr + 8 + 8 * i16) = f4{acc[4], acc[5], acc[6], acc[7]};
      if (i16 == 0) { mr[0] = m; mr[1] = l; }
    }
    asm volatile("s_waitcnt lgkmcnt(0)" ::: "memory");
    unsigned arrived = 0;
    if (lane == 0) arrived = __hip_atomic_fetch_add((LAS unsigned*)(L + L_FLAG) + 8, 1u, __ATOMIC_RELAXED, __HIP_MEMORY_SCOPE_WORKGROUP);
    if (rfl(arrived) == (unsigned)(NSW - 1)) {
      CFENCE();
      if (lane < 32) {
        const LAS float* mr = (const LAS float*)(L + L_MRG);
        float mw[NSW], lw[NSW];
        f4 aw[NSW];
#pragma unroll
        for (int u = 0; u < NSW; ++u) { mw[u] = mr[u * 132]; lw[u] = mr[u * 132 + 1]; aw[u] = *(const LAS f4*)(mr + u * 132 + 4 + 4 * lane); }
        const float M = fmaxf(fmaxf(fmaxf(mw[0], mw[1]), fmaxf(mw[2], mw[3])), fmaxf(fmaxf(mw[4], mw[5]), mw[6]));
        float Lsum = 0.f;
        f4 o = {0, 0, 0, 0};
#pragma unroll
        for (int u = 0; u < NSW; ++u) {
          const float f = __builtin_amdgcn_exp2f((mw[u] - M) * C2);
          Lsum = fmaf(f, lw[u], Lsum);
          o += f * aw[u];
        }
        const float inv = rcp(Lsum);
        st_sc2(v2u{canonu(pk2(o[0] * inv, o[1] * inv)), canonu(pk2(o[2] * inv, o[3] * inv))}, rsws, WS_OX + (unsigned)(h * 8 + b) * 256 + lane * 8);
      }
    }
  } else {
    __builtin_amdgcn_s_setprio(3);
    if (lane == 0) { flags[0] = 0u; flags[1] = 0u; flags[2] = 0u; flags[3] = 0u; flags[5] = 0u; flags[6] = 0u; flags[8] = 0u; }
    const unsigned E = __builtin_amdgcn_raw_buffer_load_b32(rsws, WS_EPOCH, 0, 16);
    unsigned warm = 0;
    {
      const int n = sl * 64 + lane, pi = n / 640, row = n - 640 * pi, pr = xcd + 8 * pi;
      if (pr < 20) warm = __builtin_amdgcn_raw_buffer_load_b32(mkrs(p.wmod, DM * 6 * DM * 2), (unsigned)(row * 7680 + pr * 128), 0, 2);
    }
    unsigned touch = 0;
    {
      const GAS unsigned* a = lane == 0 ? (const GAS unsigned*)(p.ws + WS_END + 128) : lane == 1 ? (const GAS unsigned*)p.out
                            : lane == 2 ? (const GAS unsigned*)(p.wo + (size_t)h * DH * DM) : lane == 3 ? (const GAS unsigned*)p.w1 : (const GAS unsigned*)p.w2;
      if (lane < 5) touch = __builtin_nontemporal_load(a);
    }
    const GAS v4u* xp = (const GAS v4u*)p.x;
    v4u xr[10];
    const int sj = lane >> 3, sub = lane & 7;
#pragma unroll
    for (int u = 0; u < 10; ++u) xr[u] = xp[sj * 80 + sub + 8 * u];
    SCHED();
    bar();
    unsigned xh[5];
#pragma unroll
    for (int u = 0; u < 5; ++u) { const int idx = u * 64 + lane, j = idx / 40, m = idx - 40 * j; xh[u] = ((const GAS unsigned*)p.x)[j * 320 + 40 * b + m]; }
    {
      const v4u sv = {SENT, SENT, SENT, SENT};
      if (lane < 16) st_sc(sv, rsws, WS_OX + (unsigned)(h * 8 + b) * 256 + lane * 16);
      if (lane < 5) st_sc2(v2u{SENT, SENT}, rsws, WS_X1B + (unsigned)(b * 320 + 10 * h) * 4 + lane * 8);
      if (dense && lane < 32) st_sc(sv, rsws, WS_MODD + (unsigned)((lane >> 2) * 640 + (mcol0 - 1920) / 2 + (lane & 3) * 4) * 4);
    }
    {
      float sm = 0.f;
#pragma unroll
      for (int u = 0; u < 10; ++u)
        sm += ((blo(xr[u].x) + bhi(xr[u].x)) + (blo(xr[u].y) + bhi(xr[u].y))) + ((blo(xr[u].z) + bhi(xr[u].z)) + (blo(xr[u].w) + bhi(xr[u].w)));
      const float mean = oct_sum(sm) * (1.f / DM);
      float sq = 0.f;
#pragma unroll
      for (int u = 0; u < 10; ++u) {
#pragma unroll
        for (int k = 0; k < 4; ++k) { const float a = blo(xr[u][k]) - mean, c2 = bhi(xr[u][k]) - mean; sq = fmaf(a, a, sq); sq = fmaf(c2, c2, sq); }
      }
      const float var = oct_sum(sq) * (1.f / DM);
      if (sub == 0) {
        LAS float* st = (LAS float*)(L + L_STAT) + sj * 2;
        st[0] = rbf(mean);
        st[1] = rcp(rbf(__builtin_amdgcn_sqrtf(rbf(rbf(var) + 1e-5f))));
      }
    }
    VMWAIT(0);
    asm volatile("" :: "v"(warm), "v"(touch));
    const unsigned T = rfl(E) + 1u;
    t10 = T & 0x3FFu;
    if (lane == 0) flags[4] = T;
    if (lane == 0) st_sc2(v2u{T, ~T}, rsws, WS_INIT + (unsigned)(h * 8 + b) * 8);
    if (has_mod) {
      unsigned spins = 0;
      while (flags[0] < (unsigned)NSW) { __builtin_amdgcn_s_sleep(1); if (++spins > SPIN_L) break; }
      CFENCE();
      f4 a0 = {0, 0, 0, 0}, a1 = {0, 0, 0, 0};
      const LAS unsigned char* ap = L + L_RT + (8 * g + tq) * 64 + tp * 8;
      const LAS unsigned char* bp = L + L_SC + (lane & 7) * 1296 + g * 16;
#pragma unroll 2
      for (int s = 0; s < 20; ++s) {
        const v4u bb = *(const LAS v4u*)(bp + s * 64);
        const v2u x0 = trread(ap + s * 2048), x1 = trread(ap + s * 2048 + 256);
        const v2u y0 = trread(ap + s * 2048 + 32), y1 = trread(ap + s * 2048 + 256 + 32);
        a0 = mfma32(v4u{x0.x, x0.y, x1.x, x1.y}, bb, a0);
        a1 = mfma32(v4u{y0.x, y0.y, y1.x, y1.y}, bb, a1);
      }
      if (i16 < 8) {
        if (dense) {
          const unsigned o = WS_MODD + (unsigned)(i16 * 640 + (mcol0 - 1920 + 4 * g) / 2) * 4;
          st_sc2(v2u{canonu(pk2(a0[0], a0[1])), canonu(pk2(a0[2], a0[3]))}, rsws, o);
          st_sc2(v2u{canonu(pk2(a1[0], a1[1])), canonu(pk2(a1[2], a1[3]))}, rsws, o + 32);
        } else {
          const unsigned o = WS_MODG + (unsigned)(i16 * 1920 + (mcol0 + 4 * g) / 2) * 8;
          st_sc(v4u{pk2(a0[0], a0[1]), T, pk2(a0[2], a0[3]), T}, rsws, o);
          st_sc(v4u{pk2(a1[0], a1[1]), T, pk2(a1[2], a1[3]), T}, rsws, o + 64);
        }
      }
    }
    {
      v2u sa[5], ca[5], fl;
      { while ((unsigned)__builtin_amdgcn_s_memrealtime() - tstart < D1) __builtin_amdgcn_s_sleep(2); }
      for (unsigned spins = 0;; ++spins) {
        CFENCE();
        bool ok = true;
#pragma unroll
        for (int u = 0; u < 5; ++u) {
          const int idx = u * 64 + lane, j = idx / 40, m = idx - 40 * j;
          const unsigned o = WS_MODG + (unsigned)(j * 1920 + 40 * b + m) * 8;
          sa[u] = ld_sc2(rsws, o);
          ca[u] = ld_sc2(rsws, o + 320 * 8);
        }
        fl = ld_sc2(rsws, WS_INIT + (unsigned)(h * 8 + (lane & 7)) * 8);
#pragma unroll
        for (int u = 0; u < 5; ++u) ok = ok && sa[u].y == T && ca[u].y == T;
        ok = ok && fl.x == T && fl.y == ~T;
        if (__all(ok) || spins > SPIN_G) break;
        __builtin_amdgcn_s_sleep(32);
      }
      asm volatile("s_waitcnt lgkmcnt(0)" ::: "memory");
#pragma unroll
      for (int u = 0; u < 5; ++u) {
        const int idx = u * 64 + lane, j = idx / 40, m = idx - 40 * j;
        const float mean = ((LAS float*)(L + L_STAT))[2 * j], den = ((LAS float*)(L + L_STAT))[2 * j + 1];
        const float n0 = rbf(rbf(blo(xh[u]) - mean) * den), n1 = rbf(rbf(bhi(xh[u]) - mean) * den);
        const float h0 = rbf(n0 * rbf(1.f + blo(ca[u].x))) + blo(sa[u].x), h1 = rbf(n1 * rbf(1.f + bhi(ca[u].x))) + bhi(sa[u].x);
        *(LAS unsigned*)(L + L_HS + j * 176 + m * 4) = pk2(h0, h1);
      }
    }
    {
      unsigned spins = 0;
      while (flags[2] < (unsigned)NSW) { __builtin_amdgcn_s_sleep(1); if (++spins > SPIN_L) break; }
      CFENCE();
    }
    {
      f4 qa[8];
#pragma unroll
      for (int t = 0; t < 8; ++t) qa[t] = f4{0, 0, 0, 0};
#pragma unroll
      for (int s = 0; s < 5; ++s) {
        const v2u bb = *(const LAS v2u*)(L + L_HS + (lane & 7) * 176 + s * 32 + g * 8);
        const int row = 16 * s + 4 * g + tq;
        const LAS unsigned char* rp = L + L_QT + row * 256 + (tp & 1) * 8;
        const int sw = 2 * (row & 7);
#pragma unroll
        for (int t = 0; t < 8; ++t) qa[t] = mfma16(trread(rp + (((2 * t + (tp >> 1)) ^ sw) * 16)), bb, qa[t]);
      }
      if (i16 < 8) {
        const unsigned o = WS_QP + (unsigned)(((h * 8 + b) * 8 + i16) * 128 + 4 * g) * 4;
#pragma unroll
        for (int t = 0; t < 8; ++t) st_sc(tg4(qa[t], t10), rsws, o + t * 64);
      }
    }
    const unsigned tpub = (unsigned)__builtin_amdgcn_s_memrealtime();
    {
      v4u qv[4];
      { while ((unsigned)__builtin_amdgcn_s_memrealtime() - tpub < D2) __builtin_amdgcn_s_sleep(2); }
      for (unsigned spins = 0;; ++spins) {
        CFENCE();
        bool ok = true;
#pragma unroll
        for (int u = 0; u < 4; ++u) qv[u] = ld_sc(rsws, WS_QP + (unsigned)(((h * 8 + 2 * u + (lane >> 5)) * 8 + b) * 128 + 4 * (lane & 31)) * 4);
#pragma unroll
        for (int u = 0; u < 4; ++u) ok = ok && tagged4(qv[u], t10);
        if (__all(ok) || spins > SPIN_G) break;
        __builtin_amdgcn_s_sleep(32);
      }
      f4 qs;
#pragma unroll
      for (int k = 0; k < 4; ++k) qs[k] = x32_sum((untg(qv[0][k]) + untg(qv[1][k])) + (untg(qv[2][k]) + untg(qv[3][k])));
      if (lane < 32) *(LAS v2u*)(L + L_Q + lane * 8) = v2u{pk2(qs[0], qs[1]), pk2(qs[2], qs[3])};
      asm volatile("s_waitcnt lgkmcnt(0)" ::: "memory");
      if (lane == 0) flags[1] = 1u;
    }
    {
      const unsigned go = WS_MODG + (unsigned)(b * 1920 + (lane < 10 ? 640 + 10 * h + lane : 1600 + 10 * h + (lane < 20 ? lane - 10 : 0))) * 8;
      const unsigned xv = ((const GAS unsigned*)p.x)[b * 320 + 10 * h + (lane < 10 ? lane : 0)];
      v2u gv;
      for (unsigned spins = 0;; ++spins) {
        CFENCE();
        gv = ld_sc2(rsws, go);
        if (__all(gv.y == T) || spins > SPIN_G) break;
        __builtin_amdgcn_s_sleep(32);
      }
      if (lane < 20) *(LAS unsigned*)(L + L_VEC + lane * 4) = gv.x;
      if (lane < 10) *(LAS unsigned*)(L + L_VEC + 80 + lane * 4) = xv;
      if (is_mlp) {
        for (unsigned spins = 0;; ++spins) {
          CFENCE();
          bool ok = true;
#pragma unroll
          for (int u = 0; u < 4; ++u) { const v2u f = ld_sc2(rsws, WS_INIT + (unsigned)(u * 64 + lane) * 8); ok = ok && f.x == T && f.y == ~T; }
          if (__all(ok) || spins > SPIN_G) break;
          __builtin_amdgcn_s_sleep(32);
        }
        {
          v4u cv[20];
          for (unsigned spins = 0;; ++spins) {
            CFENCE();
            bool ok = true;
#pragma unroll
            for (int u = 0; u < 20; ++u) cv[u] = ld_sc(rsws, WS_MODD + (unsigned)(u * 64 + lane) * 16);
#pragma unroll
            for (int u = 0; u < 20; ++u) ok = ok && valid4(cv[u]);
            if (__all(ok) || spins > SPIN_G) break;
            __builtin_amdgcn_s_sleep(32);
          }
#pragma unroll
          for (int u = 0; u < 20; ++u) {
            const int pc = u * 64 + lane, j = pc / 160, r = pc - 160 * j;
            v4u o = cv[u];
            if (r >= 80) {
#pragma unroll
              for (int k = 0; k < 4; ++k) o[k] = pk2(1.f + blo(o[k]), 1.f + bhi(o[k]));
            }
            *(LAS v4u*)(L + L_CS + (r < 80 ? 10240 + (j * 320 + 4 * r) * 4 : (j * 320 + 4 * (r - 80)) * 4)) = o;
          }
        }
      }
    }
#pragma unroll
    for (int c = 0; c < 5; ++c) { const int ci = c * 64 + lane, row = ci / 10, ch = ci - 10 * row; wov[c] = (unsigned)(row * 1280 + b * 160 + ch * 16); asm volatile("" : "+v"(wov[c])); }
#pragma unroll
    for (int c = 0; c < 5; ++c) {
      const int ci = c * 64 + lane, row = ci / 40, pos = ci - 40 * row;
      w2v[c] = (unsigned)(row * 1280 + hf * 640 + (pos ^ (2 * (row & 3))) * 16); asm volatile("" : "+v"(w2v[c]));
    }
    w1v = (unsigned)((lane >> 2) * (FF * 2) + mm * 64 + (lane & 3) * 16); asm volatile("" : "+v"(w1v));
    if (is_mlp) {
      const rsrc_t rsw1t = mkrs(p.w1, DM * FF * 2);
      const rsrc_t rsw2t = mkrs(p.w2 + (size_t)mm * 32 * DM, 32 * DM * 2);
#pragma unroll
      for (int n = 0; n < 60; ++n) {
        if (n >= CPACE) VMWAIT(CPACE - 1);
        if (n < 40) dma16(rsw1t, lds0 + L_RT + n * 1024, w1v, (unsigned)n * 16u * (FF * 2));
        else dma16(rsw2t, lds0 + L_W2 + (n - 40) * 1024, w2v[(n - 40) % 5], (unsigned)((n - 40) / 5) * 8u * 1280u);
      }
    }
    {
      unsigned spins = 0;
      while (flags[3] < (unsigned)NSW) { __builtin_amdgcn_s_sleep(1); if (++spins > SPIN_L) break; }
      CFENCE();
#pragma unroll
      for (int n = 0; n < 20; ++n) dma16(rswo, lds0 + L_T1 + n * 1024, wov[n % 5], (unsigned)(n / 5) * 32u * 1280u);
    }
  }

  __builtin_amdgcn_s_setprio(0);
  bar();
  auto until = [&](unsigned t0, unsigned ticks) { while ((unsigned)__builtin_amdgcn_s_memrealtime() - t0 < ticks) __builtin_amdgcn_s_sleep(1); };
  auto lds_wait = [&](int k, unsigned v) { unsigned spins = 0; while (flags[k] < v) { __builtin_amdgcn_s_sleep(1); if (++spins > SPIN_L) break; } CFENCE(); };
  const int t1t = w == 0 ? 0 : w - 2;
  v4u af[4];
  unsigned o1;
  { const int cc = 4 * t1t + g; o1 = WS_T1P + (unsigned)((((i16 * 32 + 4 * b + cc / 5) * 32 + h) * 5 + cc % 5) * 16); asm volatile("" : "+v"(o1)); }
  if (w == 1 || w == 2) {
    const int t = tid - 64;
    __builtin_amdgcn_s_sleep(6);
    v4u v;
    for (unsigned spins = 0;; ++spins) {
      CFENCE();
      v = ld_sc(rsws, WS_OX + (unsigned)h * 2048 + t * 16);
      if (__all(valid4(v)) || spins > SPIN_G) break;
      __builtin_amdgcn_s_sleep(4);
    }
    *(LAS v4u*)(L + L_OS + (t >> 4) * 272 + (t & 15) * 16) = v;
  } else if (w == 7) {
    VMWAIT(0);
    if (lane == 0) flags[5] = 1u;
  } else {
    lds_wait(5, 1u);
    const LAS unsigned char* ap = L + L_T1 + (8 * g + tq) * 160 + (16 * t1t + 4 * tp) * 2;
#pragma unroll
    for (int s = 0; s < 4; ++s) {
      const v2u x0 = trread(ap + s * 32 * 160), x1 = trread(ap + s * 32 * 160 + 4 * 160);
      af[s] = v4u{x0.x, x0.y, x1.x, x1.y};
    }
  }
  bar();
  if (w == 0 || (w >= 3 && w < 7)) {
    const LAS unsigned char* bp = L + L_OS + (lane & 7) * 272 + g * 16;
    v4u bb[4];
#pragma unroll
    for (int s = 0; s < 4; ++s) bb[s] = *(const LAS v4u*)(bp + s * 64);
    SCHED();
    f4 y0 = mfma32(af[0], bb[0], f4{0, 0, 0, 0}), y1 = mfma32(af[1], bb[1], f4{0, 0, 0, 0});
    y0 = mfma32(af[2], bb[2], y0); y1 = mfma32(af[3], bb[3], y1);
    if (i16 < 8) st_sc(tg4(y0 + y1, t10), rsws, o1);
  }
  v4u a4[5], a5[4];
  unsigned o5 = WS_T5P + (unsigned)((mm * 8 + (lane & 7)) * 640 + 320 * hf + 64 * w + 16 * (i16 >> 3) + 4 * g) * 4;
  asm volatile("" : "+v"(o5));
  if (is_mlp) {
    const LAS unsigned char* ap = L + L_RT + (160 * (w >> 1) + 8 * g + tq) * 64 + (w & 1) * 32 + tp * 8;
#pragma unroll
    for (int s = 0; s < 5; ++s) {
      const v2u x0 = trread(ap + s * 2048), x1 = trread(ap + s * 2048 + 256);
      a4[s] = v4u{x0.x, x0.y, x1.x, x1.y};
    }
    const int r0 = 8 * g + tq;
    const LAS unsigned char* wp = L + L_W2 + r0 * 640 + (tp & 1) * 8;
    const int f = 2 * (r0 & 3);
#pragma unroll
    for (int tt = 0; tt < 4; ++tt) {
      const int ch = (2 * (4 * (w < 5 ? w : 0) + tt) + (tp >> 1)) ^ f;
      const v2u x0 = trread(wp + ch * 16), x1 = trread(wp + 4 * 640 + ch * 16);
      a5[tt] = v4u{x0.x, x0.y, x1.x, x1.y};
    }
  }
  if (w >= 4 && w < 7) {
    const unsigned tB2 = (unsigned)__builtin_amdgcn_s_memrealtime();
    const int c = 2 * (w - 4) + (lane >> 5), pr = lane & 31;
    const bool act = c < 5;
    const unsigned o = WS_T1P + (unsigned)(((b * 32 + h) * 32 + pr) * 5 + (act ? c : 0)) * 16;
    const LAS unsigned* vec = (const LAS unsigned*)(L + L_VEC);
    const int cv = act ? c : 0;
    const unsigned g0 = vec[2 * cv], g1 = vec[2 * cv + 1], x0 = vec[20 + 2 * cv], x1 = vec[20 + 2 * cv + 1];
    until(tB2, GB);
    v4u v;
    for (unsigned spins = 0;; ++spins) {
      CFENCE();
      v = ld_sc(rsws, o);
      if (__all(tagged4(v, t10)) || spins > SPIN_G) break;
      __builtin_amdgcn_s_sleep(4);
    }
    f4 sm;
#pragma unroll
    for (int k = 0; k < 4; ++k) sm[k] = x16_sum(row16_sum(untg(v[k])));
    const float r0 = rbf(blo(x0) + rbf(blo(g0) * rbf(sm[0]))), r1 = rbf(bhi(x0) + rbf(bhi(g0) * rbf(sm[1])));
    const float r2 = rbf(blo(x1) + rbf(blo(g1) * rbf(sm[2]))), r3 = rbf(bhi(x1) + rbf(bhi(g1) * rbf(sm[3])));
    if (pr == 0 && act) {
      st_sc2(v2u{canonu(pk2(r0, r1)), canonu(pk2(r2, r3))}, rsws, WS_X1B + (unsigned)(b * 320 + 10 * h + 2 * c) * 4);
      *(LAS f4*)(L + L_X1O + c * 16) = f4{r0, r1, r2, r3};
    }
    asm volatile("s_waitcnt lgkmcnt(0)" ::: "memory");
    if (lane == 0) __hip_atomic_fetch_add((LAS unsigned*)(L + L_FLAG) + 6, 1u, __ATOMIC_RELAXED, __HIP_MEMORY_SCOPE_WORKGROUP);
  }
  lds_wait(6, 3u);
  const unsigned tB = (unsigned)__builtin_amdgcn_s_memrealtime();
  if (is_mlp) {
    {
      const unsigned o = WS_X1B + (unsigned)w * 1280 + lane * 4;
      float cs[10], sh[10];
#pragma unroll
      for (int u = 0; u < 5; ++u) {
        const unsigned c = *(const LAS unsigned*)(L + L_CS + (w * 320 + u * 64 + lane) * 4);
        const unsigned d = *(const LAS unsigned*)(L + L_CS + 10240 + (w * 320 + u * 64 + lane) * 4);
        cs[2 * u] = blo(c); cs[2 * u + 1] = bhi(c); sh[2 * u] = blo(d); sh[2 * u + 1] = bhi(d);
        asm volatile("" : "+v"(cs[2 * u]), "+v"(cs[2 * u + 1]), "+v"(sh[2 * u]), "+v"(sh[2 * u + 1]));
      }
      unsigned oh2 = (unsigned)(w * 1296 + lane * 4);
      asm volatile("" : "+v"(oh2));
      until(tB, GC);
      unsigned xv[5];
      for (unsigned spins = 0;; ++spins) {
        CFENCE();
        bool ok = true;
#pragma unroll
        for (int u = 0; u < 5; ++u) xv[u] = __builtin_amdgcn_raw_buffer_load_b32(rsws, o + u * 256, 0, 16);
#pragma unroll
        for (int u = 0; u < 5; ++u) ok = ok && xv[u] != SENT;
        if (__all(ok) || spins > SPIN_G) break;
        __builtin_amdgcn_s_sleep(4);
      }
      const float x00 = blo(rfl(xv[0]));
      float s1 = 0.f, s2 = 0.f;
#pragma unroll
      for (int u = 0; u < 5; ++u) {
        const float d0 = blo(xv[u]) - x00, d1 = bhi(xv[u]) - x00;
        s1 += d0 + d1; s2 = fmaf(d0, d0, fmaf(d1, d1, s2));
      }
      s1 = row16_sum(s1); s2 = row16_sum(s2);
      s1 = xrow_sum(s1) * (1.f / DM); s2 = xrow_sum(s2) * (1.f / DM);
      const float rs = __builtin_amdgcn_rsqf(fmaxf(s2 - s1 * s1, 0.f) + 1e-5f), mr = -(x00 + s1) * rs;
#pragma unroll
      for (int u = 0; u < 5; ++u) {
        const float n0 = fmaf(blo(xv[u]), rs, mr), n1 = fmaf(bhi(xv[u]), rs, mr);
        *(LAS unsigned*)(L + L_H2 + oh2 + u * 256) = pk2(fmaf(n0, cs[2 * u], sh[2 * u]), fmaf(n1, cs[2 * u + 1], sh[2 * u + 1]));
      }
    }
    bar();
    {
      const LAS unsigned char* bp = L + L_H2 + (lane & 7) * 1296 + (160 * (w >> 1) + 8 * g) * 2;
      v4u bb[5];
#pragma unroll
      for (int s = 0; s < 5; ++s) bb[s] = *(const LAS v4u*)(bp + s * 64);
      SCHED();
      f4 a = mfma32(a4[0], bb[0], f4{0, 0, 0, 0}), a2 = mfma32(a4[1], bb[1], f4{0, 0, 0, 0});
      a = mfma32(a4[2], bb[2], a); a2 = mfma32(a4[3], bb[3], a2); a = mfma32(a4[4], bb[4], a);
      *(LAS f4*)(L + L_T4P + ((w * 4 + g) * 8 + (lane & 7)) * 16) = a + a2;
    }
    bar();
    if (tid < 128) {
      const int j = tid >> 4, c = 2 * (tid & 15), t = c >> 4, gg = (c & 15) >> 2, r = c & 3;
      const LAS float* z = (const LAS float*)(L + L_T4P + ((t * 4 + gg) * 8 + j) * 16) + r;
      const float z0 = (z[0] + z[256]) + (z[512] + z[768]), z1 = (z[1] + z[257]) + (z[513] + z[769]);
      *(LAS unsigned*)(L + L_H1 + j * 80 + c * 2) = pk2(silu(rbf(z0)), silu(rbf(z1)));
    }
    bar();
    if (w < 5) {
      const v4u bb = *(const LAS v4u*)(L + L_H1 + (lane & 7) * 80 + g * 16);
      f4 y[4];
#pragma unroll
      for (int tt = 0; tt < 4; ++tt) y[tt] = mfma32(a5[tt], bb, f4{0, 0, 0, 0});
      const bool up = i16 >= 8;
      st_sc(tg4(up ? y[1] : y[0], t10), rsws, o5);
      st_sc(tg4(up ? y[3] : y[2], t10), rsws, o5 + 128);
    }
  }
  const unsigned tD = (unsigned)__builtin_amdgcn_s_memrealtime();
  if (w < 5) {
    const bool act = lane < NMLP;
    const unsigned o = WS_T5P + (unsigned)(((act ? lane : 0) * 8 + b) * 640 + 20 * h + 4 * w) * 4;
    const LAS unsigned* vec = (const LAS unsigned*)(L + L_VEC);
    const unsigned g0 = vec[10 + 2 * w], g1 = vec[10 + 2 * w + 1];
    const f4 x1 = *(const LAS f4*)(L + L_X1O + w * 16);
    if (is_mlp) until(tD, GD1); else until(tB, GD2);
    v4u v;
    for (unsigned spins = 0;; ++spins) {
      CFENCE();
      v = ld_sc(rsws, o);
      if (__all(tagged4(v, t10)) || spins > SPIN_G) break;
      __builtin_amdgcn_s_sleep(4);
    }
    f4 sm;
#pragma unroll
    for (int k = 0; k < 4; ++k) sm[k] = wave_sum(act ? untg(v[k]) : 0.f);
    if (lane == 0) {
      const float r0 = x1[0] + rbf(blo(g0) * rbf(sm[0])), r1 = x1[1] + rbf(bhi(g0) * rbf(sm[1]));
      const float r2 = x1[2] + rbf(blo(g1) * rbf(sm[2])), r3 = x1[3] + rbf(bhi(g1) * rbf(sm[3]));
      *(GAS v2u*)(p.out + b * DM + 20 * h + 4 * w) = v2u{pk2(r0, r1), pk2(r2, r3)};
    }
  }
  if (id == 0 && tid == 448) {
    const unsigned E = __builtin_amdgcn_raw_buffer_load_b32(rsws, WS_EPOCH, 0, 16);
    __builtin_amdgcn_raw_buffer_store_b32(E + 1u, rsws, WS_EPOCH, 0, 16);
  }
}

}

extern "C" void kernel_launch(void* const* d_in, const int* in_sizes, int n_in, void* d_out, int out_size, void* d_ws,
                              size_t ws_size, hipStream_t stream) {
  Params p;
  p.x = (const u16*)d_in[0];
  p.kc = (const u16*)d_in[1];
  p.vc = (const u16*)d_in[2];
  p.wq = (const u16*)d_in[3];
  p.wo = (const u16*)d_in[4];
  p.w1 = (const u16*)d_in[6];
  p.w2 = (const u16*)d_in[7];
  p.c = (const u16*)d_in[8];
  p.wmod = (const u16*)d_in[9];
  p.out = (u16*)d_out;
  p.ws = (unsigned char*)d_ws;
  hipLaunchKernelGGL(fused_block, dim3(256), dim3(512), 0, stream, p);
}
```

```cpp
#include <hip/hip_runtime.h>
#include <stdint.h>

namespace {

typedef unsigned short u16;
typedef unsigned v4u __attribute__((ext_vector_type(4)));
typedef unsigned v2u __attribute__((ext_vector_type(2)));
typedef float f4 __attribute__((ext_vector_type(4)));
typedef short s4 __attribute__((ext_vector_type(4)));
typedef __bf16 bf2 __attribute__((ext_vector_type(2)));
typedef __bf16 bf8 __attribute__((ext_vector_type(8)));
typedef __amdgpu_buffer_rsrc_t rsrc_t;
#define LAS __attribute__((address_space(3)))
#define GAS __attribute__((address_space(1)))
#define DI __device__ __forceinline__

constexpr int NB = 8, DM = 640, NH = 32, DH = 128, SK = 4096, WIN = 1024, FF = 1728;
constexpr int NSW = 7;
constexpr int NL = 37;
constexpr int PACE = 3;
constexpr int NPRE = 16;
constexpr int HN = 45;
constexpr int LEAD = 3;
constexpr int CPACE = 14;
constexpr unsigned D1 = 600;
constexpr unsigned D2 = 100;
constexpr unsigned GB = 25;
constexpr unsigned GC = 30;
constexpr unsigned GD1 = 40;
constexpr unsigned GD2 = 200;

constexpr int NMLP = 54;
constexpr unsigned WS_EPOCH = 0;
constexpr unsigned WS_INIT = 4096;
constexpr unsigned WS_MODG = 8192;
constexpr unsigned WS_MODD = WS_MODG + NB * 1920 * 8;
constexpr unsigned WS_QP = WS_MODD + NB * 640 * 4;
constexpr unsigned WS_OX = WS_QP + NH * 8 * 8 * 128 * 4;
constexpr unsigned WS_T1P = WS_OX + NH * 8 * 256;
constexpr unsigned WS_X1B = WS_T1P + NB * NH * DM * 4;
constexpr unsigned WS_T5P = WS_X1B + NB * 320 * 4;
constexpr unsigned WS_END = WS_T5P + NMLP * NB * DM * 4;
constexpr unsigned SENT = 0xFFFFFFFFu;

constexpr unsigned L_RT = 0;
constexpr unsigned L_QT = 40960;
constexpr unsigned L_W2 = 61440;
constexpr unsigned L_T1 = 102400;
constexpr unsigned L_CS = 122880;
constexpr unsigned L_MISC = 143360;
constexpr unsigned L_FLAG = L_MISC;
constexpr unsigned L_Q = L_MISC + 64;
constexpr unsigned L_STAT = L_Q + 256;
constexpr unsigned L_VEC = L_STAT + 64;
constexpr unsigned L_X1O = L_VEC + 128;
constexpr unsigned L_SC = L_X1O + 128;
constexpr unsigned L_H2 = L_SC;
constexpr unsigned L_HS = L_SC + 8 * 1296;
constexpr unsigned L_MRG = L_HS + 8 * 176;
constexpr unsigned L_OS = L_MRG + 7 * 132 * 4;
constexpr unsigned L_T4P = L_MRG;
constexpr unsigned L_H1 = L_T4P + 4096;
constexpr unsigned L_END = 163840;
static_assert(L_OS + 8 * 272 <= L_END && L_H1 + 8 * 80 <= L_OS + 8 * 272, "LDS map");
static_assert((L_SC % 16) == 0 && (L_HS % 16) == 0 && (L_MRG % 16) == 0 && (L_OS % 16) == 0 && (L_H1 % 16) == 0, "LDS alignment");

constexpr unsigned SPIN_G = 1u << 16;
constexpr unsigned SPIN_L = 1u << 22;

struct Params {
  const u16 *x, *kc, *vc, *wq, *wo, *w1, *w2, *c, *wmod;
  u16* out;
  unsigned char* ws;
};

DI unsigned rfl(unsigned x) { return __builtin_amdgcn_readfirstlane(x); }
DI float asf(unsigned u) { return __uint_as_float(u); }
DI unsigned asu(float f) { return __float_as_uint(f); }
DI unsigned pk2(float lo, float hi) { bf2 v = {(__bf16)lo, (__bf16)hi}; return __builtin_bit_cast(unsigned, v); }
DI float rbf(float f) { return asf(pk2(f, 0.f) << 16); }
DI float blo(unsigned u) { return asf(u << 16); }
DI float bhi(unsigned u) { return asf(u & 0xffff0000u); }
DI float dot2(unsigned a, unsigned b, float c) {
  return __builtin_amdgcn_fdot2_f32_bf16(__builtin_bit_cast(bf2, a), __builtin_bit_cast(bf2, b), c, false);
}
template <int CTRL> DI float dpp(float x) {
  return __builtin_bit_cast(float, __builtin_amdgcn_mov_dpp(__builtin_bit_cast(int, x), CTRL, 0xf, 0xf, true));
}
DI float row16_sum(float x) { x += dpp<0x128>(x); x += dpp<0x124>(x); x += dpp<0x122>(x); x += dpp<0x121>(x); return x; }
DI float oct_sum(float x) { x += dpp<0xB1>(x); x += dpp<0x4E>(x); x += dpp<0x141>(x); return x; }
DI float x16_sum(float x) {
  auto s = __builtin_amdgcn_permlane16_swap(asu(x), asu(x), false, false);
  return asf(s[0]) + asf(s[1]);
}
DI float x32_sum(float x) {
  auto t = __builtin_amdgcn_permlane32_swap(asu(x), asu(x), false, false);
  return asf(t[0]) + asf(t[1]);
}
DI float xrow_sum(float x) { return x32_sum(x16_sum(x)); }
DI float xrow_max(float x) {
  auto s = __builtin_amdgcn_permlane16_swap(asu(x), asu(x), false, false);
  x = fmaxf(asf(s[0]), asf(s[1]));
  auto t = __builtin_amdgcn_permlane32_swap(asu(x), asu(x), false, false);
  return fmaxf(asf(t[0]), asf(t[1]));
}
DI float wave_sum(float x) { return xrow_sum(row16_sum(x)); }
DI f4 mfma32(v4u a, v4u b, f4 c) {
  return __builtin_amdgcn_mfma_f32_16x16x32_bf16(__builtin_bit_cast(bf8, a), __builtin_bit_cast(bf8, b), c, 0, 0, 0);
}
DI f4 mfma16(v2u a, v2u b, f4 c) {
  return __builtin_amdgcn_mfma_f32_16x16x16bf16_1k(__builtin_bit_cast(s4, a), __builtin_bit_cast(s4, b), c, 0, 0, 0);
}
DI v2u trread(const LAS unsigned char* p) {
  return __builtin_bit_cast(v2u, __builtin_amdgcn_ds_read_tr16_b64_v4i16((LAS s4*)p));
}
DI rsrc_t mkrs(const void* p, unsigned bytes) { return __builtin_amdgcn_make_buffer_rsrc((void*)p, 0, (int)bytes, 0x00020000); }
DI v4u ld_nt(rsrc_t r, unsigned vo, unsigned so) { return __builtin_amdgcn_raw_buffer_load_b128(r, vo, so, 2); }
DI v4u ld_sc(rsrc_t r, unsigned vo) { return __builtin_amdgcn_raw_buffer_load_b128(r, vo, 0, 16); }
DI v2u ld_sc2(rsrc_t r, unsigned vo) { return __builtin_amdgcn_raw_buffer_load_b64(r, vo, 0, 16); }
DI void st_sc(v4u v, rsrc_t r, unsigned vo) { __builtin_amdgcn_raw_buffer_store_b128(v, r, vo, 0, 16); }
DI void st_sc2(v2u v, rsrc_t r, unsigned vo) { __builtin_amdgcn_raw_buffer_store_b64(v, r, vo, 0, 16); }
DI unsigned canonu(unsigned u) { return u == 0xFFFFFFFFu ? 0x7FFF7FFFu : u; }
DI void dma16(rsrc_t r, unsigned lds, unsigned vo, unsigned so) {
  unsigned keep;
  asm volatile("s_nop 4\n\ts_mov_b32 %0, m0\n\ts_mov_b32 m0, %1\n\ts_nop 0\n\tbuffer_load_dwordx4 %2, %3, %4 offen nt lds\n\ts_mov_b32 m0, %0"
               : "=&s"(keep) : "s"(lds), "v"(vo), "s"(r), "s"(so) : "memory");
}
template <int N> DI void vmwait() { asm volatile("s_waitcnt vmcnt(%0)" :: "n"(N) : "memory"); }
#define VMWAIT(n) vmwait<(n)>()
DI void bar() { asm volatile("s_waitcnt lgkmcnt(0)\n\ts_barrier" ::: "memory"); }
#define CFENCE() asm volatile("" ::: "memory")
#define SCHED() __builtin_amdgcn_sched_barrier(0)
DI unsigned tg(float f, unsigned t10) { return (asu(f) & ~0x3FFu) | t10; }
DI v4u tg4(f4 v, unsigned t10) { return v4u{tg(v[0], t10), tg(v[1], t10), tg(v[2], t10), tg(v[3], t10)}; }
DI bool tagged4(v4u v, unsigned t10) { return (((v.x ^ t10) | (v.y ^ t10) | (v.z ^ t10) | (v.w ^ t10)) & 0x3FFu) == 0u; }
DI float untg(unsigned u) { return asf(u & ~0x3FFu); }
DI bool valid4(v4u v) { return v.x != SENT && v.y != SENT && v.z != SENT && v.w != SENT; }
DI float rcp(float x) { return __builtin_amdgcn_rcpf(x); }
DI float silu(float v) { return v * rcp(1.f + __builtin_amdgcn_exp2f(v * -1.4426950408889634f)); }

__global__ void __launch_bounds__(512, 2) fused_block(Params p) {
  __shared__ __attribute__((aligned(16))) unsigned char smem[L_END];
  LAS unsigned char* L = (LAS unsigned char*)smem;
  const unsigned lds0 = (unsigned)(uintptr_t)L;
  const int tid = threadIdx.x, lane = tid & 63;
  const int w = (int)rfl(tid >> 6);
  const int id = blockIdx.x, xcd = id & 7, sl = id >> 3;
  const int h = xcd * 4 + (sl >> 3), b = sl & 7;
  const int mpr = (sl >> 1) * 8 + xcd;
  const bool has_mod = sl < 16 && mpr < 60;
  const bool crit = has_mod && mpr < 20;
  const int mcol0 = (2 * mpr + (sl & 1)) * 32;
  const rsrc_t rsk = mkrs(p.kc + ((size_t)(b * NH + h) * SK + (SK - WIN)) * DH, WIN * DH * 2);
  const unsigned vo = (unsigned)(lane * 16 + w * 1024);
  v4u kr[NL], cr[2];
  if (w < NSW) {
    if (!crit) {
#pragma unroll
      for (int i = 0; i < PACE; ++i) kr[i] = ld_nt(rsk, vo, i * 7168);
    }
    if (has_mod) {
      if (w < 5) {
        const GAS v4u* cp = (const GAS v4u*)p.c;
        cr[0] = cp[w * 128 + lane];
        cr[1] = cp[w * 128 + 64 + lane];
      }
      const rsrc_t rsrt = mkrs(p.wmod, DM * 6 * DM * 2);
      const unsigned rt_vo = (unsigned)((lane >> 2) * 7680 + mcol0 * 2 + (lane & 3) * 16);
      const unsigned l0 = (unsigned)(uintptr_t)(LAS unsigned char*)smem;
#pragma unroll
      for (int u = 0; u < 6; ++u) {
        const int m = w + NSW * u;
        if (m < 40) dma16(rsrt, l0 + L_RT + m * 1024, rt_vo, (unsigned)m * 16u * 7680u);
      }
    }
  }
  SCHED();
  const int g = lane >> 4, i16 = lane & 15;
  const int tq = i16 >> 2, tp = lane & 3;
  const rsrc_t rsws = mkrs(p.ws, WS_END);
  volatile LAS unsigned* flags = (volatile LAS unsigned*)(L + L_FLAG);
  const int quad = ((sl - 16) >> 2) * 8 + xcd;
  const bool is_mlp = sl >= 16 && quad < 27;
  const int mm = 2 * quad + (sl & 1), hf = (sl >> 1) & 1;
  const bool dense = has_mod && mpr >= 30 && mpr < 50;

  const rsrc_t rswo = mkrs(p.wo + (size_t)h * DH * DM, DH * DM * 2);
  unsigned wov[5], w2v[5], w1v;
  const unsigned tstart = (unsigned)__builtin_amdgcn_s_memrealtime();

  unsigned t10 = 0;
  if (w < NSW) {
    const rsrc_t rsv = mkrs(p.vc + ((size_t)(b * NH + h) * SK + (SK - WIN)) * DH, WIN * DH * 2);
    v4u vr[NL];
    if (w >= 4) __builtin_amdgcn_s_setprio(1);
    auto silu_c = [&]() {
      if (w < 5) {
#pragma unroll
        for (int u = 0; u < 2; ++u) {
          const int q = w * 128 + u * 64 + lane, j = q / 80, k8 = q - 80 * j;
          v4u o;
          o.x = pk2(silu(blo(cr[u].x)), silu(bhi(cr[u].x)));
          o.y = pk2(silu(blo(cr[u].y)), silu(bhi(cr[u].y)));
          o.z = pk2(silu(blo(cr[u].z)), silu(bhi(cr[u].z)));
          o.w = pk2(silu(blo(cr[u].w)), silu(bhi(cr[u].w)));
          *(LAS v4u*)(L + L_SC + j * 1296 + k8 * 16) = o;
        }
      }
    };
    bar();
    if (has_mod) {
      silu_c();
      VMWAIT(0);
      asm volatile("s_waitcnt lgkmcnt(0)" ::: "memory");
      if (lane == 0) __hip_atomic_fetch_add((LAS unsigned*)(L + L_FLAG), 1u, __ATOMIC_RELAXED, __HIP_MEMORY_SCOPE_WORKGROUP);
    }
    {
      const rsrc_t rsq = mkrs(p.wq, DM * NH * DH * 2);
      for (int n = w; n < 20; n += NSW) {
        const int rl = 4 * n + g;
        dma16(rsq, lds0 + L_QT + n * 1024, (unsigned)((80 * b + rl) * 8192 + h * 256 + ((i16 ^ (2 * (rl & 7))) * 16)), 0);
      }
    }
    if (crit) {
#pragma unroll
      for (int i = 0; i < PACE; ++i) kr[i] = ld_nt(rsk, vo, i * 7168);
    }
    SCHED();
#pragma unroll
    for (int i = PACE; i < NL + NPRE; ++i) {
      if (has_mod && i < HN) VMWAIT(PACE); else VMWAIT(PACE - 1);
      if (i < NL) kr[i] = ld_nt(rsk, vo, i * 7168); else vr[i - NL] = ld_nt(rsv, vo, (i - NL) * 7168);
      SCHED();
      if (i == PACE + 4) {
        asm volatile("s_waitcnt lgkmcnt(0)" ::: "memory");
        if (lane == 0) __hip_atomic_fetch_add((LAS unsigned*)(L + L_FLAG) + 2, 1u, __ATOMIC_RELAXED, __HIP_MEMORY_SCOPE_WORKGROUP);
      }
    }
    {
      unsigned spins = 0;
      while (flags[1] == 0u) { __builtin_amdgcn_s_sleep(1); if (++spins > SPIN_L) break; }
    }
    CFENCE();
    const v4u q4 = *(const LAS v4u*)(L + L_Q + i16 * 16);
    t10 = flags[4] & 0x3FFu;
    float s[NL];
#pragma unroll
    for (int i = 0; i < NL; ++i) {
      float d = dot2(kr[i].x, q4.x, 0.f);
      d = dot2(kr[i].y, q4.y, d); d = dot2(kr[i].z, q4.z, d); d = dot2(kr[i].w, q4.w, d);
      s[i] = row16_sum(d);
      if ((i & 3) == 3 && NPRE + (i >> 2) < NL) { SCHED(); VMWAIT(PACE - 1); vr[NPRE + (i >> 2)] = ld_nt(rsv, vo, (NPRE + (i >> 2)) * 7168); SCHED(); }
    }
    constexpr int NV1 = NPRE + (NL >> 2);
    if (w >= 4) s[NL - 1] = -INFINITY;
    float m = s[0];
#pragma unroll
    for (int i = 1; i < NL; ++i) m = fmaxf(m, s[i]);
    m = xrow_max(m);
    constexpr float C2 = 0.08838834764831845f * 1.4426950408889634f;
    const float mc = m * C2;
    unsigned pp[NL];
    float l = 0.f;
#pragma unroll
    for (int i = 0; i < NL; ++i) {
      pp[i] = pk2(0.f, __builtin_amdgcn_exp2f(fmaf(s[i], C2, -mc)));
      l += asf(pp[i]);
    }
    l = xrow_sum(l);
    float acc[8];
#pragma unroll
    for (int k = 0; k < 8; ++k) acc[k] = 0.f;
#pragma unroll
    for (int i = 0; i < NL; ++i) {
      const unsigned p1 = pp[i], p0 = p1 >> 16;
      acc[0] = dot2(vr[i].x, p0, acc[0]); acc[1] = dot2(vr[i].x, p1, acc[1]);
      acc[2] = dot2(vr[i].y, p0, acc[2]); acc[3] = dot2(vr[i].y, p1, acc[3]);
      acc[4] = dot2(vr[i].z, p0, acc[4]); acc[5] = dot2(vr[i].z, p1, acc[5]);
      acc[6] = dot2(vr[i].w, p0, acc[6]); acc[7] = dot2(vr[i].w, p1, acc[7]);
      SCHED();
      if (NV1 + i < NL) { VMWAIT(PACE - 1); vr[NV1 + i] = ld_nt(rsv, vo, (NV1 + i) * 7168); SCHED(); }
      if (NV1 + i == NL - 1 - LEAD) {
        if (lane == 0) __hip_atomic_fetch_add((LAS unsigned*)(L + L_FLAG) + 3, 1u, __ATOMIC_RELAXED, __HIP_MEMORY_SCOPE_WORKGROUP);
      }
    }
#pragma unroll
    for (int k = 0; k < 8; ++k) acc[k] = xrow_sum(acc[k]);
    if (g == 0) {
      LAS float* mr = (LAS float*)(L + L_MRG) + w * 132;
      *(LAS f4*)(mr + 4 + 8 * i16) = f4{acc[0], acc[1], acc[2], acc[3]};
      *(LAS f4*)(mr + 8 + 8 * i16) = f4{acc[4], acc[5], acc[6], acc[7]};
      if (i16 == 0) { mr[0] = m; mr[1] = l; }
    }
    asm volatile("s_waitcnt lgkmcnt(0)" ::: "memory");
    unsigned arrived = 0;
    if (lane == 0) arrived = __hip_atomic_fetch_add((LAS unsigned*)(L + L_FLAG) + 8, 1u, __ATOMIC_RELAXED, __HIP_MEMORY_SCOPE_WORKGROUP);
    if (rfl(arrived) == (unsigned)(NSW - 1)) {
      CFENCE();
      if (lane < 32) {
        const LAS float* mr = (const LAS float*)(L + L_MRG);
        float mw[NSW], lw[NSW];
        f4 aw[NSW];
#pragma unroll
        for (int u = 0; u < NSW; ++u) { mw[u] = mr[u * 132]; lw[u] = mr[u * 132 + 1]; aw[u] = *(const LAS f4*)(mr + u * 132 + 4 + 4 * lane); }
        const float M = fmaxf(fmaxf(fmaxf(mw[0], mw[1]), fmaxf(mw[2], mw[3])), fmaxf(fmaxf(mw[4], mw[5]), mw[6]));
        float Lsum = 0.f;
        f4 o = {0, 0, 0, 0};
#pragma unroll
        for (int u = 0; u < NSW; ++u) {
          const float f = __builtin_amdgcn_exp2f((mw[u] - M) * C2);
          Lsum = fmaf(f, lw[u], Lsum);
          o += f * aw[u];
        }
        const float inv = rcp(Lsum);
        st_sc2(v2u{canonu(pk2(o[0] * inv, o[1] * inv)), canonu(pk2(o[2] * inv, o[3] * inv))}, rsws, WS_OX + (unsigned)(h * 8 + b) * 256 + lane * 8);
      }
    }
  } else {
    __builtin_amdgcn_s_setprio(3);
    if (lane == 0) { flags[0] = 0u; flags[1] = 0u; flags[2] = 0u; flags[3] = 0u; flags[5] = 0u; flags[6] = 0u; flags[8] = 0u; }
    const unsigned E = __builtin_amdgcn_raw_buffer_load_b32(rsws, WS_EPOCH, 0, 16);
    unsigned warm = 0;
    {
      const int n = sl * 64 + lane, pi = n / 640, row = n - 640 * pi, pr = xcd + 8 * pi;
      if (pr < 20) warm = __builtin_amdgcn_raw_buffer_load_b32(mkrs(p.wmod, DM * 6 * DM * 2), (unsigned)(row * 7680 + pr * 128), 0, 2);
    }
    const GAS v4u* xp = (const GAS v4u*)p.x;
    v4u xr[10];
    const int sj = lane >> 3, sub = lane & 7;
#pragma unroll
    for (int u = 0; u < 10; ++u) xr[u] = xp[sj * 80 + sub + 8 * u];
    SCHED();
    bar();
    unsigned xh[5];
#pragma unroll
    for (int u = 0; u < 5; ++u) { const int idx = u * 64 + lane, j = idx / 40, m = idx - 40 * j; xh[u] = ((const GAS unsigned*)p.x)[j * 320 + 40 * b + m]; }
    {
      const v4u sv = {SENT, SENT, SENT, SENT};
      if (lane < 16) st_sc(sv, rsws, WS_OX + (unsigned)(h * 8 + b) * 256 + lane * 16);
      if (lane < 5) st_sc2(v2u{SENT, SENT}, rsws, WS_X1B + (unsigned)(b * 320 + 10 * h) * 4 + lane * 8);
      if (dense && lane < 32) st_sc(sv, rsws, WS_MODD + (unsigned)((lane >> 2) * 640 + (mcol0 - 1920) / 2 + (lane & 3) * 4) * 4);
    }
    {
      float sm = 0.f;
#pragma unroll
      for (int u = 0; u < 10; ++u)
        sm += ((blo(xr[u].x) + bhi(xr[u].x)) + (blo(xr[u].y) + bhi(xr[u].y))) + ((blo(xr[u].z) + bhi(xr[u].z)) + (blo(xr[u].w) + bhi(xr[u].w)));
      const float mean = oct_sum(sm) * (1.f / DM);
      float sq = 0.f;
#pragma unroll
      for (int u = 0; u < 10; ++u) {
#pragma unroll
        for (int k = 0; k < 4; ++k) { const float a = blo(xr[u][k]) - mean, c2 = bhi(xr[u][k]) - mean; sq = fmaf(a, a, sq); sq = fmaf(c2, c2, sq); }
      }
      const float var = oct_sum(sq) * (1.f / DM);
      if (sub == 0) {
        LAS float* st = (LAS float*)(L + L_STAT) + sj * 2;
        st[0] = rbf(mean);
        st[1] = rcp(rbf(__builtin_amdgcn_sqrtf(rbf(rbf(var) + 1e-5f))));
      }
    }
    VMWAIT(0);
    asm volatile("" :: "v"(warm));
    const unsigned T = rfl(E) + 1u;
    t10 = T & 0x3FFu;
    if (lane == 0) flags[4] = T;
    if (lane == 0) st_sc2(v2u{T, ~T}, rsws, WS_INIT + (unsigned)(h * 8 + b) * 8);
    if (has_mod) {
      unsigned spins = 0;
      while (flags[0] < (unsigned)NSW) { __builtin_amdgcn_s_sleep(1); if (++spins > SPIN_L) break; }
      CFENCE();
      f4 a0 = {0, 0, 0, 0}, a1 = {0, 0, 0, 0};
      const LAS unsigned char* ap = L + L_RT + (8 * g + tq) * 64 + tp * 8;
      const LAS unsigned char* bp = L + L_SC + (lane & 7) * 1296 + g * 16;
#pragma unroll 2
      for (int s = 0; s < 20; ++s) {
        const v4u bb = *(const LAS v4u*)(bp + s * 64);
        const v2u x0 = trread(ap + s * 2048), x1 = trread(ap + s * 2048 + 256);
        const v2u y0 = trread(ap + s * 2048 + 32), y1 = trread(ap + s * 2048 + 256 + 32);
        a0 = mfma32(v4u{x0.x, x0.y, x1.x, x1.y}, bb, a0);
        a1 = mfma32(v4u{y0.x, y0.y, y1.x, y1.y}, bb, a1);
      }
      if (i16 < 8) {
        if (dense) {
          const unsigned o = WS_MODD + (unsigned)(i16 * 640 + (mcol0 - 1920 + 4 * g) / 2) * 4;
          st_sc2(v2u{canonu(pk2(a0[0], a0[1])), canonu(pk2(a0[2], a0[3]))}, rsws, o);
          st_sc2(v2u{canonu(pk2(a1[0], a1[1])), canonu(pk2(a1[2], a1[3]))}, rsws, o + 32);
        } else {
          const unsigned o = WS_MODG + (unsigned)(i16 * 1920 + (mcol0 + 4 * g) / 2) * 8;
          st_sc(v4u{pk2(a0[0], a0[1]), T, pk2(a0[2], a0[3]), T}, rsws, o);
          st_sc(v4u{pk2(a1[0], a1[1]), T, pk2(a1[2], a1[3]), T}, rsws, o + 64);
        }
      }
    }
    {
      v2u sa[5], ca[5], fl;
      { while ((unsigned)__builtin_amdgcn_s_memrealtime() - tstart < D1) __builtin_amdgcn_s_sleep(2); }
      for (unsigned spins = 0;; ++spins) {
        CFENCE();
        bool ok = true;
#pragma unroll
        for (int u = 0; u < 5; ++u) {
          const int idx = u * 64 + lane, j = idx / 40, m = idx - 40 * j;
          const unsigned o = WS_MODG + (unsigned)(j * 1920 + 40 * b + m) * 8;
          sa[u] = ld_sc2(rsws, o);
          ca[u] = ld_sc2(rsws, o + 320 * 8);
        }
        fl = ld_sc2(rsws, WS_INIT + (unsigned)(h * 8 + (lane & 7)) * 8);
#pragma unroll
        for (int u = 0; u < 5; ++u) ok = ok && sa[u].y == T && ca[u].y == T;
        ok = ok && fl.x == T && fl.y == ~T;
        if (__all(ok) || spins > SPIN_G) break;
        __builtin_amdgcn_s_sleep(32);
      }
      asm volatile("s_waitcnt lgkmcnt(0)" ::: "memory");
#pragma unroll
      for (int u = 0; u < 5; ++u) {
        const int idx = u * 64 + lane, j = idx / 40, m = idx - 40 * j;
        const float mean = ((LAS float*)(L + L_STAT))[2 * j], den = ((LAS float*)(L + L_STAT))[2 * j + 1];
        const float n0 = rbf(rbf(blo(xh[u]) - mean) * den), n1 = rbf(rbf(bhi(xh[u]) - mean) * den);
        const float h0 = rbf(n0 * rbf(1.f + blo(ca[u].x))) + blo(sa[u].x), h1 = rbf(n1 * rbf(1.f + bhi(ca[u].x))) + bhi(sa[u].x);
        *(LAS unsigned*)(L + L_HS + j * 176 + m * 4) = pk2(h0, h1);
      }
    }
    {
      unsigned spins = 0;
      while (flags[2] < (unsigned)NSW) { __builtin_amdgcn_s_sleep(1); if (++spins > SPIN_L) break; }
      CFENCE();
    }
    {
      f4 qa[8];
#pragma unroll
      for (int t = 0; t < 8; ++t) qa[t] = f4{0, 0, 0, 0};
#pragma unroll
      for (int s = 0; s < 5; ++s) {
        const v2u bb = *(const LAS v2u*)(L + L_HS + (lane & 7) * 176 + s * 32 + g * 8);
        const int row = 16 * s + 4 * g + tq;
        const LAS unsigned char* rp = L + L_QT + row * 256 + (tp & 1) * 8;
        const int sw = 2 * (row & 7);
#pragma unroll
        for (int t = 0; t < 8; ++t) qa[t] = mfma16(trread(rp + (((2 * t + (tp >> 1)) ^ sw) * 16)), bb, qa[t]);
      }
      if (i16 < 8) {
        const unsigned o = WS_QP + (unsigned)(((h * 8 + b) * 8 + i16) * 128 + 4 * g) * 4;
#pragma unroll
        for (int t = 0; t < 8; ++t) st_sc(tg4(qa[t], t10), rsws, o + t * 64);
      }
    }
    const unsigned tpub = (unsigned)__builtin_amdgcn_s_memrealtime();
    {
      v4u qv[4];
      { while ((unsigned)__builtin_amdgcn_s_memrealtime() - tpub < D2) __builtin_amdgcn_s_sleep(2); }
      for (unsigned spins = 0;; ++spins) {
        CFENCE();
        bool ok = true;
#pragma unroll
        for (int u = 0; u < 4; ++u) qv[u] = ld_sc(rsws, WS_QP + (unsigned)(((h * 8 + 2 * u + (lane >> 5)) * 8 + b) * 128 + 4 * (lane & 31)) * 4);
#pragma unroll
        for (int u = 0; u < 4; ++u) ok = ok && tagged4(qv[u], t10);
        if (__all(ok) || spins > SPIN_G) break;
        __builtin_amdgcn_s_sleep(32);
      }
      f4 qs;
#pragma unroll
      for (int k = 0; k < 4; ++k) qs[k] = x32_sum((untg(qv[0][k]) + untg(qv[1][k])) + (untg(qv[2][k]) + untg(qv[3][k])));
      if (lane < 32) *(LAS v2u*)(L + L_Q + lane * 8) = v2u{pk2(qs[0], qs[1]), pk2(qs[2], qs[3])};
      asm volatile("s_waitcnt lgkmcnt(0)" ::: "memory");
      if (lane == 0) flags[1] = 1u;
    }
    {
      const unsigned go = WS_MODG + (unsigned)(b * 1920 + (lane < 10 ? 640 + 10 * h + lane : 1600 + 10 * h + (lane < 20 ? lane - 10 : 0))) * 8;
      const unsigned xv = ((const GAS unsigned*)p.x)[b * 320 + 10 * h + (lane < 10 ? lane : 0)];
      v2u gv;
      for (unsigned spins = 0;; ++spins) {
        CFENCE();
        gv = ld_sc2(rsws, go);
        if (__all(gv.y == T) || spins > SPIN_G) break;
        __builtin_amdgcn_s_sleep(32);
      }
      if (lane < 20) *(LAS unsigned*)(L + L_VEC + lane * 4) = gv.x;
      if (lane < 10) *(LAS unsigned*)(L + L_VEC + 80 + lane * 4) = xv;
      if (is_mlp) {
        for (unsigned spins = 0;; ++spins) {
          CFENCE();
          bool ok = true;
#pragma unroll
          for (int u = 0; u < 4; ++u) { const v2u f = ld_sc2(rsws, WS_INIT + (unsigned)(u * 64 + lane) * 8); ok = ok && f.x == T && f.y == ~T; }
          if (__all(ok) || spins > SPIN_G) break;
          __builtin_amdgcn_s_sleep(32);
        }
        {
          v4u cv[20];
          for (unsigned spins = 0;; ++spins) {
            CFENCE();
            bool ok = true;
#pragma unroll
            for (int u = 0; u < 20; ++u) cv[u] = ld_sc(rsws, WS_MODD + (unsigned)(u * 64 + lane) * 16);
#pragma unroll
            for (int u = 0; u < 20; ++u) ok = ok && valid4(cv[u]);
            if (__all(ok) || spins > SPIN_G) break;
            __builtin_amdgcn_s_sleep(32);
          }
#pragma unroll
          for (int u = 0; u < 20; ++u) {
            const int pc = u * 64 + lane, j = pc / 160, r = pc - 160 * j;
            v4u o = cv[u];
            if (r >= 80) {
#pragma unroll
              for (int k = 0; k < 4; ++k) o[k] = pk2(1.f + blo(o[k]), 1.f + bhi(o[k]));
            }
            *(LAS v4u*)(L + L_CS + (r < 80 ? 10240 + (j * 320 + 4 * r) * 4 : (j * 320 + 4 * (r - 80)) * 4)) = o;
          }
        }
      }
    }
#pragma unroll
    for (int c = 0; c < 5; ++c) { const int ci = c * 64 + lane, row = ci / 10, ch = ci - 10 * row; wov[c] = (unsigned)(row * 1280 + b * 160 + ch * 16); asm volatile("" : "+v"(wov[c])); }
#pragma unroll
    for (int c = 0; c < 5; ++c) {
      const int ci = c * 64 + lane, row = ci / 40, pos = ci - 40 * row;
      w2v[c] = (unsigned)(row * 1280 + hf * 640 + (pos ^ (2 * (row & 3))) * 16); asm volatile("" : "+v"(w2v[c]));
    }
    w1v = (unsigned)((lane >> 2) * (FF * 2) + mm * 64 + (lane & 3) * 16); asm volatile("" : "+v"(w1v));
    if (is_mlp) {
      const rsrc_t rsw1t = mkrs(p.w1, DM * FF * 2);
      const rsrc_t rsw2t = mkrs(p.w2 + (size_t)mm * 32 * DM, 32 * DM * 2);
#pragma unroll
      for (int n = 0; n < 60; ++n) {
        if (n >= CPACE) VMWAIT(CPACE - 1);
        if (n < 40) dma16(rsw1t, lds0 + L_RT + n * 1024, w1v, (unsigned)n * 16u * (FF * 2));
        else dma16(rsw2t, lds0 + L_W2 + (n - 40) * 1024, w2v[(n - 40) % 5], (unsigned)((n - 40) / 5) * 8u * 1280u);
      }
    }
    {
      unsigned spins = 0;
      while (flags[3] < (unsigned)NSW) { __builtin_amdgcn_s_sleep(1); if (++spins > SPIN_L) break; }
      CFENCE();
#pragma unroll
      for (int n = 0; n < 20; ++n) dma16(rswo, lds0 + L_T1 + n * 1024, wov[n % 5], (unsigned)(n / 5) * 32u * 1280u);
    }
  }

  __builtin_amdgcn_s_setprio(0);
  bar();
  auto until = [&](unsigned t0, unsigned ticks) { while ((unsigned)__builtin_amdgcn_s_memrealtime() - t0 < ticks) __builtin_amdgcn_s_sleep(1); };
  auto lds_wait = [&](int k, unsigned v) { unsigned spins = 0; while (flags[k] < v) { __builtin_amdgcn_s_sleep(1); if (++spins > SPIN_L) break; } CFENCE(); };
  const int t1t = w == 0 ? 0 : w - 2;
  v4u af[4];
  unsigned o1;
  { const int cc = 4 * t1t + g; o1 = WS_T1P + (unsigned)((((i16 * 32 + 4 * b + cc / 5) * 32 + h) * 5 + cc % 5) * 16); asm volatile("" : "+v"(o1)); }
  if (w == 1 || w == 2) {
    const int t = tid - 64;
    __builtin_amdgcn_s_sleep(6);
    v4u v;
    for (unsigned spins = 0;; ++spins) {
      CFENCE();
      v = ld_sc(rsws, WS_OX + (unsigned)h * 2048 + t * 16);
      if (__all(valid4(v)) || spins > SPIN_G) break;
      __builtin_amdgcn_s_sleep(4);
    }
    *(LAS v4u*)(L + L_OS + (t >> 4) * 272 + (t & 15) * 16) = v;
  } else if (w == 7) {
    VMWAIT(0);
    if (lane == 0) flags[5] = 1u;
  } else {
    lds_wait(5, 1u);
    const LAS unsigned char* ap = L + L_T1 + (8 * g + tq) * 160 + (16 * t1t + 4 * tp) * 2;
#pragma unroll
    for (int s = 0; s < 4; ++s) {
      const v2u x0 = trread(ap + s * 32 * 160), x1 = trread(ap + s * 32 * 160 + 4 * 160);
      af[s] = v4u{x0.x, x0.y, x1.x, x1.y};
    }
  }
  bar();
  if (w == 0 || (w >= 3 && w < 7)) {
    const LAS unsigned char* bp = L + L_OS + (lane & 7) * 272 + g * 16;
    v4u bb[4];
#pragma unroll
    for (int s = 0; s < 4; ++s) bb[s] = *(const LAS v4u*)(bp + s * 64);
    SCHED();
    f4 y0 = mfma32(af[0], bb[0], f4{0, 0, 0, 0}), y1 = mfma32(af[1], bb[1], f4{0, 0, 0, 0});
    y0 = mfma32(af[2], bb[2], y0); y1 = mfma32(af[3], bb[3], y1);
    if (i16 < 8) st_sc(tg4(y0 + y1, t10), rsws, o1);
  }
  v4u a4[5], a5[4];
  unsigned o5 = WS_T5P + (unsigned)((mm * 8 + (lane & 7)) * 640 + 320 * hf + 64 * w + 16 * (i16 >> 3) + 4 * g) * 4;
  asm volatile("" : "+v"(o5));
  if (is_mlp) {
    const LAS unsigned char* ap = L + L_RT + (160 * (w >> 1) + 8 * g + tq) * 64 + (w & 1) * 32 + tp * 8;
#pragma unroll
    for (int s = 0; s < 5; ++s) {
      const v2u x0 = trread(ap + s * 2048), x1 = trread(ap + s * 2048 + 256);
      a4[s] = v4u{x0.x, x0.y, x1.x, x1.y};
    }
    const int r0 = 8 * g + tq;
    const LAS unsigned char* wp = L + L_W2 + r0 * 640 + (tp & 1) * 8;
    const int f = 2 * (r0 & 3);
#pragma unroll
    for (int tt = 0; tt < 4; ++tt) {
      const int ch = (2 * (4 * (w < 5 ? w : 0) + tt) + (tp >> 1)) ^ f;
      const v2u x0 = trread(wp + ch * 16), x1 = trread(wp + 4 * 640 + ch * 16);
      a5[tt] = v4u{x0.x, x0.y, x1.x, x1.y};
    }
  }
  if (w >= 4 && w < 7) {
    const unsigned tB2 = (unsigned)__builtin_amdgcn_s_memrealtime();
    const int c = 2 * (w - 4) + (lane >> 5), pr = lane & 31;
    const bool act = c < 5;
    const unsigned o = WS_T1P + (unsigned)(((b * 32 + h) * 32 + pr) * 5 + (act ? c : 0)) * 16;
    const LAS unsigned* vec = (const LAS unsigned*)(L + L_VEC);
    const int cv = act ? c : 0;
    const unsigned g0 = vec[2 * cv], g1 = vec[2 * cv + 1], x0 = vec[20 + 2 * cv], x1 = vec[20 + 2 * cv + 1];
    until(tB2, GB);
    v4u v;
    for (unsigned spins = 0;; ++spins) {
      CFENCE();
      v = ld_sc(rsws, o);
      if (__all(tagged4(v, t10)) || spins > SPIN_G) break;
      __builtin_amdgcn_s_sleep(4);
    }
    f4 sm;
#pragma unroll
    for (int k = 0; k < 4; ++k) sm[k] = x16_sum(row16_sum(untg(v[k])));
    const float r0 = rbf(blo(x0) + rbf(blo(g0) * rbf(sm[0]))), r1 = rbf(bhi(x0) + rbf(bhi(g0) * rbf(sm[1])));
    const float r2 = rbf(blo(x1) + rbf(blo(g1) * rbf(sm[2]))), r3 = rbf(bhi(x1) + rbf(bhi(g1) * rbf(sm[3])));
    if (pr == 0 && act) {
      st_sc2(v2u{canonu(pk2(r0, r1)), canonu(pk2(r2, r3))}, rsws, WS_X1B + (unsigned)(b * 320 + 10 * h + 2 * c) * 4);
      *(LAS f4*)(L + L_X1O + c * 16) = f4{r0, r1, r2, r3};
    }
    asm volatile("s_waitcnt lgkmcnt(0)" ::: "memory");
    if (lane == 0) __hip_atomic_fetch_add((LAS unsigned*)(L + L_FLAG) + 6, 1u, __ATOMIC_RELAXED, __HIP_MEMORY_SCOPE_WORKGROUP);
  }
  lds_wait(6, 3u);
  const unsigned tB = (unsigned)__builtin_amdgcn_s_memrealtime();
  if (is_mlp) {
    {
      const unsigned o = WS_X1B + (unsigned)w * 1280 + lane * 4;
      float cs[10], sh[10];
#pragma unroll
      for (int u = 0; u < 5; ++u) {
        const unsigned c = *(const LAS unsigned*)(L + L_CS + (w * 320 + u * 64 + lane) * 4);
        const unsigned d = *(const LAS unsigned*)(L + L_CS + 10240 + (w * 320 + u * 64 + lane) * 4);
        cs[2 * u] = blo(c); cs[2 * u + 1] = bhi(c); sh[2 * u] = blo(d); sh[2 * u + 1] = bhi(d);
        asm volatile("" : "+v"(cs[2 * u]), "+v"(cs[2 * u + 1]), "+v"(sh[2 * u]), "+v"(sh[2 * u + 1]));
      }
      unsigned oh2 = (unsigned)(w * 1296 + lane * 4);
      asm volatile("" : "+v"(oh2));
      until(tB, GC);
      unsigned xv[5];
      for (unsigned spins = 0;; ++spins) {
        CFENCE();
        bool ok = true;
#pragma unroll
        for (int u = 0; u < 5; ++u) xv[u] = __builtin_amdgcn_raw_buffer_load_b32(rsws, o + u * 256, 0, 16);
#pragma unroll
        for (int u = 0; u < 5; ++u) ok = ok && xv[u] != SENT;
        if (__all(ok) || spins > SPIN_G) break;
        __builtin_amdgcn_s_sleep(4);
      }
      const float x00 = blo(rfl(xv[0]));
      float s1 = 0.f, s2 = 0.f;
#pragma unroll
      for (int u = 0; u < 5; ++u) {
        const float d0 = blo(xv[u]) - x00, d1 = bhi(xv[u]) - x00;
        s1 += d0 + d1; s2 = fmaf(d0, d0, fmaf(d1, d1, s2));
      }
      s1 = row16_sum(s1); s2 = row16_sum(s2);
      s1 = xrow_sum(s1) * (1.f / DM); s2 = xrow_sum(s2) * (1.f / DM);
      const float rs = __builtin_amdgcn_rsqf(fmaxf(s2 - s1 * s1, 0.f) + 1e-5f), mr = -(x00 + s1) * rs;
#pragma unroll
      for (int u = 0; u < 5; ++u) {
        const float n0 = fmaf(blo(xv[u]), rs, mr), n1 = fmaf(bhi(xv[u]), rs, mr);
        *(LAS unsigned*)(L + L_H2 + oh2 + u * 256) = pk2(fmaf(n0, cs[2 * u], sh[2 * u]), fmaf(n1, cs[2 * u + 1], sh[2 * u + 1]));
      }
    }
    bar();
    {
      const LAS unsigned char* bp = L + L_H2 + (lane & 7) * 1296 + (160 * (w >> 1) + 8 * g) * 2;
      v4u bb[5];
#pragma unroll
      for (int s = 0; s < 5; ++s) bb[s] = *(const LAS v4u*)(bp + s * 64);
      SCHED();
      f4 a = mfma32(a4[0], bb[0], f4{0, 0, 0, 0}), a2 = mfma32(a4[1], bb[1], f4{0, 0, 0, 0});
      a = mfma32(a4[2], bb[2], a); a2 = mfma32(a4[3], bb[3], a2); a = mfma32(a4[4], bb[4], a);
      *(LAS f4*)(L + L_T4P + ((w * 4 + g) * 8 + (lane & 7)) * 16) = a + a2;
    }
    bar();
    if (tid < 128) {
      const int j = tid >> 4, c = 2 * (tid & 15), t = c >> 4, gg = (c & 15) >> 2, r = c & 3;
      const LAS float* z = (const LAS float*)(L + L_T4P + ((t * 4 + gg) * 8 + j) * 16) + r;
      const float z0 = (z[0] + z[256]) + (z[512] + z[768]), z1 = (z[1] + z[257]) + (z[513] + z[769]);
      *(LAS unsigned*)(L + L_H1 + j * 80 + c * 2) = pk2(silu(rbf(z0)), silu(rbf(z1)));
    }
    bar();
    if (w < 5) {
      const v4u bb = *(const LAS v4u*)(L + L_H1 + (lane & 7) * 80 + g * 16);
      f4 y[4];
#pragma unroll
      for (int tt = 0; tt < 4; ++tt) y[tt] = mfma32(a5[tt], bb, f4{0, 0, 0, 0});
      const bool up = i16 >= 8;
      st_sc(tg4(up ? y[1] : y[0], t10), rsws, o5);
      st_sc(tg4(up ? y[3] : y[2], t10), rsws, o5 + 128);
    }
  }
  const unsigned tD = (unsigned)__builtin_amdgcn_s_memrealtime();
  if (w < 5) {
    const bool act = lane < NMLP;
    const unsigned o = WS_T5P + (unsigned)(((act ? lane : 0) * 8 + b) * 640 + 20 * h + 4 * w) * 4;
    const LAS unsigned* vec = (const LAS unsigned*)(L + L_VEC);
    const unsigned g0 = vec[10 + 2 * w], g1 = vec[10 + 2 * w + 1];
    const f4 x1 = *(const LAS f4*)(L + L_X1O + w * 16);
    if (is_mlp) until(tD, GD1); else until(tB, GD2);
    v4u v;
    for (unsigned spins = 0;; ++spins) {
      CFENCE();
      v = ld_sc(rsws, o);
      if (__all(tagged4(v, t10)) || spins > SPIN_G) break;
      __builtin_amdgcn_s_sleep(4);
    }
    f4 sm;
#pragma unroll
    for (int k = 0; k < 4; ++k) sm[k] = wave_sum(act ? untg(v[k]) : 0.f);
    if (lane == 0) {
      const float r0 = x1[0] + rbf(blo(g0) * rbf(sm[0])), r1 = x1[1] + rbf(bhi(g0) * rbf(sm[1]));
      const float r2 = x1[2] + rbf(blo(g1) * rbf(sm[2])), r3 = x1[3] + rbf(bhi(g1) * rbf(sm[3]));
      *(GAS v2u*)(p.out + b * DM + 20 * h + 4 * w) = v2u{pk2(r0, r1), pk2(r2, r3)};
    }
  }
  if (id == 0 && tid == 448) {
    const unsigned E = __builtin_amdgcn_raw_buffer_load_b32(rsws, WS_EPOCH, 0, 16);
    __builtin_amdgcn_raw_buffer_store_b32(E + 1u, rsws, WS_EPOCH, 0, 16);
  }
}

}

extern "C" void kernel_launch(void* const* d_in, const int* in_sizes, int n_in, void* d_out, int out_size, void* d_ws,
                              size_t ws_size, hipStream_t stream) {
  Params p;
  p.x = (const u16*)d_in[0];
  p.kc = (const u16*)d_in[1];
  p.vc = (const u16*)d_in[2];
  p.wq = (const u16*)d_in[3];
  p.wo = (const u16*)d_in[4];
  p.w1 = (const u16*)d_in[6];
  p.w2 = (const u16*)d_in[7];
  p.c = (const u16*)d_in[8];
  p.wmod = (const u16*)d_in[9];
  p.out = (u16*)d_out;
  p.ws = (unsigned char*)d_ws;
  hipLaunchKernelGGL(fused_block, dim3(256), dim3(512), 0, stream, p);
}
```
